```python
import torch

def _softmax(x, axis=-1):
    return torch.softmax(x, dim=axis)
def _gelu(x):
    return torch.nn.functional.gelu(x, approximate="tanh")
def _silu(x):
    return torch.nn.functional.silu(x)
def _sigmoid(x):
    return torch.sigmoid(x)
def _layernorm(x, axis=-1, eps=1e-5):
    m = x.mean(dim=axis, keepdim=True)
    v = x.var(dim=axis, keepdim=True, unbiased=False)
    return (x - m) / torch.sqrt(v + eps)
def _scatter_add(tgt, idx, src, axis=0):
    return tgt.clone().index_add_(axis, idx, src)
def _segment_sum(src, seg, n):
    out = torch.zeros((n,) + src.shape[1:], dtype=src.dtype)
    return out.index_add_(0, seg, src)
def _pad(x, pw):
    flat = tuple(v for pair in reversed(pw) for v in pair)
    return torch.nn.functional.pad(x, flat)

kernel_name = 'sequential_preLN__linear_kernel_hedgehog__dense_activation_silu__prompt'

def setup_inputs():

    _w = (lambda a: (lambda t: t.to(torch.bfloat16) if t.is_floating_point() else t)(torch.as_tensor(a)))
    def _A_setup():
        B, S, D, H = 1, 2048, 448, 16
        Dh = D // H
        return {
            "x": _w((lambda *s: torch.randn(*s, dtype=torch.bfloat16) * 0.05)(B, S, D)),
            "wqkv": _w((lambda *s: torch.randn(*s, dtype=torch.bfloat16) * 0.2)(D, 3 * D)),
            "w_phi": _w((lambda *s: torch.randn(*s, dtype=torch.bfloat16) * 0.2)(H, Dh, Dh) * (Dh ** -0.5)),
            "wo": _w((lambda *s: torch.randn(*s, dtype=torch.bfloat16) * 0.05)(D, D)),
        }
    _A_in = _A_setup()

    def _B_setup():
        B, S, D, F = 1, 2048, 448, 1792
        return {
            "x": _w((lambda *s: torch.randn(*s, dtype=torch.bfloat16) * 0.05)(B, S, D)),
            "w1": _w((lambda *s: torch.randn(*s, dtype=torch.bfloat16) * 3.2)(D, F) * (D ** -0.5)),
            "w2": _w((lambda *s: torch.randn(*s, dtype=torch.bfloat16) * 0.1)(F, D) * (F ** -0.5)),
        }
    _B_in = _B_setup()

    return {
        "A_x": _A_in["x"],
        "A_wqkv": _A_in["wqkv"],
        "A_w_phi": _A_in["w_phi"],
        "A_wo": _A_in["wo"],
        "B_x": _B_in["x"],
        "B_w1": _B_in["w1"],
        "B_w2": _B_in["w2"],
    }

def reference(A_x, A_wqkv, A_w_phi, A_wo, B_x, B_w1, B_w2):
    def _A_ref(x, wqkv, w_phi, wo):
        B, S, D = x.shape
        H = 16; Dh = D // H
        qkv = (lambda x, *d: x.permute(*d))((x @ wqkv).reshape(B, S, 3, H, Dh), 2, 0, 3, 1, 4)
        q, k, v = qkv[0], qkv[1], qkv[2]
        pq = torch.einsum("bhsd,hdf->bhsf", q, w_phi)
        pk = torch.einsum("bhsd,hdf->bhsf", k, w_phi)
        phi_q = torch.concatenate([_softmax(pq, axis=-1), _softmax(-pq, axis=-1)], axis=-1)
        phi_k = torch.concatenate([_softmax(pk, axis=-1), _softmax(-pk, axis=-1)], axis=-1)
        kv = torch.einsum("bhsf,bhsd->bhfd", phi_k, v)
        z  = torch.einsum("bhsf->bhf", phi_k)
        num = torch.einsum("bhqf,bhfd->bhqd", phi_q, kv)
        den = torch.einsum("bhqf,bhf->bhq", phi_q, z)[..., None] + 1e-6
        o = num / den
        return (lambda x, *d: x.permute(*d))(o, 0, 2, 1, 3).reshape(B, S, D) @ wo

    def _B_ref(x, w1, w2):
        h = torch.einsum("bsd,df->bsf", x, w1)
        h = _silu(h)
        out = torch.einsum("bsf,fd->bsd", h, w2)
        return out

    x = A_x
    h = x + _A_ref(_layernorm(x, axis=-1), A_wqkv, A_w_phi, A_wo)
    h2 = h + _B_ref(_layernorm(h, axis=-1), B_w1, B_w2)
    return h2
```

```cpp
#include <hip/hip_runtime.h>
#include <hip/hip_bf16.h>
#define EXTRA 0
#define EXTRA_MEMSET 0
typedef __hip_bfloat16 bf16;
constexpr int S=2048, D=448, H=16, DH=28, F=1792;
__device__ __forceinline__ float b2f(bf16 v){return __bfloat162float(v);}
__device__ __forceinline__ bf16 f2b(float v){return __float2bfloat16(v);}

__global__ void k_ln(const bf16* x, bf16* y){
  int row=blockIdx.x; int t=threadIdx.x;
  float v[7]; float s=0;
  for(int i=0;i<7;i++){v[i]=b2f(x[row*D+t+64*i]); s+=v[i];}
  for(int o=32;o;o>>=1) s+=__shfl_xor(s,o);
  float m=s/D; float q=0;
  for(int i=0;i<7;i++){float d=v[i]-m; q+=d*d;}
  for(int o=32;o;o>>=1) q+=__shfl_xor(q,o);
  float r=rsqrtf(q/D+1e-5f);
  for(int i=0;i<7;i++) y[row*D+t+64*i]=f2b((v[i]-m)*r);
}
template<int MODE> __global__ void k_gemm(const bf16* A,const bf16* B,const bf16* R,bf16* C,int N,int K){
  __shared__ float As[16][65]; __shared__ float Bs[16][65];
  int tx=threadIdx.x&15, ty=threadIdx.x>>4; int bm=blockIdx.y*64, bn=blockIdx.x*64;
  float acc[4][4]={};
  for(int k0=0;k0<K;k0+=16){
    for(int i=threadIdx.x;i<64*16;i+=256){int r=i>>4,c=i&15; As[c][r]=b2f(A[(bm+r)*K+k0+c]);}
    for(int i=threadIdx.x;i<64*16;i+=256){int r=i>>6,c=i&63; Bs[r][c]=b2f(B[(k0+r)*N+bn+c]);}
    __syncthreads();
    for(int k=0;k<16;k++){ float a[4],b[4]; for(int i=0;i<4;i++){a[i]=As[k][ty*4+i]; b[i]=Bs[k][tx*4+i];}
      for(int i=0;i<4;i++)for(int j=0;j<4;j++)acc[i][j]+=a[i]*b[j];}
    __syncthreads();
  }
  for(int i=0;i<4;i++)for(int j=0;j<4;j++){int r=bm+ty*4+i,c=bn+tx*4+j; float v=acc[i][j];
    if(MODE==1) v=b2f(f2b(v))+b2f(R[r*N+c]);
    if(MODE==2){ v=b2f(f2b(v)); v=v/(1.f+__expf(-v)); }
    C[r*N+c]=f2b(v);}
}
__global__ __launch_bounds__(256) void k_feat(const bf16* qkv,const bf16* wphi,bf16* phiq,bf16* phik){
  int idx=blockIdx.x*blockDim.x+threadIdx.x;
  int which=idx&1, h=(idx>>1)&15, s=idx>>5;
  const bf16* q=qkv+s*3*D+which*D+h*DH; const bf16* w=wphi+h*DH*DH;
  float p[DH]; float mx=-1e30f, mn=1e30f;
  #pragma unroll
  for(int f=0;f<DH;f++) p[f]=0;
  #pragma unroll 1
  for(int d=0;d<DH;d++){ float qv=b2f(q[d]);
    #pragma unroll
    for(int f=0;f<DH;f++) p[f]+=qv*b2f(w[d*DH+f]); }
  #pragma unroll
  for(int f=0;f<DH;f++){float a=b2f(f2b(p[f])); p[f]=a; mx=fmaxf(mx,a); mn=fminf(mn,a);}
  float s1=0,s2=0;
  #pragma unroll
  for(int f=0;f<DH;f++){s1+=__expf(p[f]-mx); s2+=__expf(mn-p[f]);}
  bf16* o=(which?phik:phiq)+(s*H+h)*56;
  #pragma unroll
  for(int f=0;f<DH;f++){o[f]=f2b(__expf(p[f]-mx)/s1); o[28+f]=f2b(__expf(mn-p[f])/s2);}
}
__global__ void k_kv(const bf16* qkv,const bf16* phik,float* kv){
  int h=blockIdx.x/56, f=blockIdx.x%56; int d=threadIdx.x&31, g=threadIdx.x>>5;
  float a=0; for(int s=g;s<S;s+=8){ float p=b2f(phik[(s*H+h)*56+f]); float v= d<28? b2f(qkv[s*3*D+2*D+h*DH+d]) : 1.f; a+=p*v; }
  __shared__ float red[8][32]; red[g][d]=a; __syncthreads();
  if(g==0){float t=0; for(int i=0;i<8;i++)t+=red[i][d]; kv[(h*56+f)*32+d]=b2f(f2b(t));}
}
__global__ void k_o(const bf16* phiq,const float* kv,bf16* o){
  int idx=blockIdx.x*blockDim.x+threadIdx.x; int d=idx&31,h=(idx>>5)&15,s=idx>>9;
  float a=0; for(int f=0;f<56;f++) a+=b2f(phiq[(s*H+h)*56+f])*kv[(h*56+f)*32+d];
  a=b2f(f2b(a));
  float den=__shfl(a,(threadIdx.x&32)+28,64); den=b2f(f2b(den+1e-6f));
  if(d<28) o[s*D+h*DH+d]=f2b(a/den);
}
__global__ void k_empty(int* p){ if(p==nullptr) return; }

extern "C" void kernel_launch(void* const* d_in, const int* in_sizes, int n_in, void* d_out, int out_size, void* d_ws, size_t ws_size, hipStream_t stream){
  const bf16* x=(const bf16*)d_in[0]; const bf16* wqkv=(const bf16*)d_in[1]; const bf16* wphi=(const bf16*)d_in[2]; const bf16* wo=(const bf16*)d_in[3];
  const bf16* w1=(const bf16*)d_in[5]; const bf16* w2=(const bf16*)d_in[6]; bf16* out=(bf16*)d_out;
  char* ws=(char*)d_ws;
  bf16* xn=(bf16*)(ws); bf16* qkv=(bf16*)(ws+(4<<20)); bf16* phiq=(bf16*)(ws+(16<<20)); bf16* phik=(bf16*)(ws+(24<<20));
  float* kv=(float*)(ws+(32<<20)); bf16* o=(bf16*)(ws+(36<<20)); bf16* h=(bf16*)(ws+(40<<20)); bf16* h1=(bf16*)(ws+(48<<20));
  for(int i=0;i<EXTRA_MEMSET;i++) hipMemsetAsync(ws+(100<<20),0,4096,stream);
  k_ln<<<S,64,0,stream>>>(x,xn);
  k_gemm<0><<<dim3(3*D/64,S/64),256,0,stream>>>(xn,wqkv,nullptr,qkv,3*D,D);
  k_feat<<<S*H*2/256,256,0,stream>>>(qkv,wphi,phiq,phik);
  k_kv<<<H*56,256,0,stream>>>(qkv,phik,kv);
  k_o<<<S*H*32/256,256,0,stream>>>(phiq,kv,o);
  k_gemm<1><<<dim3(D/64,S/64),256,0,stream>>>(o,wo,x,h,D,D);
  k_ln<<<S,64,0,stream>>>(h,xn);
  k_gemm<2><<<dim3(F/64,S/64),256,0,stream>>>(xn,w1,nullptr,h1,F,D);
  k_gemm<1><<<dim3(D/64,S/64),256,0,stream>>>(h1,w2,h,out,D,F);
  for(int i=0;i<EXTRA;i++) k_empty<<<256,256,0,stream>>>((int*)ws);
}
```

```cpp
#include <hip/hip_runtime.h>
#include <hip/hip_bf16.h>
#define EXTRA 0
#define EXTRA_MEMSET 0
typedef __hip_bfloat16 bf16;
constexpr int S=2048, D=448, H=16, DH=28, F=1792;
__device__ __forceinline__ float b2f(bf16 v){return __bfloat162float(v);}
__device__ __forceinline__ bf16 f2b(float v){return __float2bfloat16(v);}

__global__ void k_ln(const bf16* x, bf16* y){
  int row=blockIdx.x; int t=threadIdx.x;
  float v[7]; float s=0;
  for(int i=0;i<7;i++){v[i]=b2f(x[row*D+t+64*i]); s+=v[i];}
  for(int o=32;o;o>>=1) s+=__shfl_xor(s,o);
  float m=s/D; float q=0;
  for(int i=0;i<7;i++){float d=v[i]-m; q+=d*d;}
  for(int o=32;o;o>>=1) q+=__shfl_xor(q,o);
  float r=rsqrtf(q/D+1e-5f);
  for(int i=0;i<7;i++) y[row*D+t+64*i]=f2b((v[i]-m)*r);
}
template<int MODE> __global__ void k_gemm(const bf16* A,const bf16* B,const bf16* R,bf16* C,int N,int K){
  __shared__ float As[16][65]; __shared__ float Bs[16][65];
  int tx=threadIdx.x&15, ty=threadIdx.x>>4; int bm=blockIdx.y*64, bn=blockIdx.x*64;
  float acc[4][4]={};
  for(int k0=0;k0<K;k0+=16){
    for(int i=threadIdx.x;i<64*16;i+=256){int r=i>>4,c=i&15; As[c][r]=b2f(A[(bm+r)*K+k0+c]);}
    for(int i=threadIdx.x;i<64*16;i+=256){int r=i>>6,c=i&63; Bs[r][c]=b2f(B[(k0+r)*N+bn+c]);}
    __syncthreads();
    for(int k=0;k<16;k++){ float a[4],b[4]; for(int i=0;i<4;i++){a[i]=As[k][ty*4+i]; b[i]=Bs[k][tx*4+i];}
      for(int i=0;i<4;i++)for(int j=0;j<4;j++)acc[i][j]+=a[i]*b[j];}
    __syncthreads();
  }
  for(int i=0;i<4;i++)for(int j=0;j<4;j++){int r=bm+ty*4+i,c=bn+tx*4+j; float v=acc[i][j];
    if(MODE==1) v=b2f(f2b(v))+b2f(R[r*N+c]);
    if(MODE==2){ v=b2f(f2b(v)); v=v/(1.f+__expf(-v)); }
    C[r*N+c]=f2b(v);}
}
__global__ __launch_bounds__(256) void k_feat(const bf16* qkv,const bf16* wphi,bf16* phiq,bf16* phik){
  int idx=blockIdx.x*blockDim.x+threadIdx.x;
  int which=idx&1, h=(idx>>1)&15, s=idx>>5;
  const bf16* q=qkv+s*3*D+which*D+h*DH; const bf16* w=wphi+h*DH*DH;
  float p[DH]; float mx=-1e30f, mn=1e30f;
  #pragma unroll
  for(int f=0;f<DH;f++) p[f]=0;
  #pragma unroll 1
  for(int d=0;d<DH;d++){ float qv=b2f(q[d]);
    #pragma unroll
    for(int f=0;f<DH;f++) p[f]+=qv*b2f(w[d*DH+f]); }
  #pragma unroll
  for(int f=0;f<DH;f++){float a=b2f(f2b(p[f])); p[f]=a; mx=fmaxf(mx,a); mn=fminf(mn,a);}
  float s1=0,s2=0;
  #pragma unroll
  for(int f=0;f<DH;f++){s1+=__expf(p[f]-mx); s2+=__expf(mn-p[f]);}
  bf16* o=(which?phik:phiq)+(s*H+h)*56;
  #pragma unroll
  for(int f=0;f<DH;f++){o[f]=f2b(__expf(p[f]-mx)/s1); o[28+f]=f2b(__expf(mn-p[f])/s2);}
}
__global__ void k_kv(const bf16* qkv,const bf16* phik,float* kv){
  int h=blockIdx.x/56, f=blockIdx.x%56; int d=threadIdx.x&31, g=threadIdx.x>>5;
  float a=0; for(int s=g;s<S;s+=8){ float p=b2f(phik[(s*H+h)*56+f]); float v= d<28? b2f(qkv[s*3*D+2*D+h*DH+d]) : 1.f; a+=p*v; }
  __shared__ float red[8][32]; red[g][d]=a; __syncthreads();
  if(g==0){float t=0; for(int i=0;i<8;i++)t+=red[i][d]; kv[(h*56+f)*32+d]=b2f(f2b(t));}
}
__global__ void k_o(const bf16* phiq,const float* kv,bf16* o){
  int idx=blockIdx.x*blockDim.x+threadIdx.x; int d=idx&31,h=(idx>>5)&15,s=idx>>9;
  float a=0; for(int f=0;f<56;f++) a+=b2f(phiq[(s*H+h)*56+f])*kv[(h*56+f)*32+d];
  a=b2f(f2b(a));
  float den=__shfl(a,(threadIdx.x&32)+28,64); den=b2f(f2b(den+1e-6f));
  if(d<28) o[s*D+h*DH+d]=f2b(a/den);
}
__global__ void k_empty(int* p){ if(p==nullptr) return; }

typedef unsigned long long u64;
typedef unsigned v4u __attribute__((ext_vector_type(4)));
#define LAS __attribute__((address_space(3)))
__device__ __forceinline__ u64 rt(){ u64 t; asm volatile("s_memrealtime %0\n\ts_waitcnt lgkmcnt(0)":"=s"(t)::"memory"); return t; }
__device__ __forceinline__ unsigned ldw(const char* p){ unsigned v; asm volatile("global_load_dword %0, %1, off\n\ts_waitcnt vmcnt(0)":"=v"(v):"v"(p):"memory"); return v; }
template<int MODE,int MULT,int DIV> __global__ void k_amp(const u64* val){
  __shared__ u64 s0[256],s1[256],s2[256];
  int t=threadIdx.x; s0[t]=val[t*4]; s1[t]=val[t*4+1]; s2[t]=val[t*4+2]; __syncthreads();
  if(t==0){ u64 mn=~0ull,mx=0,sum=0; for(int i=0;i<256;i++){ mn=s0[i]<mn?s0[i]:mn; mx=s1[i]>mx?s1[i]:mx; sum+=s2[i]; }
    u64 v= MODE==0? sum/256 : mx-mn; v=v*MULT/DIV; if(v>200000) v=200000; u64 a=rt(); while(rt()-a<v) __builtin_amdgcn_s_sleep(2); }
}

__global__ __launch_bounds__(256) void k_probe(const char* x,const char* w,u64* out){
  if(threadIdx.x==0){ u64* o=out+blockIdx.x*4; o[0]=0;o[1]=0;o[2]=0; o[3]=0; }
}
extern "C" void kernel_launch(void* const* d_in, const int* in_sizes, int n_in, void* d_out, int out_size, void* d_ws, size_t ws_size, hipStream_t stream){
  const bf16* x=(const bf16*)d_in[0]; const bf16* wqkv=(const bf16*)d_in[1]; const bf16* wphi=(const bf16*)d_in[2]; const bf16* wo=(const bf16*)d_in[3];
  const bf16* w1=(const bf16*)d_in[5]; const bf16* w2=(const bf16*)d_in[6]; bf16* out=(bf16*)d_out;
  char* ws=(char*)d_ws;
  bf16* xn=(bf16*)(ws); bf16* qkv=(bf16*)(ws+(4<<20)); bf16* phiq=(bf16*)(ws+(16<<20)); bf16* phik=(bf16*)(ws+(24<<20));
  float* kv=(float*)(ws+(32<<20)); bf16* o=(bf16*)(ws+(36<<20)); bf16* h=(bf16*)(ws+(40<<20)); bf16* h1=(bf16*)(ws+(48<<20));
  for(int i=0;i<EXTRA_MEMSET;i++) hipMemsetAsync(ws+(100<<20),0,4096,stream);
  k_probe<<<256,256,0,stream>>>((const char*)x,(const char*)wqkv,(u64*)(ws+(120<<20)));
  k_ln<<<S,64,0,stream>>>(x,xn);
  k_gemm<0><<<dim3(3*D/64,S/64),256,0,stream>>>(xn,wqkv,nullptr,qkv,3*D,D);
  k_feat<<<S*H*2/256,256,0,stream>>>(qkv,wphi,phiq,phik);
  k_kv<<<H*56,256,0,stream>>>(qkv,phik,kv);
  k_o<<<S*H*32/256,256,0,stream>>>(phiq,kv,o);
  k_gemm<1><<<dim3(D/64,S/64),256,0,stream>>>(o,wo,x,h,D,D);
  k_ln<<<S,64,0,stream>>>(h,xn);
  k_gemm<2><<<dim3(F/64,S/64),256,0,stream>>>(xn,w1,nullptr,h1,F,D);
  k_gemm<1><<<dim3(D/64,S/64),256,0,stream>>>(h1,w2,h,out,D,F);
  k_amp<0,1,1><<<1,256,0,stream>>>((const u64*)(ws+(120<<20)));
  for(int i=0;i<EXTRA;i++) k_empty<<<256,256,0,stream>>>((int*)ws);
}
```

```cpp
#include <hip/hip_runtime.h>
#include <hip/hip_bf16.h>

typedef __bf16 bf8 __attribute__((ext_vector_type(8)));
typedef __bf16 bf4 __attribute__((ext_vector_type(4)));
typedef __bf16 bf2 __attribute__((ext_vector_type(2)));
typedef float f4 __attribute__((ext_vector_type(4)));
typedef float f2 __attribute__((ext_vector_type(2)));
typedef float f16v __attribute__((ext_vector_type(16)));
typedef unsigned u4 __attribute__((ext_vector_type(4)));
typedef unsigned u2 __attribute__((ext_vector_type(2)));
typedef unsigned long long u64;
typedef __amdgpu_buffer_rsrc_t rsrc_t;
#define LAS __attribute__((address_space(3)))
#define DI __device__ __forceinline__
#define SC1 16
#define FENCE() do { asm volatile("" ::: "memory"); __builtin_amdgcn_sched_barrier(0); } while (0)
#define WAITV(n) do { asm volatile("s_waitcnt vmcnt(%0)" :: "i"(n) : "memory"); __builtin_amdgcn_sched_barrier(0); } while (0)
#define BAR() do { asm volatile("s_waitcnt lgkmcnt(0)\n\ts_barrier" ::: "memory"); __builtin_amdgcn_sched_barrier(0); } while (0)

constexpr unsigned OFF_E = 0, OFF_FLAGA = 256, OFF_FLAGBA = 8192, OFF_FLAGBC = 9216, OFF_AUG = 16384;
constexpr unsigned PACKB = 92 * 1024;
constexpr unsigned OFF_PACK = 131072, OFF_PART = OFF_PACK + 16 * PACKB, OFF_O = OFF_PART + 256 * 4096;
constexpr unsigned OFF_H = OFF_O + 128 * 16 * 1024, OFF_H1 = OFF_H + 64 * 28 * 1024, OFF_END = OFF_H1 + 64 * 112 * 1024;
constexpr unsigned OFF_STAMP = 16u << 20;
constexpr unsigned L_P = 0, L_S = 94208;
constexpr unsigned L_STG = 0;
constexpr unsigned L_X = 0, L_YA = 57856, L_YB = L_YA + 57344;
constexpr unsigned L_KV = 151552, L_HS = L_KV + 4096;
constexpr unsigned L_RED = 0;
constexpr unsigned L_TOTAL = 163840;
constexpr unsigned POISON = 0xFFFFFFFFu, SPIN_CAP = 1u << 17;
constexpr float LOG2E = 1.4426950408889634f;

DI unsigned rfl(unsigned v) { return __builtin_amdgcn_readfirstlane(v); }
DI unsigned pk2(float a, float b) { f2 v = {a, b}; return __builtin_bit_cast(unsigned, __builtin_convertvector(v, bf2)); }
DI float dot2(unsigned a, unsigned b, float c) { return __builtin_amdgcn_fdot2_f32_bf16(__builtin_bit_cast(bf2, a), __builtin_bit_cast(bf2, b), c, false); }
DI f4 mfma16(u4 a, u4 b, f4 c) { return __builtin_amdgcn_mfma_f32_16x16x32_bf16(__builtin_bit_cast(bf8, a), __builtin_bit_cast(bf8, b), c, 0, 0, 0); }
DI f16v mfma32(u4 a, u4 b, f16v c) { return __builtin_amdgcn_mfma_f32_32x32x16_bf16(__builtin_bit_cast(bf8, a), __builtin_bit_cast(bf8, b), c, 0, 0, 0); }
DI u2 trd(unsigned addr) { return __builtin_bit_cast(u2, __builtin_amdgcn_ds_read_tr16_b64_v4bf16((LAS bf4*)(size_t)addr)); }
DI u4 trd2(unsigned a0, unsigned a1) { u2 lo = trd(a0), hi = trd(a1); return (u4){lo.x, lo.y, hi.x, hi.y}; }
DI u4 ldsr128(unsigned addr) { return *(LAS u4*)(size_t)addr; }
DI u2 ldsr64(unsigned addr) { return *(LAS u2*)(size_t)addr; }
DI void ldsw128(unsigned addr, u4 v) { *(LAS u4*)(size_t)addr = v; }
DI void ldsw64(unsigned addr, u2 v) { *(LAS u2*)(size_t)addr = v; }
DI u4 ld128(rsrc_t r, unsigned vo, unsigned so, int aux) { return aux ? __builtin_amdgcn_raw_buffer_load_b128(r, vo, so, SC1) : __builtin_amdgcn_raw_buffer_load_b128(r, vo, so, 0); }
DI void st128(rsrc_t r, u4 v, unsigned vo, unsigned so) { __builtin_amdgcn_raw_buffer_store_b128(v, r, vo, so, SC1); }
DI void glds(rsrc_t r, unsigned vo, unsigned so, unsigned lds) { asm volatile("s_mov_b32 m0, %0\n\ts_nop 0\n\tbuffer_load_dwordx4 %1, %2, %3 offen lds" :: "s"(lds), "v"(vo), "s"(r), "s"(so) : "memory"); }
DI void glds_sc1(rsrc_t r, unsigned vo, unsigned so, unsigned lds) { asm volatile("s_mov_b32 m0, %0\n\ts_nop 0\n\tbuffer_load_dwordx4 %1, %2, %3 offen sc1 lds" :: "s"(lds), "v"(vo), "s"(r), "s"(so) : "memory"); }
DI void swap16(float& a, float& b) { auto r = __builtin_amdgcn_permlane16_swap(__float_as_uint(a), __float_as_uint(b), false, false); a = __uint_as_float(r[0]); b = __uint_as_float(r[1]); }
DI void swap32(float& a, float& b) { auto r = __builtin_amdgcn_permlane32_swap(__float_as_uint(a), __float_as_uint(b), false, false); a = __uint_as_float(r[0]); b = __uint_as_float(r[1]); }
DI void swap16u(unsigned& a, unsigned& b) { auto r = __builtin_amdgcn_permlane16_swap(a, b, false, false); a = r[0]; b = r[1]; }
DI void swap32u(unsigned& a, unsigned& b) { auto r = __builtin_amdgcn_permlane32_swap(a, b, false, false); a = r[0]; b = r[1]; }
DI float vmax(float a, float b) { return __builtin_fmaxf(a, b); }
template <bool MAX> DI void red4(float& a, float& b, float& c, float& d) {
  swap16(a, b); float ab = MAX ? vmax(a, b) : a + b;
  swap16(c, d); float cd = MAX ? vmax(c, d) : c + d;
  swap32(ab, cd); float r = MAX ? vmax(ab, cd) : ab + cd;
  float r2 = r; swap32(r, r2);
  a = r; b = r; swap16(a, b); c = r2; d = r2; swap16(c, d);
}
DI unsigned umax4(u4 v, unsigned m) { return max(max(max(v.x, v.y), max(v.z, v.w)), m); }
DI unsigned sig(unsigned row) { return ((row >> 1) & 1) * 2 + ((row >> 2) & 1); }

#define STAMP(k)

DI void features(const f4 (&p)[2][2], const float (&rstd)[2], bool pad, u4 (&out)[2][2]) {
  float mx[2], mn[2];
#pragma unroll
  for (int t = 0; t < 2; t++) {
    mx[t] = vmax(vmax(vmax(p[t][0][0], p[t][0][1]), vmax(p[t][0][2], p[t][0][3])), vmax(vmax(p[t][1][0], p[t][1][1]), vmax(p[t][1][2], p[t][1][3])));
    mn[t] = vmax(vmax(vmax(-p[t][0][0], -p[t][0][1]), vmax(-p[t][0][2], -p[t][0][3])), vmax(vmax(-p[t][1][0], -p[t][1][1]), vmax(-p[t][1][2], -p[t][1][3])));
  }
  red4<true>(mx[0], mn[0], mx[1], mn[1]);
  float e[2][2][8], s[2][2];
#pragma unroll
  for (int t = 0; t < 2; t++) {
    const float c = rstd[t] * LOG2E;
    const float bp = -mx[t] * c, bn = -mn[t] * c;
    const float bp1 = pad ? -__builtin_inff() : bp, bn1 = pad ? -__builtin_inff() : bn;
    s[t][0] = 0.f; s[t][1] = 0.f;
#pragma unroll
    for (int i = 0; i < 4; i++) {
      e[t][0][i] = __builtin_amdgcn_exp2f(__builtin_fmaf(p[t][0][i], c, bp));
      e[t][0][4 + i] = __builtin_amdgcn_exp2f(__builtin_fmaf(p[t][1][i], c, bp1));
      e[t][1][i] = __builtin_amdgcn_exp2f(__builtin_fmaf(p[t][0][i], -c, bn));
      e[t][1][4 + i] = __builtin_amdgcn_exp2f(__builtin_fmaf(p[t][1][i], -c, bn1));
      s[t][0] += e[t][0][i] + e[t][0][4 + i];
      s[t][1] += e[t][1][i] + e[t][1][4 + i];
    }
  }
  red4<false>(s[0][0], s[0][1], s[1][0], s[1][1]);
#pragma unroll
  for (int t = 0; t < 2; t++)
#pragma unroll
    for (int q = 0; q < 2; q++) {
      const float r = __builtin_amdgcn_rcpf(s[t][q]);
      out[t][q] = (u4){pk2(e[t][q][0] * r, e[t][q][1] * r), pk2(e[t][q][2] * r, e[t][q][3] * r), pk2(e[t][q][4] * r, e[t][q][5] * r), pk2(e[t][q][6] * r, e[t][q][7] * r)};
    }
}

template <int N> DI void recheck(u4 (&d)[N], rsrc_t ws, unsigned vo, unsigned so, unsigned stride) {
  unsigned m = 0;
#pragma unroll
  for (int i = 0; i < N; i++) m = umax4(d[i], m);
  if (__builtin_expect(__any(m == POISON), 0)) {
    for (unsigned sp = 0; sp < SPIN_CAP; sp++) {
      asm volatile("" ::: "memory");
      m = 0;
#pragma unroll
      for (int i = 0; i < N; i++) { d[i] = ld128(ws, vo, so + i * stride, 1); m = umax4(d[i], m); }
      if (!__any(m == POISON)) break;
    }
  }
}

__global__ __launch_bounds__(256, 1) void fused_block(const char* __restrict__ gx, const char* __restrict__ gwqkv, const char* __restrict__ gwphi, const char* __restrict__ gwo,
                                                      const char* __restrict__ gw1, const char* __restrict__ gw2, char* __restrict__ gout, char* gws) {
  extern __shared__ __attribute__((aligned(16))) char sm[];
  STAMP(0);
  const unsigned lane = threadIdx.x & 63, wv = rfl(threadIdx.x >> 6);
  const unsigned b = blockIdx.x, xc = b & 7, bi = b >> 3;
  {
    u64 pc; asm volatile("s_getpc_b64 %0" : "=s"(pc));
    const char* p = (const char*)(pc & ~127ull) + (bi * 8 + (lane & 7)) * 128;
    if (wv == 3) asm volatile("s_mov_b32 m0, %0\n\ts_nop 0\n\tglobal_load_lds_dword %1, off" :: "s"(L_TOTAL - 256), "v"(p) : "memory");
  }
  const unsigned rbA = (xc >> 1) * 4 + (bi & 3), hd = (xc & 1) * 8 + (bi >> 2);
  const unsigned rbC = xc * 2 + (bi & 1), jc = bi >> 1;
  const bool act = jc < 14;
  const unsigned half = jc >= 7 ? 1 : 0, cg = act ? jc - 7 * half : 0;
  const unsigned r = lane & 15, g = lane >> 4;
  const rsrc_t rx = __builtin_amdgcn_make_buffer_rsrc((void*)gx, 0, 2048 * 896, 0x20000);
  const rsrc_t rq = __builtin_amdgcn_make_buffer_rsrc((void*)gwqkv, 0, 448 * 2688, 0x20000);
  const rsrc_t rp = __builtin_amdgcn_make_buffer_rsrc((void*)gwphi, 0, 16 * 784 * 2, 0x20000);
  const rsrc_t ro = __builtin_amdgcn_make_buffer_rsrc((void*)gwo, 0, 448 * 896, 0x20000);
  const rsrc_t r1 = __builtin_amdgcn_make_buffer_rsrc((void*)gw1, 0, 448 * 3584, 0x20000);
  const rsrc_t r2 = __builtin_amdgcn_make_buffer_rsrc((void*)gw2, 0, 1792 * 896, 0x20000);
  const rsrc_t rout = __builtin_amdgcn_make_buffer_rsrc((void*)gout, 0, 2048 * 896, 0x20000);
  const rsrc_t ws = __builtin_amdgcn_make_buffer_rsrc((void*)gws, 0, OFF_END, 0x20000);
  const unsigned tag = *(const unsigned*)(gws + OFF_E) + 0x9E3779B9u;
  const unsigned l16 = lane * 16;
  const u4 poison = {POISON, POISON, POISON, POISON};
  constexpr unsigned OOB = 0x80000000u;

  unsigned short pl[2][8];
  {
    const unsigned pc = wv == 0 ? 1 : wv == 1 ? 2 : 0;
    if (rbA < 14) {
      const unsigned v0 = wv < 3 ? g * 8 * 2688 + r * 2 : OOB, v1 = (wv < 3 && r < 12) ? v0 + 32 : OOB;
      const unsigned sb = rbA * 32 * 2688 + pc * 896 + hd * 56;
#pragma unroll
      for (int j = 0; j < 8; j++) { pl[0][j] = __builtin_amdgcn_raw_buffer_load_b16(rq, v0, sb + j * 2688, 0); pl[1][j] = __builtin_amdgcn_raw_buffer_load_b16(rq, v1, sb + j * 2688, 0); }
    } else {
      unsigned f = (rbA - 14) * 16 + r; f = f >= 28 ? f - 4 : f;
      const unsigned v0 = wv == 3 ? (g * 4 * 28 + f) * 2 : OOB, v1 = (wv == 3 && g < 3) ? v0 + 16 * 56 : OOB;
#pragma unroll
      for (int j = 0; j < 4; j++) { pl[0][j] = __builtin_amdgcn_raw_buffer_load_b16(rp, v0, hd * 1568 + j * 56, 0); pl[0][4 + j] = __builtin_amdgcn_raw_buffer_load_b16(rp, v1, hd * 1568 + j * 56, 0); }
#pragma unroll
      for (int j = 0; j < 8; j++) pl[1][j] = 0;
    }
  }
  FENCE();
  u4 xf[2][14];
  const unsigned xv = (rbA * 128 + wv * 32 + r) * 896 + g * 16;
#pragma unroll
  for (int s = 0; s < 14; s++) xf[0][s] = ld128(rx, xv, s * 64, 0);
  FENCE();
  {
    u4 fr[2];
#pragma unroll
    for (int u = 0; u < 2; u++) fr[u] = (u4){pl[u][0] | (unsigned)pl[u][1] << 16, pl[u][2] | (unsigned)pl[u][3] << 16, pl[u][4] | (unsigned)pl[u][5] << 16, pl[u][6] | (unsigned)pl[u][7] << 16};
    const unsigned pb = OFF_PACK + hd * PACKB;
    const unsigned i0 = wv < 2 ? 2 * wv : 60, is = wv < 2 ? 4 : 2;
    if (wv < 3) {
      float c0 = 0.f, c1 = 0.f, z0 = 0.f, z1 = 0.f;
#pragma unroll
      for (int k = 0; k < 4; k++) { c0 = dot2(fr[0][k], 0x3F803F80u, c0); c1 = dot2(fr[1][k], 0x3F803F80u, c1); }
      red4<false>(c0, c1, z0, z1);
      const float c = g == 0 ? c0 : c1;
      const unsigned hi = pk2(c, 0.f) & 0xFFFFu, cs = hi | (pk2(c - __uint_as_float(hi << 16), 0.f) << 16);
      if (rbA < 14) { st128(ws, fr[0], l16, pb + (i0 + is * rbA) * 1024); st128(ws, fr[1], l16, pb + (i0 + is * rbA + 1) * 1024); }
      if (lane < 32) __builtin_amdgcn_raw_buffer_store_b32(cs, ws, g * 1024 + r * 16, pb + (i0 + is * 14) * 1024 + (rbA >> 2) * 256 + (rbA & 3) * 4, SC1);
    } else if (rbA >= 14) st128(ws, fr[0], l16, pb + (90 + rbA - 14) * 1024);
    st128(ws, poison, l16, OFF_PART + (hd * 16 + rbA) * 4096 + wv * 1024);
  }
  FENCE();
#pragma unroll
  for (int s = 0; s < 14; s++) xf[1][s] = ld128(rx, xv, 16 * 896 + s * 64, 0);
  WAITV(14);
  if (lane == 0) __builtin_amdgcn_raw_buffer_store_b32(tag, ws, 0, OFF_FLAGA + hd * 256 + (rbA * 4 + wv) * 4, SC1);
  FENCE();
  STAMP(1);
  float rstd[2]; u4 xaug[2];
  {
    float s0 = 0.f, q0 = 0.f, s1 = 0.f, q1 = 0.f;
#pragma unroll
    for (int s = 0; s < 14; s++)
#pragma unroll
      for (int k = 0; k < 4; k++) { s0 = dot2(xf[0][s][k], 0x3F803F80u, s0); q0 = dot2(xf[0][s][k], xf[0][s][k], q0); }
#pragma unroll
    for (int s = 0; s < 14; s++)
#pragma unroll
      for (int k = 0; k < 4; k++) { s1 = dot2(xf[1][s][k], 0x3F803F80u, s1); q1 = dot2(xf[1][s][k], xf[1][s][k], q1); }
    red4<false>(s0, q0, s1, q1);
    const float m0 = s0 * (1.f / 448), m1 = s1 * (1.f / 448);
    rstd[0] = __builtin_amdgcn_rsqf(vmax(q0 * (1.f / 448) - m0 * m0, 0.f) + 1e-5f);
    rstd[1] = __builtin_amdgcn_rsqf(vmax(q1 * (1.f / 448) - m1 * m1, 0.f) + 1e-5f);
    const unsigned a0 = pk2(-m0, -m0), a1 = pk2(-m1, -m1);
    xaug[0] = (u4){a0, a0, a0, a0}; xaug[1] = (u4){a1, a1, a1, a1};
  }
  STAMP(2);
  for (unsigned sp = 0; sp < SPIN_CAP; sp++) {
    asm volatile("" ::: "memory");
    const unsigned f = __builtin_amdgcn_raw_buffer_load_b32(ws, lane * 4, OFF_FLAGA + hd * 256, SC1);
    if (__all(f == tag)) break;
  }
  FENCE();
  STAMP(3);
  {
    const unsigned pb = OFF_PACK + hd * PACKB + wv * 1024;
#pragma unroll
    for (int s = 0; s < 23; s++) glds_sc1(ws, l16, pb + s * 4096, L_P + wv * 1024 + s * 4096);
  }
  const unsigned imgv = (lane >> 3), imgc = ((lane & 7) ^ (sig(lane >> 3) << 1)) * 16;
  const unsigned w2v = imgv * 896 + imgc;
  auto w2_issue = [&](int ph, unsigned lds) {
#pragma unroll
    for (int k = 0; k < 7; k++) {
      const unsigned q = wv * 7 + k, f0 = (q >> 1) * 128 + ph * 32 + (q & 1) * 16;
      glds(r2, w2v, f0 * 896 + cg * 128, lds + k * 2048);
      glds(r2, w2v, (f0 + 8) * 896 + cg * 128, lds + k * 2048 + 1024);
    }
  };
  u4 w2f[4][7][2];
  const unsigned tq = (lane & 15) >> 2, tp = lane & 3, blk = (lane >> 4) & 1, hf = lane >> 5;
  auto w2_read = [&](int ph, unsigned lds) {
    const unsigned row = 4 * hf + tq;
#pragma unroll
    for (int n = 0; n < 2; n++) {
      const unsigned a = lds + row * 128 + (((2 * n + blk) ^ sig(row)) << 5) + tp * 8;
#pragma unroll
      for (int k = 0; k < 7; k++) w2f[ph][k][n] = trd2(a + k * 2048, a + k * 2048 + 1024);
    }
  };
  const unsigned lsw = L_S + wv * 14336;
  if (act) w2_issue(0, lsw);
  FENCE();
  f4 ak[2][2] = {}, av[2][2] = {};
#pragma unroll
  for (int c = 0; c < 3; c++) {
    if (act) { if (c == 0) WAITV(18 + 14); else if (c == 1) WAITV(13 + 14); else WAITV(8 + 14); }
    else { if (c == 0) WAITV(18); else if (c == 1) WAITV(13); else WAITV(8); }
    BAR();
#pragma unroll
    for (int s = 5 * c; s < 5 * c + 5; s++) {
      const u4 k0 = ldsr128(L_P + l16 + s * 4096), k1 = ldsr128(L_P + l16 + s * 4096 + 1024), v0 = ldsr128(L_P + l16 + s * 4096 + 2048), v1 = ldsr128(L_P + l16 + s * 4096 + 3072);
#pragma unroll
      for (int t = 0; t < 2; t++) {
        const u4 xb = s < 14 ? xf[t][s < 14 ? s : 0] : xaug[t];
        ak[t][0] = mfma16(k0, xb, ak[t][0]); ak[t][1] = mfma16(k1, xb, ak[t][1]);
        av[t][0] = mfma16(v0, xb, av[t][0]); av[t][1] = mfma16(v1, xb, av[t][1]);
      }
    }
  }
  STAMP(4);
  if (act) WAITV(14); else WAITV(0);
  BAR();
  st128(ws, poison, l16, OFF_O + ((rbA * 8 + wv * 2) * 16 + hd) * 1024);
  st128(ws, poison, l16, OFF_O + ((rbA * 8 + wv * 2 + 1) * 16 + hd) * 1024);
  if (act) {
    const unsigned t16 = rbC * 8 + half * 4 + wv;
    const unsigned hv = (g & 1) * 1024 + ((t16 & 1) * 16 + r + (g >> 1) * 32) * 16;
    st128(ws, poison, hv, OFF_H + ((t16 >> 1) * 28 + cg * 4) * 1024);
    st128(ws, poison, hv, OFF_H + ((t16 >> 1) * 28 + cg * 4 + 2) * 1024);
#pragma unroll
    for (int k = 0; k < 8; k++) st128(ws, poison, l16, OFF_H1 + (((rbC * 4 + wv) * 4 + (k >> 1)) * 28 + 2 * jc + (k & 1)) * 1024);
  } else if (rbC < 14 && wv < 2) st128(ws, poison, l16, OFF_AUG + (rbC * 4 + (jc - 14) * 2 + wv) * 1024);
  FENCE();
  const u4 wp0 = ldsr128(L_P + 90 * 1024 + l16), wp1 = ldsr128(L_P + 91 * 1024 + l16);
  const bool pad = g == 3;
  {
    f4 p[2][2]; u4 ph[2][2];
#pragma unroll
    for (int t = 0; t < 2; t++) {
      const u4 kb = {pk2(ak[t][0][0], ak[t][0][1]), pk2(ak[t][0][2], ak[t][0][3]), pk2(ak[t][1][0], ak[t][1][1]), pk2(ak[t][1][2], ak[t][1][3])};
      p[t][0] = mfma16(wp0, kb, (f4){0, 0, 0, 0}); p[t][1] = mfma16(wp1, kb, (f4){0, 0, 0, 0});
    }
    features(p, rstd, pad, ph);
    const unsigned fs = ((r & 7) << 2) | (r >> 2);
#pragma unroll
    for (int t = 0; t < 2; t++) {
      const unsigned rb_ = L_STG + (wv * 32 + t * 16 + r) * 256;
      ldsw64(rb_ + (((0 + g) ^ fs) << 3), (u2){ph[t][0].x, ph[t][0].y});
      ldsw64(rb_ + (((4 + g) ^ fs) << 3), (u2){ph[t][0].z, ph[t][0].w});
      ldsw64(rb_ + (((8 + g) ^ fs) << 3), (u2){ph[t][1].x, ph[t][1].y});
      ldsw64(rb_ + (((12 + g) ^ fs) << 3), (u2){ph[t][1].z, ph[t][1].w});
      const float rs = rstd[t];
      const float one = pad ? 1.f : av[t][1][0] * rs;
      ldsw64(rb_ + (((16 + g) ^ fs) << 3), (u2){pk2(av[t][0][0] * rs, av[t][0][1] * rs), pk2(av[t][0][2] * rs, av[t][0][3] * rs)});
      ldsw64(rb_ + (((20 + g) ^ fs) << 3), (u2){pk2(one, av[t][1][1] * rs), pk2(av[t][1][2] * rs, av[t][1][3] * rs)});
    }
  }
  STAMP(5);
  BAR();
  {
    const unsigned sp_ = wv >> 1, u = wv & 1;
    const unsigned row = 4 * g + tq, fs = ((row & 7) << 2) | (row >> 2);
    const unsigned a0 = L_STG + row * 256 + (((8 * sp_ + tp) ^ fs) << 3), a1 = L_STG + row * 256 + (((8 * sp_ + 4 + tp) ^ fs) << 3), bv = L_STG + row * 256 + (((16 + 4 * u + tp) ^ fs) << 3);
    f4 c0 = {0, 0, 0, 0}, c1 = {0, 0, 0, 0};
#pragma unroll
    for (int k = 0; k < 4; k++) {
      const u4 vb = trd2(bv + k * 8192, bv + k * 8192 + 4096);
      c0 = mfma16(trd2(a0 + k * 8192, a0 + k * 8192 + 4096), vb, c0);
      c1 = mfma16(trd2(a1 + k * 8192, a1 + k * 8192 + 4096), vb, c1);
    }
    st128(ws, (u4){pk2(c0[0], c0[1]), pk2(c0[2], c0[3]), pk2(c1[0], c1[1]), pk2(c1[2], c1[3])}, l16, OFF_PART + (hd * 16 + rbA) * 4096 + wv * 1024);
  }
  FENCE();
  STAMP(6);
  WAITV(1);
  if (act) { w2_read(0, lsw); asm volatile("s_waitcnt lgkmcnt(0)" ::: "memory"); FENCE(); w2_issue(1, lsw); }
  FENCE();
  u4 phq[2][2];
  {
    f4 aq[2][2] = {};
#pragma unroll
    for (int s = 0; s < 15; s++) {
      const u4 q0 = ldsr128(L_P + 61440 + l16 + s * 2048), q1 = ldsr128(L_P + 61440 + l16 + s * 2048 + 1024);
#pragma unroll
      for (int t = 0; t < 2; t++) {
        const u4 xb = s < 14 ? xf[t][s < 14 ? s : 0] : xaug[t];
        aq[t][0] = mfma16(q0, xb, aq[t][0]); aq[t][1] = mfma16(q1, xb, aq[t][1]);
      }
    }
    BAR();
    if (threadIdx.x < 2) __builtin_amdgcn_raw_buffer_store_b32(tag, ws, lane == 0 ? OFF_FLAGBA + (rbA * 16 + hd) * 4 : OFF_FLAGBC + (rbC * 16 + jc) * 4, 0, SC1);
    FENCE();
    STAMP(7);
    f4 p[2][2];
#pragma unroll
    for (int t = 0; t < 2; t++) {
      const u4 qb = {pk2(aq[t][0][0], aq[t][0][1]), pk2(aq[t][0][2], aq[t][0][3]), pk2(aq[t][1][0], aq[t][1][1]), pk2(aq[t][1][2], aq[t][1][3])};
      p[t][0] = mfma16(wp0, qb, (f4){0, 0, 0, 0}); p[t][1] = mfma16(wp1, qb, (f4){0, 0, 0, 0});
    }
    features(p, rstd, pad, phq);
  }
  FENCE();
  STAMP(8);
  u4 pt[16];
#pragma unroll
  for (int k = 0; k < 16; k++) pt[k] = ld128(ws, l16, OFF_PART + (hd * 16 + k) * 4096 + wv * 1024, 1);
  const unsigned fbv = lane < 16 ? OFF_FLAGBA + (rbC * 16 + lane) * 4 : lane < 32 ? OFF_FLAGBC + (rbC * 16 + lane - 16) * 4 : OFF_FLAGBC + ((act ? jc : 0) * 16 + 14 + (lane & 1)) * 4;
  unsigned fb = __builtin_amdgcn_raw_buffer_load_b32(ws, fbv, 0, SC1);
  u2 xr[4];
  const unsigned t16 = rbC * 8 + half * 4 + wv;
#pragma unroll
  for (int u = 0; u < 4; u++) xr[u] = __builtin_amdgcn_raw_buffer_load_b64(rx, (t16 * 16 + r) * 896 + g * 8, cg * 128 + u * 32, 0);
  FENCE();
  const unsigned wov = imgv * 896 + imgc;
  if (act) {
    WAITV(21);
    w2_read(1, lsw); asm volatile("s_waitcnt lgkmcnt(0)" ::: "memory"); FENCE();
#pragma unroll
    for (int k = 0; k < 14; k++) glds(ro, wov, (wv * 14 + k) * 8 * 896 + cg * 128, L_X + (wv * 14 + k) * 1024);
    if (wv == 0 && lane < 32) ldsw128(L_X + 448 * 128 + l16, (u4){0, 0, 0, 0});
    w2_issue(2, lsw);
  }
  FENCE();
  recheck<16>(pt, ws, l16, OFF_PART + hd * 16 * 4096 + wv * 1024, 4096);
  STAMP(9);
  {
    const unsigned one = (r & 1) ? 0x3F800000u : 0x00003F80u, hit = (r >> 2) == g;
    const u4 s0 = {hit && !(r & 2) ? one : 0, hit && (r & 2) ? one : 0, 0, 0}, s1 = {0, 0, s0.x, s0.y};
    f4 c0 = {0, 0, 0, 0}, c1 = {0, 0, 0, 0};
#pragma unroll
    for (int k = 0; k < 16; k++) { c0 = mfma16(s0, pt[k], c0); c1 = mfma16(s1, pt[k], c1); }
    ldsw128(L_KV + wv * 1024 + l16, (u4){pk2(c0[0], c0[1]), pk2(c0[2], c0[3]), pk2(c1[0], c1[1]), pk2(c1[2], c1[3])});
  }
  for (unsigned sp = 0; sp < SPIN_CAP; sp++) {
    if (__all(fb == tag)) break;
    asm volatile("" ::: "memory");
    fb = __builtin_amdgcn_raw_buffer_load_b32(ws, fbv, 0, SC1);
  }
  BAR();
  {
    const u4 kv00 = ldsr128(L_KV + l16), kv01 = ldsr128(L_KV + 1024 + l16), kv10 = ldsr128(L_KV + 2048 + l16), kv11 = ldsr128(L_KV + 3072 + l16);
    const u4 z0 = ldsr128(L_KV + 1024 + (g * 16 + 12) * 16), z1 = ldsr128(L_KV + 3072 + (g * 16 + 12) * 16);
#pragma unroll
    for (int t = 0; t < 2; t++) {
      const f4 zero = {0, 0, 0, 0};
      const f4 n0 = mfma16(kv10, phq[t][1], mfma16(kv00, phq[t][0], zero)), n1 = mfma16(kv11, phq[t][1], mfma16(kv01, phq[t][0], zero));
      const f4 dn = mfma16(z1, phq[t][1], mfma16(z0, phq[t][0], zero));
      const float rc = __builtin_amdgcn_rcpf(dn[0] + 1e-6f);
      st128(ws, (u4){pk2(n0[0] * rc, n0[1] * rc), pk2(n0[2] * rc, n0[3] * rc), pk2(pad ? 0.f : n1[0] * rc, n1[1] * rc), pk2(n1[2] * rc, n1[3] * rc)},
            l16, OFF_O + ((rbA * 8 + wv * 2 + t) * 16 + hd) * 1024);
    }
  }
  FENCE();
  STAMP(10);
  const unsigned w1v = imgv * 3584 + imgc;
  const unsigned crow = 8 * hf + tq;
  if (!act) {
    if (rbC < 14) {
#pragma unroll
      for (int k = 0; k < 14; k++) glds(r1, w1v, (wv * 14 + k) * 8 * 3584 + rbC * 256 + (jc - 14) * 128, L_YA + (wv * 14 + k) * 1024);
      WAITV(0); BAR();
      if (wv < 2) {
        const unsigned a0 = L_YA + crow * 128 + (((2 * wv + blk) ^ sig(crow)) << 5) + tp * 8, a1 = L_YA + (crow + 4) * 128 + (((2 * wv + blk) ^ sig(crow + 4)) << 5) + tp * 8;
        const u4 ones = {0x3F803F80u, 0x3F803F80u, 0x3F803F80u, 0x3F803F80u};
        f16v c = {};
#pragma unroll 4
        for (int k = 0; k < 28; k++) c = mfma32(ones, trd2(a0 + k * 2048, a1 + k * 2048), c);
        const unsigned hi = pk2(c[0], 0.f) & 0xFFFFu, cs = hi | (pk2(c[0] - __uint_as_float(hi << 16), 0.f) << 16);
        st128(ws, (u4){hf == 0 ? cs : 0, 0, 0, 0}, l16, OFF_AUG + (rbC * 4 + (jc - 14) * 2 + wv) * 1024);
      }
    }
  } else {
    WAITV(2);
    w2_read(2, lsw);
    BAR();
#pragma unroll
    for (int k = 0; k < 14; k++) glds(r1, w1v, (wv * 14 + k) * 8 * 3584 + jc * 256, L_YA + (wv * 14 + k) * 1024);
    FENCE();
    u4 of[16];
#pragma unroll
    for (int k = 0; k < 16; k++) of[k] = ld128(ws, l16, OFF_O + (t16 * 16 + k) * 1024, 1);
    FENCE();
#pragma unroll
    for (int k = 0; k < 7; k++) glds(r1, w1v, (wv * 7 + k) * 8 * 3584 + jc * 256 + 128, L_YB + (wv * 7 + k) * 1024);
    FENCE();
    recheck<16>(of, ws, l16, OFF_O + t16 * 16 * 1024, 1024);
    STAMP(11);
    f4 ah[4] = {};
    {
      const unsigned row = 4 * g + tq;
#pragma unroll
      for (int k = 0; k < 16; k++) {
        const unsigned sg = sig(row + 28 * k);
#pragma unroll
        for (int u = 0; u < 4; u++) {
          const unsigned a = L_X + (row + 28 * k) * 128 + ((u ^ sg) << 5) + tp * 8;
          ah[u] = mfma16(trd2(a, a + 2048), of[k], ah[u]);
        }
      }
    }
    {
      unsigned d[4][2];
#pragma unroll
      for (int u = 0; u < 4; u++) {
        d[u][0] = pk2(ah[u][0] + __uint_as_float(xr[u].x << 16), ah[u][1] + __uint_as_float(xr[u].x & 0xFFFF0000u));
        d[u][1] = pk2(ah[u][2] + __uint_as_float(xr[u].y << 16), ah[u][3] + __uint_as_float(xr[u].y & 0xFFFF0000u));
      }
      const unsigned hv = (g & 1) * 1024 + ((t16 & 1) * 16 + r + (g >> 1) * 32) * 16;
#pragma unroll
      for (int a = 0; a < 2; a++) {
        swap16u(d[2 * a][0], d[2 * a + 1][0]); swap16u(d[2 * a][1], d[2 * a + 1][1]);
        const u4 v = {d[2 * a][0], d[2 * a][1], d[2 * a + 1][0], d[2 * a + 1][1]};
        st128(ws, v, hv, OFF_H + ((t16 >> 1) * 28 + cg * 4 + 2 * a) * 1024);
        ldsw128(L_HS + (wv >> 1) * 4096 + a * 2048 + hv, v);
      }
    }
    FENCE();
    STAMP(12);
    BAR();
#pragma unroll
    for (int k = 0; k < 7; k++) glds(r1, w1v, (224 + (wv * 7 + k) * 8) * 3584 + jc * 256 + 128, L_X + (wv * 7 + k) * 1024);
    FENCE();
    const unsigned t32 = rbC * 4 + wv;
    u4 hfr[4][7], aug[4];
#pragma unroll
    for (int k = 0; k < 28; k++) hfr[k / 7][k % 7] = ld128(ws, l16, OFF_H + (t32 * 28 + k) * 1024, 1);
#pragma unroll
    for (int k = 0; k < 4; k++) aug[k] = ld128(ws, l16, OFF_AUG + (jc * 4 + k) * 1024, 1);
    FENCE();
    const unsigned sw0 = crow * 128 + tp * 8, sw1 = (crow + 4) * 128 + tp * 8;
    const unsigned ca0 = sw0 + ((blk ^ sig(crow)) << 5), ca1 = sw1 + ((blk ^ sig(crow + 4)) << 5);
    const unsigned cb0 = sw0 + (((2 + blk) ^ sig(crow)) << 5), cb1 = sw1 + (((2 + blk) ^ sig(crow + 4)) << 5);
    f16v cu[4] = {};
    float sh = 0.f, qh = 0.f;
#pragma unroll
    for (int c = 0; c < 4; c++) {
      recheck<7>(hfr[c], ws, l16, OFF_H + (t32 * 28 + c * 7) * 1024, 1024);
      if (c == 0) STAMP(13);
#pragma unroll
      for (int k = 0; k < 7; k++) {
        const unsigned o = L_YA + (c * 7 + k) * 2048;
        cu[0] = mfma32(trd2(o + ca0, o + ca1), hfr[c][k], cu[0]);
        cu[1] = mfma32(trd2(o + cb0, o + cb1), hfr[c][k], cu[1]);
#pragma unroll
        for (int e = 0; e < 4; e++) { sh = dot2(hfr[c][k][e], 0x3F803F80u, sh); qh = dot2(hfr[c][k][e], hfr[c][k][e], qh); }
      }
    }
    recheck<4>(aug, ws, l16, OFF_AUG + jc * 4 * 1024, 1024);
    swap32(sh, qh); { float t = sh + qh, t2 = t; swap32(t, t2); sh = t; qh = t2; }
    const float mh = sh * (1.f / 448), var = vmax(qh * (1.f / 448) - mh * mh, 0.f) + 1e-5f;
    const float rsh = __builtin_amdgcn_rsqf(var), sdh = var * rsh, c1 = -rsh * LOG2E;
    const unsigned am = pk2(-mh, -mh);
    const u4 haug = {am, am, am, am};
    auto silu_store = [&](int ph) {
      unsigned d[8];
#pragma unroll
      for (int i = 0; i < 8; i++) {
        const float a = cu[ph][2 * i], bb = cu[ph][2 * i + 1];
        d[i] = pk2(a * __builtin_amdgcn_rcpf(__builtin_fmaf(__builtin_amdgcn_exp2f(a * c1), sdh, sdh)), bb * __builtin_amdgcn_rcpf(__builtin_fmaf(__builtin_amdgcn_exp2f(bb * c1), sdh, sdh)));
      }
      st128(ws, (u4){d[0], d[1], d[2], d[3]}, l16, OFF_H1 + ((t32 * 4 + ph) * 28 + 2 * jc) * 1024);
      st128(ws, (u4){d[4], d[5], d[6], d[7]}, l16, OFF_H1 + ((t32 * 4 + ph) * 28 + 2 * jc + 1) * 1024);
    };
    cu[0] = mfma32(aug[0], haug, cu[0]); cu[1] = mfma32(aug[1], haug, cu[1]);
    STAMP(14);
    BAR();
    w2_issue(3, L_YA + wv * 14336);
    FENCE();
#pragma unroll
    for (int k = 0; k < 28; k++) {
      const unsigned o = k < 14 ? L_YB + k * 2048 : L_X + (k - 14) * 2048;
      cu[2] = mfma32(trd2(o + ca0, o + ca1), hfr[k / 7][k % 7], cu[2]);
    }
    cu[2] = mfma32(aug[2], haug, cu[2]);
    silu_store(0); silu_store(1);
    FENCE();
#pragma unroll
    for (int k = 0; k < 28; k++) {
      const unsigned o = k < 14 ? L_YB + k * 2048 : L_X + (k - 14) * 2048;
      cu[3] = mfma32(trd2(o + cb0, o + cb1), hfr[k / 7][k % 7], cu[3]);
    }
    cu[3] = mfma32(aug[3], haug, cu[3]);
    silu_store(2);
    FENCE();
    silu_store(3);
    FENCE();
    STAMP(15);
    WAITV(8);
    w2_read(3, L_YA + wv * 14336);
    f16v dd[2][2] = {};
    const unsigned hb = OFF_H1 + ((rbC * 4 + half * 2) * 4 * 28 + wv * 7) * 1024;
    u4 gf[2][2][7];
#pragma unroll
    for (int m = 0; m < 2; m++)
#pragma unroll
      for (int k = 0; k < 7; k++) gf[0][m][k] = ld128(ws, l16, hb + (m * 112 + k) * 1024, 1);
#pragma unroll
    for (int ph = 0; ph < 4; ph++) {
      if (ph < 3) {
#pragma unroll
        for (int m = 0; m < 2; m++)
#pragma unroll
          for (int k = 0; k < 7; k++) gf[(ph + 1) & 1][m][k] = ld128(ws, l16, hb + (m * 112 + (ph + 1) * 28 + k) * 1024, 1);
      }
      FENCE();
#pragma unroll
      for (int m = 0; m < 2; m++) {
        recheck<7>(gf[ph & 1][m], ws, l16, hb + (m * 112 + ph * 28) * 1024, 1024);
        if (ph == 0 && m == 0) STAMP(16);
#pragma unroll
        for (int k = 0; k < 7; k++) {
          dd[m][0] = mfma32(w2f[ph][k][0], gf[ph & 1][m][k], dd[m][0]);
          dd[m][1] = mfma32(w2f[ph][k][1], gf[ph & 1][m][k], dd[m][1]);
        }
      }
      FENCE();
    }
    STAMP(17);
    BAR();
#pragma unroll
    for (int d = 0; d < 4; d++) {
      if (d != wv) {
        const unsigned slot = L_RED + (d * 3 + (wv > d ? wv - 1 : wv)) * 4096 + l16;
        const f16v& v = dd[d >> 1][d & 1];
#pragma unroll
        for (int q = 0; q < 4; q++) ldsw128(slot + q * 1024, __builtin_bit_cast(u4, (f4){v[4 * q], v[4 * q + 1], v[4 * q + 2], v[4 * q + 3]}));
      }
    }
    f16v mine = wv == 0 ? dd[0][0] : wv == 1 ? dd[0][1] : wv == 2 ? dd[1][0] : dd[1][1];
    BAR();
    unsigned d[4][2];
#pragma unroll
    for (int q = 0; q < 4; q++) {
      f4 a = {mine[4 * q], mine[4 * q + 1], mine[4 * q + 2], mine[4 * q + 3]};
#pragma unroll
      for (int s = 0; s < 3; s++) a += __builtin_bit_cast(f4, ldsr128(L_RED + (wv * 3 + s) * 4096 + q * 1024 + l16));
      const u2 hr = ldsr64(L_HS + (wv >> 1) * 4096 + ((wv & 1) * 2 + (q >> 1)) * 1024 + ((lane & 31) + 32 * (q & 1)) * 16 + hf * 8);
      d[q][0] = pk2(a[0] + __uint_as_float(hr.x << 16), a[1] + __uint_as_float(hr.x & 0xFFFF0000u));
      d[q][1] = pk2(a[2] + __uint_as_float(hr.y << 16), a[3] + __uint_as_float(hr.y & 0xFFFF0000u));
    }
    const unsigned ov = ((rbC * 4 + half * 2 + (wv >> 1)) * 32 + (lane & 31)) * 896 + hf * 16;
#pragma unroll
    for (int a = 0; a < 2; a++) {
      swap32u(d[2 * a][0], d[2 * a + 1][0]); swap32u(d[2 * a][1], d[2 * a + 1][1]);
      __builtin_amdgcn_raw_buffer_store_b128((u4){d[2 * a][0], d[2 * a][1], d[2 * a + 1][0], d[2 * a + 1][1]}, rout, ov, cg * 128 + (wv & 1) * 64 + a * 32, 0);
    }
    if (b == 0 && threadIdx.x == 0) __builtin_amdgcn_raw_buffer_store_b32(tag, ws, 0, OFF_E, SC1);
  }
  STAMP(18);
}

extern "C" void kernel_launch(void* const* d_in, const int* in_sizes, int n_in, void* d_out, int out_size, void* d_ws, size_t ws_size, hipStream_t stream) {
  (void)hipFuncSetAttribute((const void*)fused_block, hipFuncAttributeMaxDynamicSharedMemorySize, L_TOTAL);
  fused_block<<<256, 256, L_TOTAL, stream>>>((const char*)d_in[0], (const char*)d_in[1], (const char*)d_in[2], (const char*)d_in[3],
                                             (const char*)d_in[5], (const char*)d_in[6], (char*)d_out, (char*)d_ws);
}
```

```cpp
#include <hip/hip_runtime.h>
#include <hip/hip_bf16.h>
#define EXTRA 0
#define EXTRA_MEMSET 0
typedef __hip_bfloat16 bf16;
constexpr int S=2048, D=448, H=16, DH=28, F=1792;
__device__ __forceinline__ float b2f(bf16 v){return __bfloat162float(v);}
__device__ __forceinline__ bf16 f2b(float v){return __float2bfloat16(v);}

__global__ void k_ln(const bf16* x, bf16* y){
  int row=blockIdx.x; int t=threadIdx.x;
  float v[7]; float s=0;
  for(int i=0;i<7;i++){v[i]=b2f(x[row*D+t+64*i]); s+=v[i];}
  for(int o=32;o;o>>=1) s+=__shfl_xor(s,o);
  float m=s/D; float q=0;
  for(int i=0;i<7;i++){float d=v[i]-m; q+=d*d;}
  for(int o=32;o;o>>=1) q+=__shfl_xor(q,o);
  float r=rsqrtf(q/D+1e-5f);
  for(int i=0;i<7;i++) y[row*D+t+64*i]=f2b((v[i]-m)*r);
}
template<int MODE> __global__ void k_gemm(const bf16* A,const bf16* B,const bf16* R,bf16* C,int N,int K){
  __shared__ float As[16][65]; __shared__ float Bs[16][65];
  int tx=threadIdx.x&15, ty=threadIdx.x>>4; int bm=blockIdx.y*64, bn=blockIdx.x*64;
  float acc[4][4]={};
  for(int k0=0;k0<K;k0+=16){
    for(int i=threadIdx.x;i<64*16;i+=256){int r=i>>4,c=i&15; As[c][r]=b2f(A[(bm+r)*K+k0+c]);}
    for(int i=threadIdx.x;i<64*16;i+=256){int r=i>>6,c=i&63; Bs[r][c]=b2f(B[(k0+r)*N+bn+c]);}
    __syncthreads();
    for(int k=0;k<16;k++){ float a[4],b[4]; for(int i=0;i<4;i++){a[i]=As[k][ty*4+i]; b[i]=Bs[k][tx*4+i];}
      for(int i=0;i<4;i++)for(int j=0;j<4;j++)acc[i][j]+=a[i]*b[j];}
    __syncthreads();
  }
  for(int i=0;i<4;i++)for(int j=0;j<4;j++){int r=bm+ty*4+i,c=bn+tx*4+j; float v=acc[i][j];
    if(MODE==1) v=b2f(f2b(v))+b2f(R[r*N+c]);
    if(MODE==2){ v=b2f(f2b(v)); v=v/(1.f+__expf(-v)); }
    C[r*N+c]=f2b(v);}
}
__global__ __launch_bounds__(256) void k_feat(const bf16* qkv,const bf16* wphi,bf16* phiq,bf16* phik){
  int idx=blockIdx.x*blockDim.x+threadIdx.x;
  int which=idx&1, h=(idx>>1)&15, s=idx>>5;
  const bf16* q=qkv+s*3*D+which*D+h*DH; const bf16* w=wphi+h*DH*DH;
  float p[DH]; float mx=-1e30f, mn=1e30f;
  #pragma unroll
  for(int f=0;f<DH;f++) p[f]=0;
  #pragma unroll 1
  for(int d=0;d<DH;d++){ float qv=b2f(q[d]);
    #pragma unroll
    for(int f=0;f<DH;f++) p[f]+=qv*b2f(w[d*DH+f]); }
  #pragma unroll
  for(int f=0;f<DH;f++){float a=b2f(f2b(p[f])); p[f]=a; mx=fmaxf(mx,a); mn=fminf(mn,a);}
  float s1=0,s2=0;
  #pragma unroll
  for(int f=0;f<DH;f++){s1+=__expf(p[f]-mx); s2+=__expf(mn-p[f]);}
  bf16* o=(which?phik:phiq)+(s*H+h)*56;
  #pragma unroll
  for(int f=0;f<DH;f++){o[f]=f2b(__expf(p[f]-mx)/s1); o[28+f]=f2b(__expf(mn-p[f])/s2);}
}
__global__ void k_kv(const bf16* qkv,const bf16* phik,float* kv){
  int h=blockIdx.x/56, f=blockIdx.x%56; int d=threadIdx.x&31, g=threadIdx.x>>5;
  float a=0; for(int s=g;s<S;s+=8){ float p=b2f(phik[(s*H+h)*56+f]); float v= d<28? b2f(qkv[s*3*D+2*D+h*DH+d]) : 1.f; a+=p*v; }
  __shared__ float red[8][32]; red[g][d]=a; __syncthreads();
  if(g==0){float t=0; for(int i=0;i<8;i++)t+=red[i][d]; kv[(h*56+f)*32+d]=b2f(f2b(t));}
}
__global__ void k_o(const bf16* phiq,const float* kv,bf16* o){
  int idx=blockIdx.x*blockDim.x+threadIdx.x; int d=idx&31,h=(idx>>5)&15,s=idx>>9;
  float a=0; for(int f=0;f<56;f++) a+=b2f(phiq[(s*H+h)*56+f])*kv[(h*56+f)*32+d];
  a=b2f(f2b(a));
  float den=__shfl(a,(threadIdx.x&32)+28,64); den=b2f(f2b(den+1e-6f));
  if(d<28) o[s*D+h*DH+d]=f2b(a/den);
}
__global__ void k_empty(int* p){ if(p==nullptr) return; }

typedef unsigned long long u64;
typedef unsigned v4u __attribute__((ext_vector_type(4)));
#define LAS __attribute__((address_space(3)))
__device__ __forceinline__ u64 rt(){ u64 t; asm volatile("s_memrealtime %0\n\ts_waitcnt lgkmcnt(0)":"=s"(t)::"memory"); return t; }
__device__ __forceinline__ unsigned ldw(const char* p){ unsigned v; asm volatile("global_load_dword %0, %1, off\n\ts_waitcnt vmcnt(0)":"=v"(v):"v"(p):"memory"); return v; }
template<int MODE,int MULT,int DIV> __global__ void k_amp(const u64* val){
  u64 now=rt(); __shared__ u64 s0[256],s1[256],s2[256];
  int t=threadIdx.x; s0[t]=val[t*4]; s1[t]=val[t*4+1]; s2[t]=val[t*4+2]; __syncthreads();
  if(t==0){ u64 mn=~0ull,mx=0,sum=0; for(int i=0;i<256;i++){ mn=s0[i]<mn?s0[i]:mn; mx=s1[i]>mx?s1[i]:mx; sum+=s2[i]; }
    u64 v= MODE==0? sum/256 : MODE==1? mx-mn : now-mn; v=v*MULT/DIV; if(v>200000) v=200000; u64 a=rt(); while(rt()-a<v) __builtin_amdgcn_s_sleep(2); }
}

__global__ __launch_bounds__(256) void k_probe(const char* x,const char* w,u64* out){
  u64 t1=rt();
  if(threadIdx.x==0){ u64* o=out+blockIdx.x*4; o[0]=t1;o[1]=t1;o[2]=0; o[3]=0; }
}
extern "C" void kernel_launch(void* const* d_in, const int* in_sizes, int n_in, void* d_out, int out_size, void* d_ws, size_t ws_size, hipStream_t stream){
  const bf16* x=(const bf16*)d_in[0]; const bf16* wqkv=(const bf16*)d_in[1]; const bf16* wphi=(const bf16*)d_in[2]; const bf16* wo=(const bf16*)d_in[3];
  const bf16* w1=(const bf16*)d_in[5]; const bf16* w2=(const bf16*)d_in[6]; bf16* out=(bf16*)d_out;
  char* ws=(char*)d_ws;
  bf16* xn=(bf16*)(ws); bf16* qkv=(bf16*)(ws+(4<<20)); bf16* phiq=(bf16*)(ws+(16<<20)); bf16* phik=(bf16*)(ws+(24<<20));
  float* kv=(float*)(ws+(32<<20)); bf16* o=(bf16*)(ws+(36<<20)); bf16* h=(bf16*)(ws+(40<<20)); bf16* h1=(bf16*)(ws+(48<<20));
  for(int i=0;i<EXTRA_MEMSET;i++) hipMemsetAsync(ws+(100<<20),0,4096,stream);
  k_probe<<<256,256,0,stream>>>((const char*)x,(const char*)wqkv,(u64*)(ws+(120<<20)));
  k_ln<<<S,64,0,stream>>>(x,xn);
  k_gemm<0><<<dim3(3*D/64,S/64),256,0,stream>>>(xn,wqkv,nullptr,qkv,3*D,D);
  k_feat<<<S*H*2/256,256,0,stream>>>(qkv,wphi,phiq,phik);
  k_kv<<<H*56,256,0,stream>>>(qkv,phik,kv);
  k_o<<<S*H*32/256,256,0,stream>>>(phiq,kv,o);
  k_gemm<1><<<dim3(D/64,S/64),256,0,stream>>>(o,wo,x,h,D,D);
  k_ln<<<S,64,0,stream>>>(h,xn);
  k_gemm<2><<<dim3(F/64,S/64),256,0,stream>>>(xn,w1,nullptr,h1,F,D);
  k_gemm<1><<<dim3(D/64,S/64),256,0,stream>>>(h1,w2,h,out,D,F);
  k_amp<2,0,1><<<1,256,0,stream>>>((const u64*)(ws+(120<<20)));
  for(int i=0;i<EXTRA;i++) k_empty<<<256,256,0,stream>>>((int*)ws);
}
```

```cpp
#include <hip/hip_runtime.h>

__global__ __launch_bounds__(256, 1) void fused_block(const void* x, const void* wqkv, const void* wphi, const void* wo, const void* w1, const void* w2, void* out, void* ws) {
  asm volatile(
R"ASM(
CODE0:
s_load_dwordx16 s[4:19], %0, 0x0
s_mov_b32 s62, %1
s_getpc_b64 s[56:57]
s_and_b32 s71, s62, 0xf8
v_lshrrev_b32 v252, 5, v0
v_and_b32 v252, 6, v252
v_and_b32 v253, 1, v0
v_or3_b32 v252, v252, v253, s71
v_and_b32 v253, 2, v0
v_lshl_or_b32 v252, v253, 7, v252
v_lshlrev_b32 v252, 7, v252
v_min_u32 v252, CODE1-CODE0-256, v252
global_load_dword v254, v252, s[56:57]
v_lshrrev_b32 v253, 6, v0
v_and_b32 v249, 63, v0
v_readfirstlane_b32 s63, v253
v_and_b32 v248, 31, v0
v_bfe_u32 v247, v0, 5, 1
v_lshlrev_b32 v1, 4, v249
s_and_b32 s71, s62, 7
s_lshr_b32 s72, s62, 3
s_lshr_b32 s73, s71, 1
s_lshl_b32 s73, s73, 2
s_and_b32 s74, s72, 3
s_or_b32 s64, s73, s74
s_and_b32 s73, s71, 1
s_lshl_b32 s73, s73, 3
s_lshr_b32 s74, s72, 2
s_or_b32 s65, s73, s74
s_lshl_b32 s73, s71, 1
s_and_b32 s74, s72, 1
s_or_b32 s66, s73, s74
s_lshr_b32 s67, s72, 1
s_mov_b32 s27, 0x20000
s_mov_b32 s23, 0x20000
v_lshrrev_b32 v252, 2, v249
v_and_b32 v253, 3, v249
v_mul_u32_u24 v242, 0xa80, v252
v_lshl_add_u32 v242, v253, 4, v242
s_cmp_eq_u32 s63, 2
s_cselect_b32 s73, -1, s63
s_add_u32 s73, s73, 1
s_mul_i32 s73, s73, 0x380
s_mul_i32 s75, s64, 0x15000
s_add_u32 s75, s75, s73
s_mul_i32 s73, s65, 56
s_add_u32 s75, s75, s73
s_cmp_lt_u32 s64, 14
s_cselect_b32 s73, 0x126000, 0
s_cselect_b32 s56, 0, s63
s_cmp_lt_u32 s63, 3
s_cselect_b32 s26, s73, 0
s_lshl_b32 s76, s63, 11
s_add_u32 m0, s76, 0x1c000
s_add_u32 s76, s75, 0xa400
s_mov_b32 s22, 0x1c0000
s_cmp_eq_u32 s56, 3
s_waitcnt lgkmcnt(0)
s_mov_b64 s[24:25], s[6:7]
s_mov_b64 s[20:21], s[4:5]
s_cbranch_scc0 PHI1B
v_cmp_gt_u32 vcc, 28, v248
v_add_u32 v250, -4, v248
v_mul_u32_u24 v252, 0xe0, v247
v_cndmask_b32 v250, v250, v248, vcc
s_sub_u32 s73, s64, 14
v_lshl_add_u32 v252, v250, 1, v252
v_and_b32 v250, s73, v247
v_cmp_eq_u32 vcc, 0, v250
v_mov_b32 v253, 0x80000000
s_mul_i32 s71, s65, 0x620
s_mul_i32 s73, s73, 0x380
v_cndmask_b32 v253, v253, v252, vcc
s_add_u32 s71, s71, s73
s_mov_b64 s[52:53], s[8:9]
s_mov_b32 s54, 0x6200
s_mov_b32 s55, 0x20000
buffer_load_ushort v4, v252, s[52:55], s71 offen
buffer_load_ushort v5, v252, s[52:55], s71 offen offset:56
buffer_load_ushort v6, v252, s[52:55], s71 offen offset:112
buffer_load_ushort v7, v252, s[52:55], s71 offen offset:168
buffer_load_ushort v8, v253, s[52:55], s71 offen offset:448
buffer_load_ushort v9, v253, s[52:55], s71 offen offset:504
buffer_load_ushort v10, v253, s[52:55], s71 offen offset:560
buffer_load_ushort v11, v253, s[52:55], s71 offen offset:616
PHI1B:
buffer_load_dwordx4 v242, s[24:27], s75 offen lds
buffer_load_dwordx4 v242, s[24:27], s76 offen offset:1024 lds
v_lshrrev_b32 v252, 3, v249
v_and_b32 v253, 7, v249
v_mul_u32_u24 v250, 0x380, v252
v_lshrrev_b32 v251, 1, v252
v_xor_b32 v251, v251, v253
v_lshlrev_b32 v251, 4, v251
v_add_u32 v239, v251, v250
v_xor_b32 v251, 64, v251
v_add_u32 v238, v251, v250
s_lshl_b32 s71, s64, 2
s_add_u32 s71, s71, s63
s_mul_i32 s87, s71, 0x7000
s_lshl_b32 s88, s63, 10
s_mul_i32 s89, s63, 0x7000
s_lshl_b32 s71, s64, 1
s_add_u32 s71, s71, s65
s_mul_i32 s72, s71, 37
s_lshr_b32 s72, s72, 8
s_mul_i32 s72, s72, 7
s_sub_u32 s71, s71, s72
s_mul_i32 s80, s71, 0x1000
s_add_u32 s81, s80, 0x1000
s_cmp_eq_u32 s81, 0x7000
s_cselect_b32 s81, 0, s81
s_add_u32 s82, s81, 0x1000
s_cmp_eq_u32 s82, 0x7000
s_cselect_b32 s82, 0, s82
s_add_u32 s83, s82, 0x1000
s_cmp_eq_u32 s83, 0x7000
s_cselect_b32 s83, 0, s83
s_add_u32 s84, s83, 0x1000
s_cmp_eq_u32 s84, 0x7000
s_cselect_b32 s84, 0, s84
s_add_u32 s85, s84, 0x1000
s_cmp_eq_u32 s85, 0x7000
s_cselect_b32 s85, 0, s85
s_add_u32 s86, s85, 0x1000
s_cmp_eq_u32 s86, 0x7000
s_cselect_b32 s86, 0, s86
s_add_u32 m0, s89, 0
s_lshr_b32 s77, s80, 5
s_add_u32 s77, s77, s87
buffer_load_dwordx4 v239, s[20:23], s77 offen lds
s_add_u32 s77, s77, 0x1800
buffer_load_dwordx4 v238, s[20:23], s77 offen offset:1024 lds
s_add_u32 s77, s77, 0x1800
buffer_load_dwordx4 v239, s[20:23], s77 offen offset:2048 lds
s_add_u32 s77, s77, 0x1800
buffer_load_dwordx4 v238, s[20:23], s77 offen offset:3072 lds
s_mov_b64 s[44:45], s[18:19]
s_load_dword s70, s[44:45], 0x0
s_mov_b64 s[48:49], s[18:19]
s_mov_b64 s[52:53], s[18:19]
s_mov_b64 s[40:41], s[16:17]
s_mov_b64 s[28:29], s[10:11]
s_mov_b64 s[32:33], s[12:13]
s_mov_b64 s[36:37], s[14:15]
s_mov_b32 s42, 0x1c0000
s_mov_b32 s46, 0xf6a000
s_mov_b32 s31, 0x20000
s_mov_b32 s35, 0x20000
s_mov_b32 s39, 0x20000
s_mov_b32 s43, 0x20000
s_mov_b32 s47, 0x20000
s_mov_b32 s51, 0x20000
s_mov_b32 s55, 0x20000
s_mul_i32 s4, s65, 0x17000
s_add_u32 s4, s4, 0x50000
s_lshl_b32 s71, s65, 2
s_add_u32 s71, s71, s63
s_lshl_b32 s71, s71, 14
s_add_u32 s6, s71, 0x1c0000
s_lshl_b32 s71, s64, 10
s_add_u32 s5, s6, s71
s_mul_i32 s71, s63, 28
s_min_u32 s72, s63, 1
s_lshl_b32 s72, s72, 1
s_add_u32 s71, s71, s72
s_lshl_b32 s72, s64, 1
s_add_u32 s72, s72, s71
s_lshl_b32 s72, s72, 10
s_add_u32 s75, s72, s4
s_add_u32 s73, s64, 14
s_lshl_b32 s73, s73, 10
s_add_u32 s73, s73, s4
s_cmp_lt_u32 s64, 14
s_cselect_b32 s72, 0xf6a000, 0
s_cselect_b32 s7, 0, 0xf6a000
s_cmp_lt_u32 s63, 3
s_cselect_b32 s74, s75, s73
s_cselect_b32 s7, s72, s7
s_cselect_b32 s8, s72, 0
s_lshl_b32 s71, s63, 4
s_add_u32 s71, s71, s64
s_lshl_b32 s71, s71, 7
s_add_u32 s71, s71, s4
s_add_u32 s76, s71, 0x15800
s_lshl_b32 s71, s64, 2
s_add_u32 s71, s71, s63
s_lshl_b32 s71, s71, 6
s_lshl_b32 s72, s65, 12
s_add_u32 s72, s72, 0x24000
s_add_u32 s10, s71, s72
s_mov_b32 s58, 0
s_mov_b32 s59, -1
s_mov_b32 s73, 0x5040100
v_mov_b32 v20, 0x3f803f80
v_mov_b32 v21, 0x3f803f80
v_mov_b32 v22, 0x3f803f80
v_mov_b32 v23, 0x3f803f80
v_mov_b32 v24, -1
v_mov_b32 v25, -1
v_mov_b32 v26, -1
v_mov_b32 v27, -1
v_mov_b32 v253, 0x80000000
v_lshlrev_b32 v237, 2, v249
v_cmp_gt_u32 vcc, 16, v249
v_cndmask_b32 v237, v237, v253, s[58:59]
v_and_b32 v246, 15, v249
v_lshrrev_b32 v245, 4, v249
v_cndmask_b32 v235, v253, v237, vcc
v_bfe_u32 v244, v249, 2, 2
v_and_b32 v243, 3, v249
v_lshlrev_b32 v243, 3, v243
v_lshl_add_u32 v252, v247, 3, v244
v_bfe_u32 v253, v249, 4, 1
v_lshl_or_b32 v253, v253, 5, v243
v_lshl_add_u32 v252, v252, 6, v253
s_lshl_b32 s71, s63, 11
s_add_u32 s71, s71, 0x1c000
v_add_u32 v250, s71, v252
v_mul_u32_u24 v241, 0x380, v247
v_lshl_add_u32 v241, v248, 4, v241
v_cmp_gt_u32 vcc, 24, v248
v_mov_b32 v253, 0x80000000
s_lshl_b32 s71, s64, 4
s_add_u32 s71, s71, s65
s_lshl_b32 s71, s71, 2
s_add_u32 s57, s71, s63
v_cndmask_b32 v240, v253, v241, vcc
s_mul_i32 s79, s57, 0x700
s_waitcnt lgkmcnt(0)
s_add_u32 s70, s70, 0x9e3779b9
s_mov_b32 s9, 0x20000
v_mov_b32 v236, s70
s_mov_b32 s96, s74
s_mov_b32 s97, s75
s_mov_b32 s98, s76
v_mov_b32 v28, v250
v_lshrrev_b32 v252, 1, v248
v_and_b32 v252, 7, v252
v_xor_b32 v252, v252, v247
v_lshlrev_b32 v252, 4, v252
v_lshl_or_b32 v252, v248, 7, v252
v_add_u32 v234, s89, v252
v_xor_b32 v253, 32, v252
v_add_u32 v233, s89, v253
v_xor_b32 v253, 64, v252
v_add_u32 v232, s89, v253
v_xor_b32 v253, 0x60, v252
v_add_u32 v231, s89, v253
s_cmp_ge_u32 s67, 7
s_cselect_b32 s69, 1, 0
s_mul_i32 s71, s69, 7
s_sub_u32 s68, s67, s71
s_cmp_lt_u32 s67, 14
s_cselect_b32 s50, 0xf6a000, 0
s_cselect_b32 s30, 0x62000, 0
s_cselect_b32 s38, 0x188000, 0
s_cselect_b32 s68, s68, 0
s_lshl_b32 s17, s68, 7
s_lshl_b32 s71, s64, 2
s_add_u32 s71, s71, s63
s_lshl_b32 s71, s71, 5
s_add_u32 s71, s71, s65
s_lshl_b32 s71, s71, 10
s_add_u32 s11, s71, 0x2c0000
s_lshl_b32 s71, s66, 1
s_add_u32 s71, s71, s69
s_lshl_b32 s71, s71, 2
s_add_u32 s18, s71, s63
s_lshr_b32 s72, s18, 1
s_mul_i32 s73, s72, 7
s_add_u32 s73, s73, s68
s_lshl_b32 s74, s73, 12
s_add_u32 s12, s74, 0x4c0000
s_lshl_b32 s74, s73, 8
s_add_u32 s14, s74, 0x8000
s_lshl_b32 s72, s66, 2
s_add_u32 s72, s72, s63
s_mul_i32 s73, s72, 0x1c000
s_lshl_b32 s74, s67, 11
s_add_u32 s73, s73, s74
s_add_u32 s13, s73, 0x680000
s_lshl_b32 s71, s66, 1
s_add_u32 s71, s71, s67
s_sub_u32 s71, s71, 14
s_lshl_b32 s71, s71, 1
s_add_u32 s71, s71, s63
s_lshl_b32 s71, s71, 7
s_add_u32 s71, s71, 0x4000
s_cmp_ge_u32 s67, 14
s_cselect_b32 s15, s71, 0x4000
s_cselect_b32 s71, 0xf6a000, 0
s_cmp_lt_u32 s66, 14
s_cselect_b32 s71, s71, 0
s_cmp_lt_u32 s63, 2
s_cselect_b32 s16, s71, 0
v_bfe_u32 v252, v249, 4, 1
v_lshlrev_b32 v230, 14, v252
v_lshl_add_u32 v253, v247, 4, v246
v_lshl_add_u32 v230, v253, 4, v230
s_and_b32 s71, s63, 1
s_lshl_b32 s73, s71, 4
v_lshl_add_u32 v253, v247, 5, v246
v_add_u32 v253, s73, v253
v_lshlrev_b32 v229, 10, v252
v_lshl_add_u32 v229, v253, 4, v229
v_add_u32 v253, s73, v246
v_lshlrev_b32 v228, 7, v252
v_lshl_add_u32 v228, v253, 2, v228
v_mov_b32 v253, 0x80000000
v_cndmask_b32 v228, v228, v253, s[58:59]
v_lshrrev_b32 v252, 3, v249
v_and_b32 v250, 2, v252
v_bfe_u32 v251, v252, 2, 1
v_or_b32 v250, v250, v251
v_lshlrev_b32 v250, 1, v250
v_and_b32 v251, 7, v249
v_xor_b32 v250, v250, v251
v_lshlrev_b32 v250, 4, v250
v_mul_u32_u24 v227, 0x380, v252
v_add_u32 v227, v250, v227
v_bfe_u32 v252, v248, 1, 1
v_bfe_u32 v253, v248, 3, 1
v_lshl_or_b32 v252, v253, 1, v252
v_and_b32 v253, 4, v248
v_or_b32 v252, v252, v253
v_xor_b32 v252, v252, v247
v_lshlrev_b32 v252, 3, v252
s_lshl_b32 s71, s63, 11
v_lshl_or_b32 v252, v248, 6, v252
v_add_u32 v226, s71, v252
v_xor_b32 v225, 16, v226
v_xor_b32 v224, 32, v226
v_xor_b32 v223, 48, v226
v_lshl_add_u32 v250, v245, 2, v244
v_bfe_u32 v252, v250, 1, 1
v_bfe_u32 v253, v250, 3, 1
v_lshl_or_b32 v252, v253, 1, v252
v_and_b32 v253, 4, v250
v_or_b32 v252, v252, v253
v_lshlrev_b32 v252, 3, v252
v_xor_b32 v252, v252, v243
v_lshl_or_b32 v252, v250, 6, v252
v_add_u32 v221, 0x4000, v252
v_xor_b32 v220, 32, v221
s_and_b32 s71, s63, 1
s_lshl_b32 s71, s71, 5
s_lshr_b32 s72, s63, 1
s_lshl_b32 s72, s72, 13
v_xor_b32 v252, s71, v252
v_add_u32 v222, s72, v252
s_mov_b32 s73, 0x5040100
v_mov_b32 v250, v28
s_waitcnt vmcnt(4)
ds_read_b64_tr_b16 v[28:29], v250
ds_read_b64_tr_b16 v[30:31], v250 offset:256
ds_read_b64_tr_b16 v[32:33], v250 offset:1024
ds_read_b64_tr_b16 v[34:35], v250 offset:1280
s_waitcnt lgkmcnt(0)
s_cmp_eq_u32 s56, 3
s_cbranch_scc0 PHI2B
v_perm_b32 v28, v5, v4, s73
v_perm_b32 v29, v7, v6, s73
v_perm_b32 v30, v9, v8, s73
v_perm_b32 v31, v11, v10, s73
s_nop 1
PHI2B:
v_mfma_f32_32x32x16_bf16 v[36:51], v[20:23], v[28:31], 0
v_mfma_f32_32x32x16_bf16 v[36:51], v[20:23], v[32:35], v[36:51]
s_mov_b32 s54, s7
buffer_store_dwordx4 v[28:31], v1, s[52:55], s96 offen sc1
s_mov_b32 s54, s8
buffer_store_dwordx4 v[32:35], v1, s[52:55], s97 offen offset:1024 sc1
buffer_store_dwordx4 v[24:27], v1, s[44:47], s5 offen sc1
s_nop 6
buffer_store_dword v36, v237, s[52:55], s98 offen sc1
buffer_load_dwordx4 v[12:15], v241, s[20:23], s79 offen
buffer_load_dwordx4 v[16:19], v240, s[20:23], s79 offen offset:512
s_add_u32 m0, s89, 0x1000
s_lshr_b32 s77, s81, 5
s_add_u32 s77, s77, s87
buffer_load_dwordx4 v239, s[20:23], s77 offen lds
s_lshl_b32 s71, s62, 2
s_add_u32 s71, s71, s63
s_mul_i32 s72, s71, 0x925
s_lshr_b32 s72, s72, 16
s_mul_i32 s73, s72, 28
s_sub_u32 s73, s71, s73
s_mul_i32 s74, s73, 0xe000
s_lshl_b32 s75, s72, 7
s_add_u32 s19, s74, s75
s_lshl_b32 s74, s71, 11
s_add_u32 s91, s74, 0xd80000
s_mul_i32 s94, s71, 0x680
s_lshl_b32 s95, s71, 9
s_lshr_b32 s74, s73, 1
s_mul_i32 s74, s74, 0x15000
s_mul_i32 s90, s73, 0x3800
s_add_u32 s74, s74, s90
)ASM"
R"ASM(
s_add_u32 s74, s74, s75
s_add_u32 s74, s74, 0x14200
s_mul_i32 s73, s73, 37
s_bfe_u32 s73, s73, 0x10008
s_lshl_b32 s73, s73, 6
s_cmp_lt_u32 s71, 0x3d4
s_cselect_b32 s75, 0x188000, 0
s_cmp_lt_u32 s71, 0x310
s_cselect_b32 s19, s19, s74
s_cselect_b32 s34, 0x188000, s75
s_cselect_b32 s32, s32, s36
s_cselect_b32 s33, s33, s37
s_cselect_b32 s92, 0, s73
s_cselect_b32 s93, 3, 2
s_cselect_b32 s74, 4, 1
s_mul_i32 s74, s74, 0x380
s_lshl_b32 s75, s74, 3
s_add_u32 s75, s75, s19
s_add_u32 s90, s75, 0xfffffc00
v_lshrrev_b32 v250, 3, v249
v_and_b32 v253, 7, v249
v_mul_u32_u24 v219, s74, v250
v_lshl_add_u32 v219, v253, 4, v219
s_add_u32 s77, s77, 0x1800
buffer_load_dwordx4 v238, s[20:23], s77 offen offset:1024 lds
v_and_b32 v218, 3, v249
v_lshlrev_b32 v218, 7, v218
v_and_b32 v217, 3, v249
v_mul_u32_u24 v217, 0xe00, v217
s_add_u32 s77, s77, 0x1800
buffer_load_dwordx4 v239, s[20:23], s77 offen offset:2048 lds
s_min_u32 s72, s67, 13
s_lshl_b32 s72, s72, 10
s_add_u32 s72, s72, 0x4380
v_and_b32 v250, 1, v249
v_lshl_add_u32 v250, v250, 6, s72
s_mul_i32 s72, s68, 0x1c0
s_add_u32 s72, s72, 0xb080
v_min_u32 v253, 8, v246
v_lshl_add_u32 v253, v253, 6, s72
v_cmp_lt_u32 vcc, 33, v249
s_nop 1
v_cndmask_b32 v250, v250, v253, vcc
v_lshlrev_b32 v215, 6, v246
s_lshl_b32 s72, s66, 10
v_add_u32 v215, s72, v215
v_bfe_u32 v253, v249, 4, 1
v_lshl_add_u32 v215, v253, 14, v215
v_cndmask_b32 v215, v215, v250, s[58:59]
s_cmp_lt_u32 s67, 14
s_cselect_b32 s72, s67, s66
s_min_u32 s72, s72, 13
s_mul_i32 s72, s72, 0x380
s_add_u32 s72, s72, 0x8000
v_min_u32 v253, 13, v246
v_lshl_add_u32 v253, v253, 6, s72
v_cmp_lt_u32 vcc, 47, v249
s_nop 1
v_cndmask_b32 v215, v215, v253, vcc
v_mov_b32 v250, 0x80000000
v_cmp_gt_u32 vcc, 16, v0
s_nop 1
v_cndmask_b32 v214, v250, v237, vcc
s_add_u32 s77, s77, 0x1800
buffer_load_dwordx4 v238, s[20:23], s77 offen offset:3072 lds
s_add_u32 m0, s89, 0x2000
s_lshr_b32 s77, s82, 5
s_add_u32 s77, s77, s87
buffer_load_dwordx4 v239, s[20:23], s77 offen lds
s_add_u32 s77, s77, 0x1800
buffer_load_dwordx4 v238, s[20:23], s77 offen offset:1024 lds
s_add_u32 s77, s77, 0x1800
buffer_load_dwordx4 v239, s[20:23], s77 offen offset:2048 lds
s_add_u32 s77, s77, 0x1800
buffer_load_dwordx4 v238, s[20:23], s77 offen offset:3072 lds
s_waitcnt vmcnt(10)
buffer_store_dword v236, v235, s[44:47], s10 offen sc1
v_mov_b32 v252, 0
v_mov_b32 v253, 0
v_mov_b32 v250, 0
v_mov_b32 v251, 0
s_waitcnt vmcnt(10)
v_dot2c_f32_bf16 v252, v12, v20
v_dot2c_f32_bf16 v253, v12, v12
v_dot2c_f32_bf16 v250, v13, v20
v_dot2c_f32_bf16 v251, v13, v13
v_dot2c_f32_bf16 v252, v14, v20
v_dot2c_f32_bf16 v253, v14, v14
v_dot2c_f32_bf16 v250, v15, v20
v_dot2c_f32_bf16 v251, v15, v15
s_waitcnt vmcnt(9)
v_dot2c_f32_bf16 v252, v16, v20
v_dot2c_f32_bf16 v253, v16, v16
v_dot2c_f32_bf16 v250, v17, v20
v_dot2c_f32_bf16 v251, v17, v17
v_dot2c_f32_bf16 v252, v18, v20
v_dot2c_f32_bf16 v253, v18, v18
v_dot2c_f32_bf16 v250, v19, v20
v_dot2c_f32_bf16 v251, v19, v19
v_cmp_gt_u32 vcc, 2, v246
v_lshlrev_b32 v241, 3, v246
v_lshl_add_u32 v241, v245, 4, v241
v_add_f32 v252, v252, v250
v_add_f32 v253, v253, v251
v_mov_b32 v250, 0x80000000
s_lshl_b32 s71, s57, 6
v_add_f32_dpp v252, v252, v252 row_ror:8 row_mask:0xf bank_mask:0xf
v_add_f32_dpp v253, v253, v253 row_ror:8 row_mask:0xf bank_mask:0xf
s_nop 0
v_add_f32_dpp v252, v252, v252 row_ror:4 row_mask:0xf bank_mask:0xf
v_add_f32_dpp v253, v253, v253 row_ror:4 row_mask:0xf bank_mask:0xf
s_nop 0
v_add_f32_dpp v252, v252, v252 row_ror:2 row_mask:0xf bank_mask:0xf
v_add_f32_dpp v253, v253, v253 row_ror:2 row_mask:0xf bank_mask:0xf
s_nop 0
v_add_f32_dpp v252, v252, v252 row_ror:1 row_mask:0xf bank_mask:0xf
v_add_f32_dpp v253, v253, v253 row_ror:1 row_mask:0xf bank_mask:0xf
v_cndmask_b32 v241, v250, v241, vcc
s_add_u32 s71, s71, 0x40000
v_permlane16_swap_b32 v252, v253
v_add_f32 v252, v252, v253
v_mov_b32 v253, s70
buffer_store_dwordx2 v[252:253], v241, s[44:47], s71 offen sc1
s_add_u32 m0, s89, 0x3000
s_lshr_b32 s77, s83, 5
s_add_u32 s77, s77, s87
buffer_load_dwordx4 v239, s[20:23], s77 offen lds
s_mov_b64 s[24:25], s[44:45]
s_cmp_eq_u32 s63, 3
s_cselect_b32 s26, 0xf6a000, 0
s_cselect_b32 s10, s70, 0
v_lshlrev_b32 v241, 6, v249
v_lshlrev_b32 v240, 5, v248
v_lshl_add_u32 v240, v247, 4, v240
s_lshl_b32 s71, s64, 2
s_add_u32 s71, s71, s63
s_lshl_b32 s71, s71, 10
s_add_u32 s57, s71, 0x40000
s_add_u32 s77, s77, 0x1800
buffer_load_dwordx4 v238, s[20:23], s77 offen offset:1024 lds
s_add_u32 s77, s77, 0x1800
buffer_load_dwordx4 v239, s[20:23], s77 offen offset:2048 lds
s_add_u32 s77, s77, 0x1800
buffer_load_dwordx4 v238, s[20:23], s77 offen offset:3072 lds
s_add_u32 m0, s89, 0x4000
s_lshr_b32 s77, s84, 5
s_add_u32 s77, s77, s87
buffer_load_dwordx4 v239, s[20:23], s77 offen lds
s_add_u32 s77, s77, 0x1800
buffer_load_dwordx4 v238, s[20:23], s77 offen offset:1024 lds
s_add_u32 s77, s77, 0x1800
buffer_load_dwordx4 v239, s[20:23], s77 offen offset:2048 lds
s_add_u32 s77, s77, 0x1800
buffer_load_dwordx4 v238, s[20:23], s77 offen offset:3072 lds
s_add_u32 m0, s89, 0x5000
s_lshr_b32 s77, s85, 5
s_add_u32 s77, s77, s87
buffer_load_dwordx4 v239, s[20:23], s77 offen lds
s_add_u32 s77, s77, 0x1800
buffer_load_dwordx4 v238, s[20:23], s77 offen offset:1024 lds
s_add_u32 s77, s77, 0x1800
buffer_load_dwordx4 v239, s[20:23], s77 offen offset:2048 lds
s_add_u32 s77, s77, 0x1800
buffer_load_dwordx4 v238, s[20:23], s77 offen offset:3072 lds
s_add_u32 m0, s89, 0x6000
s_lshr_b32 s77, s86, 5
s_add_u32 s77, s77, s87
buffer_load_dwordx4 v239, s[20:23], s77 offen lds
s_add_u32 s77, s77, 0x1800
buffer_load_dwordx4 v238, s[20:23], s77 offen offset:1024 lds
s_add_u32 s77, s77, 0x1800
buffer_load_dwordx4 v239, s[20:23], s77 offen offset:2048 lds
s_add_u32 s77, s77, 0x1800
buffer_load_dwordx4 v238, s[20:23], s77 offen offset:3072 lds
ds_read_b128 v[4:7], v234
ds_read_b128 v[8:11], v233
ds_read_b128 v[28:31], v232
ds_read_b128 v[32:35], v231
s_waitcnt vmcnt(22)
ds_read_b128 v[36:39], v234 offset:4096
ds_read_b128 v[40:43], v233 offset:4096
ds_read_b128 v[44:47], v232 offset:4096
ds_read_b128 v[48:51], v231 offset:4096
s_waitcnt vmcnt(18)
ds_read_b128 v[52:55], v234 offset:8192
ds_read_b128 v[56:59], v233 offset:8192
ds_read_b128 v[60:63], v232 offset:8192
ds_read_b128 v[64:67], v231 offset:8192
s_waitcnt vmcnt(12)
ds_read_b128 v[68:71], v234 offset:12288
ds_read_b128 v[72:75], v233 offset:12288
ds_read_b128 v[76:79], v232 offset:12288
ds_read_b128 v[80:83], v231 offset:12288
s_waitcnt vmcnt(8)
ds_read_b128 v[84:87], v234 offset:16384
ds_read_b128 v[88:91], v233 offset:16384
ds_read_b128 v[92:95], v232 offset:16384
ds_read_b128 v[96:99], v231 offset:16384
s_waitcnt vmcnt(4)
ds_read_b128 v[100:103], v234 offset:20480
ds_read_b128 v[104:107], v233 offset:20480
ds_read_b128 v[108:111], v232 offset:20480
ds_read_b128 v[112:115], v231 offset:20480
s_lshl_b32 s71, s65, 12
s_add_u32 s71, s71, 0x24000
buffer_load_dword v235, v241, s[24:27], s71 offen sc1
s_waitcnt vmcnt(1)
ds_read_b128 v[116:119], v234 offset:24576
ds_read_b128 v[120:123], v233 offset:24576
ds_read_b128 v[124:127], v232 offset:24576
ds_read_b128 v[128:131], v231 offset:24576
s_waitcnt lgkmcnt(0)
s_waitcnt vmcnt(0)
v_cmp_ne_u32 vcc, s10, v235
s_cbranch_vccnz POLLA
POLLBACK:
s_barrier
s_add_u32 s7, s4, s88
s_add_u32 m0, s88, 0
s_add_u32 s71, s7, s80
buffer_load_dwordx4 v1, s[44:47], s71 offen sc1 lds
s_add_u32 m0, s88, 0x1000
s_add_u32 s71, s7, s81
buffer_load_dwordx4 v1, s[44:47], s71 offen sc1 lds
v_mul_u32_u24 v252, 0x380, v247
v_lshl_add_u32 v252, v248, 2, v252
s_lshl_b32 s72, s63, 11
s_add_u32 s72, s72, s4
s_add_u32 s72, s72, 0x15800
s_cmp_lt_u32 s63, 3
s_cselect_b32 s54, 0xf6a000, 0
buffer_load_dword v132, v252, s[52:55], s72 offen sc1
buffer_load_dword v133, v252, s[52:55], s72 offen offset:128 sc1
buffer_load_dword v134, v252, s[52:55], s72 offen offset:256 sc1
buffer_load_dword v135, v252, s[52:55], s72 offen offset:384 sc1
buffer_load_dword v136, v252, s[52:55], s72 offen offset:512 sc1
buffer_load_dword v137, v252, s[52:55], s72 offen offset:640 sc1
buffer_load_dword v138, v252, s[52:55], s72 offen offset:768 sc1
buffer_load_dwordx2 v[12:13], v240, s[44:47], s57 offen sc1
s_add_u32 m0, s88, 0x2000
s_add_u32 s71, s7, s82
buffer_load_dwordx4 v1, s[44:47], s71 offen sc1 lds
v_add_u32 v254, 0xe800, v1
s_mov_b32 s54, s16
s_mul_i32 s97, s63, 0x3800
s_mov_b32 s75, 0
s_mul_i32 s76, s63, 7
s_add_u32 s73, s75, s76
s_add_u32 m0, s88, 0x3000
s_add_u32 s71, s7, s83
buffer_load_dwordx4 v1, s[44:47], s71 offen sc1 lds
s_lshr_b32 s74, s73, 1
s_mul_i32 s74, s74, 0x15000
s_mul_i32 s73, s73, 0x3800
s_add_u32 s73, s73, s74
s_add_u32 s8, s73, s17
s_add_u32 s75, s75, 1
s_cmp_eq_u32 s75, 7
s_cselect_b32 s75, 0, s75
s_add_u32 m0, s88, 0x4000
s_add_u32 s71, s7, s84
buffer_load_dwordx4 v1, s[44:47], s71 offen sc1 lds
s_add_u32 s73, s75, s76
s_lshr_b32 s74, s73, 1
s_mul_i32 s74, s74, 0x15000
s_mul_i32 s73, s73, 0x3800
s_add_u32 s73, s73, s74
s_add_u32 s56, s73, s17
s_add_u32 m0, s88, 0x5000
s_add_u32 s71, s7, s85
buffer_load_dwordx4 v1, s[44:47], s71 offen sc1 lds
s_add_u32 s75, s75, 1
s_cmp_eq_u32 s75, 7
s_cselect_b32 s75, 0, s75
s_add_u32 s73, s75, s76
s_lshr_b32 s74, s73, 1
s_mul_i32 s74, s74, 0x15000
s_mul_i32 s73, s73, 0x3800
s_add_u32 s73, s73, s74
s_add_u32 m0, s88, 0x6000
s_add_u32 s71, s7, s86
buffer_load_dwordx4 v1, s[44:47], s71 offen sc1 lds
s_add_u32 s7, s7, 0x7800
s_add_u32 s69, s73, s17
s_add_u32 s75, s75, 1
s_cmp_eq_u32 s75, 7
s_cselect_b32 s75, 0, s75
s_add_u32 s73, s75, s76
s_lshr_b32 s74, s73, 1
s_mul_i32 s74, s74, 0x15000
s_mul_i32 s73, s73, 0x3800
s_and_b32 s72, s88, 0x400
s_add_u32 m0, s72, 0x7000
s_add_u32 s72, s72, s4
s_add_u32 s72, s72, 0x7000
buffer_load_dwordx4 v1, s[44:47], s72 offen sc1 lds
s_add_u32 s73, s73, s74
s_add_u32 s79, s73, s17
s_add_u32 s75, s75, 1
s_cmp_eq_u32 s75, 7
s_cselect_b32 s75, 0, s75
s_add_u32 s73, s75, s76
s_lshr_b32 s74, s73, 1
s_mul_i32 s74, s74, 0x15000
s_add_u32 m0, s88, 0x7800
s_add_u32 s71, s7, s80
buffer_load_dwordx4 v1, s[44:47], s71 offen sc1 lds
s_mul_i32 s73, s73, 0x3800
s_add_u32 s73, s73, s74
s_add_u32 s87, s73, s17
s_add_u32 s75, s75, 1
s_cmp_eq_u32 s75, 7
s_cselect_b32 s75, 0, s75
s_add_u32 s73, s75, s76
s_lshr_b32 s74, s73, 1
s_add_u32 m0, s88, 0x8800
s_add_u32 s71, s7, s81
buffer_load_dwordx4 v1, s[44:47], s71 offen sc1 lds
s_mul_i32 s74, s74, 0x15000
s_mul_i32 s73, s73, 0x3800
s_add_u32 s73, s73, s74
s_add_u32 s89, s73, s17
s_add_u32 s75, s75, 1
s_cmp_eq_u32 s75, 7
s_cselect_b32 s75, 0, s75
s_add_u32 s73, s75, s76
s_add_u32 m0, s88, 0x9800
s_add_u32 s71, s7, s82
buffer_load_dwordx4 v1, s[44:47], s71 offen sc1 lds
s_lshr_b32 s74, s73, 1
s_mul_i32 s74, s74, 0x15000
s_mul_i32 s73, s73, 0x3800
s_add_u32 s73, s73, s74
s_add_u32 s96, s73, s17
v_lshl_add_u32 v252, v247, 2, v244
v_and_b32 v253, 2, v252
v_bfe_u32 v250, v252, 2, 1
v_or_b32 v253, v253, v250
v_bfe_u32 v250, v249, 4, 1
v_xor_b32 v253, v253, v250
v_lshlrev_b32 v253, 5, v253
v_lshl_or_b32 v252, v252, 7, v243
s_and_b32 s71, s63, 1
s_lshl_b32 s71, s71, 6
v_or_b32 v252, v252, v253
v_xor_b32 v252, s71, v252
s_add_u32 s71, s97, 0x17000
v_add_u32 v242, s71, v252
v_xor_b32 v252, 64, v252
v_add_u32 v241, s71, v252
s_add_u32 m0, s88, 0xa800
s_add_u32 s71, s7, s83
buffer_load_dwordx4 v1, s[44:47], s71 offen sc1 lds
s_waitcnt vmcnt(18)
s_barrier
ds_read_b128 a[224:227], v1
ds_read_b128 a[228:231], v1 offset:1024
ds_read_b128 a[232:235], v1 offset:2048
ds_read_b128 a[236:239], v1 offset:3072
ds_read_b128 a[240:243], v1 offset:4096
ds_read_b128 a[244:247], v1 offset:5120
s_waitcnt lgkmcnt(4)
v_mfma_f32_32x32x16_bf16 v[144:159], a[224:227], v[4:7], 0
s_waitcnt vmcnt(16)
v_add_f32 v132, v132, v133
s_waitcnt vmcnt(14)
v_add_f32 v134, v134, v135
s_waitcnt vmcnt(12)
v_add_f32 v136, v136, v137
ds_read_b128 a[248:251], v1 offset:6144
v_mfma_f32_32x32x16_bf16 v[144:159], a[228:231], v[8:11], v[144:159]
s_add_u32 m0, s88, 0xb800
s_add_u32 s71, s7, s84
buffer_load_dwordx4 v1, s[44:47], s71 offen sc1 lds
s_waitcnt vmcnt(12)
v_add_f32 v132, v132, v138
v_add_f32 v134, v134, v136
v_add_f32 v250, v132, v134
ds_read_b128 a[252:255], v1 offset:7168
s_waitcnt lgkmcnt(4)
v_mfma_f32_32x32x16_bf16 v[144:159], a[232:235], v[28:31], v[144:159]
v_mov_b32 v137, 0
v_mov_b32 v251, v250
v_mov_b32 v138, 0
s_waitcnt vmcnt(8)
s_barrier
ds_read_b128 a[224:227], v1 offset:8192
v_mfma_f32_32x32x16_bf16 v[144:159], a[236:239], v[32:35], v[144:159]
s_add_u32 m0, s88, 0xc800
s_add_u32 s71, s7, s85
buffer_load_dwordx4 v1, s[44:47], s71 offen sc1 lds
v_mov_b32 v139, 0
v_permlane32_swap_b32 v250, v251
v_add_f32 v250, v250, v251
ds_read_b128 a[228:231], v1 offset:9216
s_waitcnt lgkmcnt(4)
v_mfma_f32_32x32x16_bf16 v[144:159], a[240:243], v[36:39], v[144:159]
v_cvt_pk_bf16_f32 v136, v250, 0
v_lshlrev_b32 v251, 16, v136
v_sub_f32 v251, v250, v251
)ASM"
R"ASM(
ds_read_b128 a[232:235], v1 offset:10240
v_mfma_f32_32x32x16_bf16 v[144:159], a[244:247], v[40:43], v[144:159]
s_add_u32 m0, s88, 0xd800
s_add_u32 s71, s7, s86
buffer_load_dwordx4 v1, s[44:47], s71 offen sc1 lds
s_add_u32 s7, s7, 0x7000
v_cvt_pk_bf16_f32 v251, v251, 0
v_and_b32 v136, 0xffff, v136
v_lshl_or_b32 v136, v251, 16, v136
ds_read_b128 a[236:239], v1 offset:11264
s_waitcnt lgkmcnt(4)
v_mfma_f32_32x32x16_bf16 v[144:159], a[248:251], v[44:47], v[144:159]
v_add_u32 v250, s88, v254
v_cndmask_b32 v136, v136, v137, s[58:59]
ds_write_b128 v250, v[136:139] offset:30720
ds_read_b128 a[240:243], v1 offset:12288
v_mfma_f32_32x32x16_bf16 v[144:159], a[252:255], v[48:51], v[144:159]
s_add_u32 m0, s88, 0xe800
s_add_u32 s71, s7, s80
buffer_load_dwordx4 v1, s[44:47], s71 offen sc1 lds
v_cmp_ne_u32 vcc, s70, v13
s_cbranch_vccnz POLLR
POLLRB:
ds_read_b128 a[244:247], v1 offset:13312
s_waitcnt lgkmcnt(5)
v_mfma_f32_32x32x16_bf16 v[144:159], a[224:227], v[52:55], v[144:159]
v_mul_f32 v252, 0x3b124925, v12
v_mov_b32 v253, v252
s_nop 1
v_permlane32_swap_b32 v252, v253
ds_read_b128 a[248:251], v1 offset:14336
v_mfma_f32_32x32x16_bf16 v[144:159], a[228:231], v[56:59], v[144:159]
buffer_store_dwordx4 v[24:27], v230, s[44:47], s11 offen sc1
v_cvt_pk_bf16_f32 v140, -v252, -v252
v_cvt_pk_bf16_f32 v141, -v252, -v252
v_cvt_pk_bf16_f32 v142, -v252, -v252
ds_read_b128 a[252:255], v1 offset:15360
s_waitcnt lgkmcnt(5)
v_mfma_f32_32x32x16_bf16 v[144:159], a[232:235], v[60:63], v[144:159]
v_cvt_pk_bf16_f32 v143, -v252, -v252
v_fma_f32 v253, -v252, v252, v253
v_max_f32 v253, 0, v253
ds_read_b128 a[224:227], v1 offset:16384
v_mfma_f32_32x32x16_bf16 v[144:159], a[236:239], v[64:67], v[144:159]
s_add_u32 m0, s88, 0xf800
s_add_u32 s71, s7, s81
buffer_load_dwordx4 v1, s[44:47], s71 offen sc1 lds
v_add_f32 v253, 0x3727c5ac, v253
v_rsq_f32 v239, v253
s_nop 0
v_mul_f32 v238, 0x3fb8aa3b, v239
ds_read_b128 a[228:231], v1 offset:17408
s_waitcnt lgkmcnt(4)
v_mfma_f32_32x32x16_bf16 v[144:159], a[240:243], v[68:71], v[144:159]
ds_read_b128 a[232:235], v1 offset:18432
v_mfma_f32_32x32x16_bf16 v[144:159], a[244:247], v[72:75], v[144:159]
buffer_store_dwordx4 v[24:27], v230, s[44:47], s11 offen offset:512 sc1
ds_read_b128 a[236:239], v1 offset:19456
s_waitcnt lgkmcnt(4)
v_mfma_f32_32x32x16_bf16 v[144:159], a[248:251], v[76:79], v[144:159]
s_waitcnt vmcnt(11)
s_waitcnt lgkmcnt(0)
s_barrier
ds_read_b128 a[240:243], v1 offset:20480
v_mfma_f32_32x32x16_bf16 v[144:159], a[252:255], v[80:83], v[144:159]
s_add_u32 m0, s88, 0x10800
s_add_u32 s71, s7, s82
buffer_load_dwordx4 v1, s[44:47], s71 offen sc1 lds
ds_read_b128 a[244:247], v1 offset:21504
v_mfma_f32_32x32x16_bf16 v[144:159], a[224:227], v[84:87], v[144:159]
ds_read_b128 a[248:251], v1 offset:22528
v_mfma_f32_32x32x16_bf16 v[144:159], a[228:231], v[88:91], v[144:159]
buffer_store_dwordx4 v[24:27], v229, s[48:51], s12 offen sc1
ds_read_b128 a[252:255], v1 offset:23552
v_mfma_f32_32x32x16_bf16 v[144:159], a[232:235], v[92:95], v[144:159]
ds_read_b128 v[176:179], v1 offset:28672
ds_read_b128 v[180:183], v1 offset:29696
ds_read_b128 a[224:227], v1 offset:24576
v_mfma_f32_32x32x16_bf16 v[144:159], a[236:239], v[96:99], v[144:159]
s_add_u32 m0, s88, 0x11800
s_add_u32 s71, s7, s83
buffer_load_dwordx4 v1, s[44:47], s71 offen sc1 lds
ds_read_b128 a[228:231], v1 offset:25600
s_waitcnt lgkmcnt(6)
v_mfma_f32_32x32x16_bf16 v[144:159], a[240:243], v[100:103], v[144:159]
ds_read_b128 a[232:235], v1 offset:26624
v_mfma_f32_32x32x16_bf16 v[144:159], a[244:247], v[104:107], v[144:159]
buffer_store_dwordx4 v[24:27], v229, s[48:51], s12 offen offset:2048 sc1
buffer_store_dword v24, v228, s[48:51], s14 offen sc1
buffer_store_dword v24, v237, s[52:55], s15 offen sc1
ds_read_b128 a[236:239], v1 offset:27648
s_waitcnt lgkmcnt(6)
v_mfma_f32_32x32x16_bf16 v[144:159], a[248:251], v[108:111], v[144:159]
ds_read_b128 a[240:243], v254 offset:30720
v_mfma_f32_32x32x16_bf16 v[144:159], a[252:255], v[112:115], v[144:159]
s_add_u32 m0, s88, 0x12800
s_add_u32 s71, s7, s84
buffer_load_dwordx4 v1, s[44:47], s71 offen sc1 lds
s_waitcnt lgkmcnt(3)
v_mfma_f32_32x32x16_bf16 v[144:159], a[224:227], v[116:119], v[144:159]
v_mfma_f32_32x32x16_bf16 v[144:159], a[228:231], v[120:123], v[144:159]
s_add_u32 m0, s88, 0x13800
s_add_u32 s71, s7, s85
buffer_load_dwordx4 v1, s[44:47], s71 offen sc1 lds
s_waitcnt lgkmcnt(2)
v_mfma_f32_32x32x16_bf16 v[144:159], a[232:235], v[124:127], v[144:159]
s_waitcnt lgkmcnt(1)
v_mfma_f32_32x32x16_bf16 v[144:159], a[236:239], v[128:131], v[144:159]
s_add_u32 m0, s88, 0x14800
s_add_u32 s71, s7, s86
buffer_load_dwordx4 v1, s[44:47], s71 offen sc1 lds
s_add_u32 s7, s7, 0x7000
s_waitcnt lgkmcnt(0)
v_mfma_f32_32x32x16_bf16 v[144:159], a[240:243], v[140:143], v[144:159]
s_nop 11
v_cvt_pk_bf16_f32 v184, v144, v145
v_cvt_pk_bf16_f32 v185, v146, v147
v_cvt_pk_bf16_f32 v186, v148, v149
v_cvt_pk_bf16_f32 v187, v150, v151
v_cvt_pk_bf16_f32 v188, v152, v153
v_cvt_pk_bf16_f32 v189, v154, v155
v_cvt_pk_bf16_f32 v190, v156, v157
v_cvt_pk_bf16_f32 v191, v158, v159
v_mfma_f32_32x32x16_bf16 v[144:159], v[176:179], v[184:187], 0
s_nop 0
v_mfma_f32_32x32x16_bf16 v[144:159], v[180:183], v[188:191], v[144:159]
s_lshl_b32 s78, s63, 11
s_add_u32 m0, s78, 0x26000
s_nop 0
buffer_load_dwordx4 v219, s[32:35], s19 offen lds
s_nop 7
v_max3_f32 v232, v144, v145, v146
v_max3_f32 v212, -v144, -v145, -v146
v_max3_f32 v232, v232, v147, v148
v_max3_f32 v212, v212, -v147, -v148
v_max3_f32 v232, v232, v149, v150
v_max3_f32 v212, v212, -v149, -v150
v_max3_f32 v232, v232, v151, v152
v_max3_f32 v212, v212, -v151, -v152
v_max3_f32 v232, v232, v153, v154
v_max3_f32 v212, v212, -v153, -v154
v_max3_f32 v232, v232, v155, v156
v_max3_f32 v212, v212, -v155, -v156
v_max3_f32 v232, v232, v157, v158
v_max3_f32 v212, v212, -v157, -v158
v_max_f32 v232, v232, v159
v_max_f32 v212, v212, -v159
v_mov_b32 v250, 0xff800000
v_mov_b32 v251, 0xff800000
v_permlane32_swap_b32 v232, v212
v_max_f32 v232, v232, v212
v_mov_b32 v212, v232
s_nop 1
v_permlane32_swap_b32 v232, v212
v_mul_f32 v252, -v232, v238
v_mul_f32 v253, -v212, v238
v_cndmask_b32 v250, v252, v250, s[58:59]
v_cndmask_b32 v251, v253, v251, s[58:59]
v_pk_fma_f32 v[192:193], v[144:145], v[238:239], v[252:253] op_sel:[0,0,1] op_sel_hi:[1,0,1] neg_lo:[1,0,0] neg_hi:[1,0,0]
v_pk_fma_f32 v[144:145], v[144:145], v[238:239], v[252:253] op_sel_hi:[1,0,0]
v_pk_fma_f32 v[194:195], v[146:147], v[238:239], v[252:253] op_sel:[0,0,1] op_sel_hi:[1,0,1] neg_lo:[1,0,0] neg_hi:[1,0,0]
v_pk_fma_f32 v[146:147], v[146:147], v[238:239], v[252:253] op_sel_hi:[1,0,0]
s_add_u32 s78, s13, 0
buffer_store_dwordx4 v[24:27], v1, s[48:51], s78 offen sc1
v_exp_f32 v192, v192
v_exp_f32 v144, v144
v_exp_f32 v193, v193
v_exp_f32 v145, v145
v_pk_fma_f32 v[196:197], v[148:149], v[238:239], v[252:253] op_sel:[0,0,1] op_sel_hi:[1,0,1] neg_lo:[1,0,0] neg_hi:[1,0,0]
v_pk_fma_f32 v[148:149], v[148:149], v[238:239], v[252:253] op_sel_hi:[1,0,0]
v_exp_f32 v194, v194
v_exp_f32 v146, v146
v_exp_f32 v195, v195
v_exp_f32 v147, v147
v_pk_fma_f32 v[198:199], v[150:151], v[238:239], v[252:253] op_sel:[0,0,1] op_sel_hi:[1,0,1] neg_lo:[1,0,0] neg_hi:[1,0,0]
v_pk_fma_f32 v[150:151], v[150:151], v[238:239], v[252:253] op_sel_hi:[1,0,0]
v_exp_f32 v196, v196
v_exp_f32 v148, v148
v_exp_f32 v197, v197
v_exp_f32 v149, v149
v_pk_fma_f32 v[200:201], v[152:153], v[238:239], v[252:253] op_sel:[0,0,1] op_sel_hi:[1,0,1] neg_lo:[1,0,0] neg_hi:[1,0,0]
v_pk_fma_f32 v[152:153], v[152:153], v[238:239], v[252:253] op_sel_hi:[1,0,0]
v_exp_f32 v198, v198
v_exp_f32 v150, v150
v_exp_f32 v199, v199
v_exp_f32 v151, v151
v_pk_fma_f32 v[202:203], v[154:155], v[238:239], v[252:253] op_sel:[0,0,1] op_sel_hi:[1,0,1] neg_lo:[1,0,0] neg_hi:[1,0,0]
v_pk_fma_f32 v[154:155], v[154:155], v[238:239], v[252:253] op_sel_hi:[1,0,0]
v_exp_f32 v200, v200
v_exp_f32 v152, v152
v_exp_f32 v201, v201
v_exp_f32 v153, v153
v_pk_fma_f32 v[204:205], v[156:157], v[238:239], v[250:251] op_sel:[0,0,1] op_sel_hi:[1,0,1] neg_lo:[1,0,0] neg_hi:[1,0,0]
v_pk_fma_f32 v[156:157], v[156:157], v[238:239], v[250:251] op_sel_hi:[1,0,0]
v_exp_f32 v202, v202
v_exp_f32 v154, v154
v_exp_f32 v203, v203
v_exp_f32 v155, v155
v_pk_fma_f32 v[206:207], v[158:159], v[238:239], v[250:251] op_sel:[0,0,1] op_sel_hi:[1,0,1] neg_lo:[1,0,0] neg_hi:[1,0,0]
v_pk_fma_f32 v[158:159], v[158:159], v[238:239], v[250:251] op_sel_hi:[1,0,0]
v_exp_f32 v204, v204
v_exp_f32 v156, v156
v_exp_f32 v205, v205
v_exp_f32 v157, v157
s_add_u32 s78, s13, 0
buffer_store_dwordx4 v[24:27], v1, s[48:51], s78 offen offset:1024 sc1
v_exp_f32 v206, v206
v_exp_f32 v158, v158
v_exp_f32 v207, v207
v_exp_f32 v159, v159
v_pk_add_f32 v[232:233], v[144:145], v[146:147]
v_pk_add_f32 v[212:213], v[192:193], v[194:195]
v_pk_add_f32 v[232:233], v[232:233], v[148:149]
v_pk_add_f32 v[212:213], v[212:213], v[196:197]
v_pk_add_f32 v[232:233], v[232:233], v[150:151]
v_pk_add_f32 v[212:213], v[212:213], v[198:199]
v_pk_add_f32 v[232:233], v[232:233], v[152:153]
v_pk_add_f32 v[212:213], v[212:213], v[200:201]
v_pk_add_f32 v[232:233], v[232:233], v[154:155]
v_pk_add_f32 v[212:213], v[212:213], v[202:203]
v_pk_add_f32 v[232:233], v[232:233], v[156:157]
v_pk_add_f32 v[212:213], v[212:213], v[204:205]
v_pk_add_f32 v[232:233], v[232:233], v[158:159]
v_pk_add_f32 v[212:213], v[212:213], v[206:207]
v_add_f32 v232, v232, v233
v_add_f32 v212, v212, v213
s_nop 1
v_permlane32_swap_b32 v232, v212
v_add_f32 v232, v232, v212
v_mov_b32 v212, v232
s_nop 1
v_permlane32_swap_b32 v232, v212
v_rcp_f32 v232, v232
v_rcp_f32 v212, v212
v_pk_mul_f32 v[144:145], v[144:145], v[232:233] op_sel_hi:[1,0]
v_pk_mul_f32 v[192:193], v[192:193], v[212:213] op_sel_hi:[1,0]
v_pk_mul_f32 v[146:147], v[146:147], v[232:233] op_sel_hi:[1,0]
v_pk_mul_f32 v[194:195], v[194:195], v[212:213] op_sel_hi:[1,0]
v_pk_mul_f32 v[148:149], v[148:149], v[232:233] op_sel_hi:[1,0]
v_pk_mul_f32 v[196:197], v[196:197], v[212:213] op_sel_hi:[1,0]
v_pk_mul_f32 v[150:151], v[150:151], v[232:233] op_sel_hi:[1,0]
v_pk_mul_f32 v[198:199], v[198:199], v[212:213] op_sel_hi:[1,0]
v_pk_mul_f32 v[152:153], v[152:153], v[232:233] op_sel_hi:[1,0]
v_pk_mul_f32 v[200:201], v[200:201], v[212:213] op_sel_hi:[1,0]
v_pk_mul_f32 v[154:155], v[154:155], v[232:233] op_sel_hi:[1,0]
v_pk_mul_f32 v[202:203], v[202:203], v[212:213] op_sel_hi:[1,0]
v_pk_mul_f32 v[156:157], v[156:157], v[232:233] op_sel_hi:[1,0]
v_pk_mul_f32 v[204:205], v[204:205], v[212:213] op_sel_hi:[1,0]
s_lshl_b32 s78, s63, 11
s_add_u32 m0, s78, 0x26000
s_nop 0
buffer_load_dwordx4 v219, s[32:35], s90 offen offset:1024 lds
v_pk_mul_f32 v[158:159], v[158:159], v[232:233] op_sel_hi:[1,0]
v_pk_mul_f32 v[206:207], v[206:207], v[212:213] op_sel_hi:[1,0]
v_cvt_pk_bf16_f32 v144, v144, v145
v_cvt_pk_bf16_f32 v192, v192, v193
v_cvt_pk_bf16_f32 v145, v146, v147
v_cvt_pk_bf16_f32 v193, v194, v195
v_cvt_pk_bf16_f32 v146, v148, v149
v_cvt_pk_bf16_f32 v194, v196, v197
v_cvt_pk_bf16_f32 v147, v150, v151
v_cvt_pk_bf16_f32 v195, v198, v199
v_cvt_pk_bf16_f32 v148, v152, v153
v_cvt_pk_bf16_f32 v196, v200, v201
v_cvt_pk_bf16_f32 v149, v154, v155
v_cvt_pk_bf16_f32 v197, v202, v203
v_cvt_pk_bf16_f32 v150, v156, v157
v_cvt_pk_bf16_f32 v198, v204, v205
v_cvt_pk_bf16_f32 v151, v158, v159
v_cvt_pk_bf16_f32 v199, v206, v207
s_waitcnt vmcnt(17)
s_waitcnt lgkmcnt(0)
s_barrier
ds_read_b128 a[224:227], v1 offset:30720
ds_read_b128 a[228:231], v1 offset:31744
ds_read_b128 a[232:235], v1 offset:32768
ds_read_b128 a[236:239], v1 offset:33792
ds_read_b128 a[240:243], v1 offset:34816
ds_read_b128 a[244:247], v1 offset:35840
s_waitcnt lgkmcnt(4)
v_mfma_f32_32x32x16_bf16 v[160:175], a[224:227], v[4:7], 0
ds_write_b64 v226, v[144:145]
ds_read_b128 a[248:251], v1 offset:36864
v_mfma_f32_32x32x16_bf16 v[160:175], a[228:231], v[8:11], v[160:175]
s_add_u32 s78, s13, 0x7000
buffer_store_dwordx4 v[24:27], v1, s[48:51], s78 offen sc1
ds_write_b64 v226, v[192:193] offset:8192
ds_read_b128 a[252:255], v1 offset:37888
s_waitcnt lgkmcnt(6)
v_mfma_f32_32x32x16_bf16 v[160:175], a[232:235], v[28:31], v[160:175]
ds_write_b64 v225, v[146:147]
ds_read_b128 a[224:227], v1 offset:38912
v_mfma_f32_32x32x16_bf16 v[160:175], a[236:239], v[32:35], v[160:175]
ds_write_b64 v225, v[194:195] offset:8192
ds_read_b128 a[228:231], v1 offset:39936
s_waitcnt lgkmcnt(8)
v_mfma_f32_32x32x16_bf16 v[160:175], a[240:243], v[36:39], v[160:175]
ds_write_b64 v224, v[148:149]
ds_read_b128 a[232:235], v1 offset:40960
v_mfma_f32_32x32x16_bf16 v[160:175], a[244:247], v[40:43], v[160:175]
ds_write_b64 v224, v[196:197] offset:8192
ds_read_b128 a[236:239], v1 offset:41984
s_waitcnt lgkmcnt(9)
v_mfma_f32_32x32x16_bf16 v[160:175], a[248:251], v[44:47], v[160:175]
ds_write_b64 v223, v[150:151]
ds_read_b128 a[240:243], v1 offset:43008
s_waitcnt lgkmcnt(9)
v_mfma_f32_32x32x16_bf16 v[160:175], a[252:255], v[48:51], v[160:175]
s_add_u32 s78, s13, 0x7000
buffer_store_dwordx4 v[24:27], v1, s[48:51], s78 offen offset:1024 sc1
ds_write_b64 v223, v[198:199] offset:8192
ds_read_b128 a[244:247], v1 offset:44032
s_waitcnt lgkmcnt(9)
v_mfma_f32_32x32x16_bf16 v[160:175], a[224:227], v[52:55], v[160:175]
ds_read_b128 a[248:251], v1 offset:45056
s_waitcnt lgkmcnt(8)
v_mfma_f32_32x32x16_bf16 v[160:175], a[228:231], v[56:59], v[160:175]
v_min_u32 v217, 6, v249
v_lshlrev_b32 v217, 7, v217
buffer_load_dword v216, v218, s[28:31], s95 offen
ds_read_b128 a[252:255], v1 offset:46080
s_waitcnt lgkmcnt(7)
v_mfma_f32_32x32x16_bf16 v[160:175], a[232:235], v[60:63], v[160:175]
ds_read_b128 a[224:227], v1 offset:47104
s_waitcnt lgkmcnt(6)
v_mfma_f32_32x32x16_bf16 v[160:175], a[236:239], v[64:67], v[160:175]
ds_read_b128 a[228:231], v1 offset:48128
s_waitcnt lgkmcnt(5)
v_mfma_f32_32x32x16_bf16 v[160:175], a[240:243], v[68:71], v[160:175]
ds_read_b128 a[232:235], v1 offset:49152
s_waitcnt lgkmcnt(4)
v_mfma_f32_32x32x16_bf16 v[160:175], a[244:247], v[72:75], v[160:175]
ds_read_b128 a[236:239], v1 offset:50176
v_mfma_f32_32x32x16_bf16 v[160:175], a[248:251], v[76:79], v[160:175]
ds_read_b128 a[240:243], v1 offset:51200
s_waitcnt lgkmcnt(4)
v_mfma_f32_32x32x16_bf16 v[160:175], a[252:255], v[80:83], v[160:175]
s_add_u32 s78, s13, 0xe000
buffer_store_dwordx4 v[24:27], v1, s[48:51], s78 offen sc1
ds_read_b128 a[244:247], v1 offset:52224
v_mfma_f32_32x32x16_bf16 v[160:175], a[224:227], v[84:87], v[160:175]
ds_read_b128 a[248:251], v1 offset:53248
s_waitcnt lgkmcnt(4)
v_mfma_f32_32x32x16_bf16 v[160:175], a[228:231], v[88:91], v[160:175]
ds_read_b128 a[252:255], v1 offset:54272
v_mfma_f32_32x32x16_bf16 v[160:175], a[232:235], v[92:95], v[160:175]
ds_read_b128 a[224:227], v1 offset:55296
s_waitcnt lgkmcnt(4)
v_mfma_f32_32x32x16_bf16 v[160:175], a[236:239], v[96:99], v[160:175]
ds_read_b128 a[228:231], v1 offset:56320
v_mfma_f32_32x32x16_bf16 v[160:175], a[240:243], v[100:103], v[160:175]
ds_read_b128 a[232:235], v1 offset:57344
s_waitcnt lgkmcnt(4)
v_mfma_f32_32x32x16_bf16 v[160:175], a[244:247], v[104:107], v[160:175]
s_add_u32 s78, s13, 0xe000
buffer_store_dwordx4 v[24:27], v1, s[48:51], s78 offen offset:1024 sc1
ds_read_b128 a[236:239], v1 offset:58368
v_mfma_f32_32x32x16_bf16 v[160:175], a[248:251], v[108:111], v[160:175]
ds_read_b128 a[240:243], v254 offset:31744
s_waitcnt lgkmcnt(4)
v_mfma_f32_32x32x16_bf16 v[160:175], a[252:255], v[112:115], v[160:175]
buffer_load_dword v216, v217, s[36:39], s94 offen
v_mfma_f32_32x32x16_bf16 v[160:175], a[224:227], v[116:119], v[160:175]
s_waitcnt lgkmcnt(3)
v_mfma_f32_32x32x16_bf16 v[160:175], a[228:231], v[120:123], v[160:175]
s_waitcnt lgkmcnt(2)
v_mfma_f32_32x32x16_bf16 v[160:175], a[232:235], v[124:127], v[160:175]
s_waitcnt lgkmcnt(1)
v_mfma_f32_32x32x16_bf16 v[160:175], a[236:239], v[128:131], v[160:175]
s_add_u32 s78, s13, 0x15000
buffer_store_dwordx4 v[24:27], v1, s[48:51], s78 offen sc1
s_waitcnt lgkmcnt(0)
v_mfma_f32_32x32x16_bf16 v[160:175], a[240:243], v[140:143], v[160:175]
v_mov_b32 v252, 1.0
s_nop 10
v_pk_mul_f32 v[160:161], v[160:161], v[238:239] op_sel:[0,1] op_sel_hi:[1,1]
v_pk_mul_f32 v[162:163], v[162:163], v[238:239] op_sel:[0,1] op_sel_hi:[1,1]
v_pk_mul_f32 v[164:165], v[164:165], v[238:239] op_sel:[0,1] op_sel_hi:[1,1]
v_pk_mul_f32 v[166:167], v[166:167], v[238:239] op_sel:[0,1] op_sel_hi:[1,1]
v_pk_mul_f32 v[168:169], v[168:169], v[238:239] op_sel:[0,1] op_sel_hi:[1,1]
v_pk_mul_f32 v[170:171], v[170:171], v[238:239] op_sel:[0,1] op_sel_hi:[1,1]
v_pk_mul_f32 v[172:173], v[172:173], v[238:239] op_sel:[0,1] op_sel_hi:[1,1]
v_pk_mul_f32 v[174:175], v[174:175], v[238:239] op_sel:[0,1] op_sel_hi:[1,1]
v_cndmask_b32 v172, v172, v252, s[58:59]
v_cvt_pk_bf16_f32 v184, v160, v161
v_cvt_pk_bf16_f32 v185, v162, v163
ds_write_b64 v226, v[184:185] offset:16384
v_cvt_pk_bf16_f32 v186, v164, v165
v_cvt_pk_bf16_f32 v187, v166, v167
ds_write_b64 v225, v[186:187] offset:16384
v_cvt_pk_bf16_f32 v188, v168, v169
v_cvt_pk_bf16_f32 v189, v170, v171
ds_write_b64 v224, v[188:189] offset:16384
v_cvt_pk_bf16_f32 v190, v172, v173
v_cvt_pk_bf16_f32 v191, v174, v175
ds_write_b64 v223, v[190:191] offset:16384
s_waitcnt vmcnt(11)
s_waitcnt lgkmcnt(0)
s_barrier
ds_read_b64_tr_b16 a[160:161], v222
)ASM"
R"ASM(
ds_read_b64_tr_b16 a[162:163], v222 offset:1024
ds_read_b64_tr_b16 a[164:165], v221
ds_read_b64_tr_b16 a[166:167], v221 offset:1024
ds_read_b64_tr_b16 a[168:169], v220
ds_read_b64_tr_b16 a[170:171], v220 offset:1024
ds_read_b64_tr_b16 a[172:173], v222 offset:2048
ds_read_b64_tr_b16 a[174:175], v222 offset:3072
ds_read_b64_tr_b16 a[176:177], v221 offset:2048
ds_read_b64_tr_b16 a[178:179], v221 offset:3072
ds_read_b64_tr_b16 a[180:181], v220 offset:2048
ds_read_b64_tr_b16 a[182:183], v220 offset:3072
ds_read_b64_tr_b16 a[184:185], v222 offset:4096
ds_read_b64_tr_b16 a[186:187], v222 offset:5120
ds_read_b64_tr_b16 a[188:189], v221 offset:4096
ds_read_b64_tr_b16 a[190:191], v221 offset:5120
ds_read_b64_tr_b16 a[192:193], v220 offset:4096
ds_read_b64_tr_b16 a[194:195], v220 offset:5120
ds_read_b64_tr_b16 a[196:197], v222 offset:6144
ds_read_b64_tr_b16 a[198:199], v222 offset:7168
ds_read_b64_tr_b16 a[200:201], v221 offset:6144
ds_read_b64_tr_b16 a[202:203], v221 offset:7168
ds_read_b64_tr_b16 a[204:205], v220 offset:6144
ds_read_b64_tr_b16 a[206:207], v220 offset:7168
s_waitcnt lgkmcnt(14)
v_mfma_f32_16x16x32_bf16 v[160:163], a[160:163], a[164:167], 0
v_mfma_f32_16x16x32_bf16 v[164:167], a[160:163], a[168:171], 0
v_mfma_f32_16x16x32_bf16 v[160:163], a[172:175], a[176:179], v[160:163]
s_waitcnt lgkmcnt(11)
v_mfma_f32_16x16x32_bf16 v[164:167], a[172:175], a[180:183], v[164:167]
s_waitcnt lgkmcnt(7)
v_mfma_f32_16x16x32_bf16 v[160:163], a[184:187], a[188:191], v[160:163]
s_waitcnt lgkmcnt(5)
v_mfma_f32_16x16x32_bf16 v[164:167], a[184:187], a[192:195], v[164:167]
s_waitcnt lgkmcnt(2)
v_mfma_f32_16x16x32_bf16 v[160:163], a[196:199], a[200:203], v[160:163]
s_waitcnt lgkmcnt(0)
v_mfma_f32_16x16x32_bf16 v[164:167], a[196:199], a[204:207], v[164:167]
ds_read_b128 a[224:227], v254
ds_read_b128 a[228:231], v254 offset:1024
ds_read_b128 a[232:235], v254 offset:2048
ds_read_b128 a[236:239], v254 offset:3072
ds_read_b128 a[240:243], v254 offset:4096
ds_read_b128 a[244:247], v254 offset:5120
v_cvt_pk_bf16_f32 v184, v160, v161
s_nop 0
v_cvt_pk_bf16_f32 v186, v164, v165
v_cvt_pk_bf16_f32 v185, v162, v163
v_cvt_pk_bf16_f32 v187, v166, v167
v_permlane16_swap_b32 v184, v186
s_nop 0
v_permlane16_swap_b32 v185, v187
v_permlane32_swap_b32 v184, v186
s_nop 0
v_permlane32_swap_b32 v185, v187
buffer_store_dwordx4 v[184:187], v1, s[44:47], s5 offen sc1
s_waitcnt lgkmcnt(4)
v_mfma_f32_32x32x16_bf16 v[144:159], a[224:227], v[4:7], 0
s_lshl_b32 s71, s64, 4
s_add_u32 s71, s71, s65
s_lshl_b32 s71, s71, 6
s_add_u32 s71, s71, 0x34000
buffer_store_dword v236, v214, s[44:47], s71 offen sc1
s_lshl_b32 s72, s66, 4
s_add_u32 s72, s72, s67
s_lshl_b32 s72, s72, 6
s_add_u32 s72, s72, 0x38000
buffer_store_dword v236, v214, s[44:47], s72 offen sc1
ds_read_b128 a[248:251], v254 offset:6144
v_mfma_f32_32x32x16_bf16 v[144:159], a[228:231], v[8:11], v[144:159]
ds_read_b128 a[252:255], v254 offset:7168
s_waitcnt lgkmcnt(4)
v_mfma_f32_32x32x16_bf16 v[144:159], a[232:235], v[28:31], v[144:159]
buffer_load_dword v216, v217, s[36:39], s94 offen offset:768
ds_read_b128 a[224:227], v254 offset:8192
v_mfma_f32_32x32x16_bf16 v[144:159], a[236:239], v[32:35], v[144:159]
ds_read_b128 a[228:231], v254 offset:9216
s_waitcnt lgkmcnt(4)
v_mfma_f32_32x32x16_bf16 v[144:159], a[240:243], v[36:39], v[144:159]
v_bfe_u32 v250, v249, 4, 1
v_lshl_or_b32 v250, v250, 5, v243
s_lshl_b32 s73, s63, 11
s_add_u32 s73, s73, 0x26000
v_xor_b32 v250, s92, v250
v_lshlrev_b32 v253, s93, v247
v_add_u32 v253, v253, v244
s_sub_u32 s74, 12, s93
v_lshl_add_u32 v252, v253, 7, v250
s_lshl_b32 s74, 1, s74
v_xor_b32 v250, 64, v252
v_add_u32 v252, s73, v252
v_add_u32 v250, s73, v250
v_add_u32 v253, s74, v252
v_add_u32 v251, s74, v250
s_waitcnt vmcnt(11)
ds_read_b64_tr_b16 v[12:13], v252
ds_read_b64_tr_b16 v[14:15], v253
ds_read_b64_tr_b16 v[16:17], v250
ds_read_b64_tr_b16 v[18:19], v251
ds_read_b128 a[232:235], v254 offset:10240
v_mfma_f32_32x32x16_bf16 v[144:159], a[244:247], v[40:43], v[144:159]
ds_read_b128 a[236:239], v254 offset:11264
s_waitcnt lgkmcnt(8)
v_mfma_f32_32x32x16_bf16 v[144:159], a[248:251], v[44:47], v[144:159]
s_add_u32 m0, s97, 0x17000
s_add_u32 s78, s8, 0
buffer_load_dwordx4 v227, s[36:39], s78 offen lds
ds_read_b128 a[240:243], v254 offset:12288
v_mfma_f32_32x32x16_bf16 v[144:159], a[252:255], v[48:51], v[144:159]
ds_read_b128 a[244:247], v254 offset:13312
s_waitcnt lgkmcnt(8)
v_mfma_f32_32x32x16_bf16 v[144:159], a[224:227], v[52:55], v[144:159]
s_cmp_lg_u32 s34, 0
s_cselect_b32 s34, 0xf6a000, 0
s_mov_b64 s[32:33], s[44:45]
s_waitcnt lgkmcnt(6)
buffer_store_dwordx4 v[12:15], v1, s[32:35], s91 offen sc1
ds_read_b128 a[248:251], v254 offset:14336
v_mfma_f32_32x32x16_bf16 v[144:159], a[228:231], v[56:59], v[144:159]
ds_read_b128 a[252:255], v254 offset:15360
s_waitcnt lgkmcnt(4)
v_mfma_f32_32x32x16_bf16 v[144:159], a[232:235], v[60:63], v[144:159]
s_add_u32 m0, s97, 0x17400
s_add_u32 s78, s8, 0x1c00
buffer_load_dwordx4 v227, s[36:39], s78 offen lds
ds_read_b128 a[224:227], v254 offset:16384
v_mfma_f32_32x32x16_bf16 v[144:159], a[236:239], v[64:67], v[144:159]
ds_read_b128 a[228:231], v254 offset:17408
s_waitcnt lgkmcnt(4)
v_mfma_f32_32x32x16_bf16 v[144:159], a[240:243], v[68:71], v[144:159]
buffer_store_dwordx4 v[16:19], v1, s[32:35], s91 offen offset:1024 sc1
ds_read_b128 a[232:235], v254 offset:18432
v_mfma_f32_32x32x16_bf16 v[144:159], a[244:247], v[72:75], v[144:159]
ds_read_b128 a[236:239], v254 offset:19456
s_waitcnt lgkmcnt(4)
v_mfma_f32_32x32x16_bf16 v[144:159], a[248:251], v[76:79], v[144:159]
s_add_u32 m0, s97, 0x17800
s_add_u32 s78, s56, 0
buffer_load_dwordx4 v227, s[36:39], s78 offen lds
ds_read_b128 a[240:243], v254 offset:20480
v_mfma_f32_32x32x16_bf16 v[144:159], a[252:255], v[80:83], v[144:159]
ds_read_b128 a[244:247], v254 offset:21504
s_waitcnt lgkmcnt(4)
v_mfma_f32_32x32x16_bf16 v[144:159], a[224:227], v[84:87], v[144:159]
s_add_u32 m0, s97, 0x17c00
s_add_u32 s78, s56, 0x1c00
buffer_load_dwordx4 v227, s[36:39], s78 offen lds
ds_read_b128 a[248:251], v254 offset:22528
v_mfma_f32_32x32x16_bf16 v[144:159], a[228:231], v[88:91], v[144:159]
ds_read_b128 a[252:255], v254 offset:23552
s_waitcnt lgkmcnt(4)
v_mfma_f32_32x32x16_bf16 v[144:159], a[232:235], v[92:95], v[144:159]
s_add_u32 s75, s6, 0
buffer_load_dwordx4 v[4:7], v1, s[44:47], s75 offen sc1
ds_read_b128 a[224:227], v254 offset:24576
v_mfma_f32_32x32x16_bf16 v[144:159], a[236:239], v[96:99], v[144:159]
ds_read_b128 a[228:231], v254 offset:25600
s_waitcnt lgkmcnt(4)
v_mfma_f32_32x32x16_bf16 v[144:159], a[240:243], v[100:103], v[144:159]
buffer_load_dwordx4 v[8:11], v1, s[44:47], s75 offen offset:1024 sc1
ds_read_b128 a[232:235], v254 offset:26624
v_mfma_f32_32x32x16_bf16 v[144:159], a[244:247], v[104:107], v[144:159]
ds_read_b128 a[236:239], v254 offset:27648
s_waitcnt lgkmcnt(4)
v_mfma_f32_32x32x16_bf16 v[144:159], a[248:251], v[108:111], v[144:159]
buffer_load_dwordx4 v[28:31], v1, s[44:47], s75 offen offset:2048 sc1
ds_read_b128 a[240:243], v254 offset:32768
v_mfma_f32_32x32x16_bf16 v[144:159], a[252:255], v[112:115], v[144:159]
s_waitcnt lgkmcnt(3)
v_mfma_f32_32x32x16_bf16 v[144:159], a[224:227], v[116:119], v[144:159]
buffer_load_dwordx4 v[32:35], v1, s[44:47], s75 offen offset:3072 sc1
v_mfma_f32_32x32x16_bf16 v[144:159], a[228:231], v[120:123], v[144:159]
s_waitcnt lgkmcnt(2)
v_mfma_f32_32x32x16_bf16 v[144:159], a[232:235], v[124:127], v[144:159]
s_add_u32 s75, s6, 0x1000
buffer_load_dwordx4 v[36:39], v1, s[44:47], s75 offen sc1
s_waitcnt lgkmcnt(1)
v_mfma_f32_32x32x16_bf16 v[144:159], a[236:239], v[128:131], v[144:159]
s_waitcnt lgkmcnt(0)
v_mfma_f32_32x32x16_bf16 v[144:159], a[240:243], v[140:143], v[144:159]
buffer_load_dwordx4 v[40:43], v1, s[44:47], s75 offen offset:1024 sc1
s_waitcnt vmcnt(8)
s_waitcnt lgkmcnt(0)
s_barrier
s_lshl_b32 s71, s62, 6
s_add_u32 s71, s71, 0x3c000
buffer_store_dword v236, v214, s[44:47], s71 offen sc1
s_nop 4
v_cvt_pk_bf16_f32 v184, v144, v145
v_cvt_pk_bf16_f32 v185, v146, v147
v_cvt_pk_bf16_f32 v186, v148, v149
v_cvt_pk_bf16_f32 v187, v150, v151
v_cvt_pk_bf16_f32 v188, v152, v153
v_cvt_pk_bf16_f32 v189, v154, v155
v_cvt_pk_bf16_f32 v190, v156, v157
v_cvt_pk_bf16_f32 v191, v158, v159
v_mfma_f32_32x32x16_bf16 v[144:159], v[176:179], v[184:187], 0
buffer_load_dwordx4 v[44:47], v1, s[44:47], s75 offen offset:2048 sc1
v_mfma_f32_32x32x16_bf16 v[144:159], v[180:183], v[188:191], v[144:159]
s_nop 11
v_max3_f32 v232, v144, v145, v146
v_max3_f32 v224, -v144, -v145, -v146
v_max3_f32 v232, v232, v147, v148
v_max3_f32 v224, v224, -v147, -v148
v_max3_f32 v232, v232, v149, v150
v_max3_f32 v224, v224, -v149, -v150
v_max3_f32 v232, v232, v151, v152
v_max3_f32 v224, v224, -v151, -v152
buffer_load_dwordx4 v[48:51], v1, s[44:47], s75 offen offset:3072 sc1
v_max3_f32 v232, v232, v153, v154
v_max3_f32 v224, v224, -v153, -v154
v_max3_f32 v232, v232, v155, v156
v_max3_f32 v224, v224, -v155, -v156
v_max3_f32 v232, v232, v157, v158
v_max3_f32 v224, v224, -v157, -v158
v_max_f32 v232, v232, v159
v_max_f32 v224, v224, -v159
v_mov_b32 v250, 0xff800000
s_add_u32 s75, s6, 0x2000
buffer_load_dwordx4 v[52:55], v1, s[44:47], s75 offen sc1
v_mov_b32 v251, 0xff800000
v_permlane32_swap_b32 v232, v224
v_max_f32 v232, v232, v224
v_mov_b32 v224, v232
s_nop 1
v_permlane32_swap_b32 v232, v224
v_mul_f32 v252, -v232, v238
v_mul_f32 v253, -v224, v238
v_cndmask_b32 v250, v252, v250, s[58:59]
v_cndmask_b32 v251, v253, v251, s[58:59]
buffer_load_dwordx4 v[56:59], v1, s[44:47], s75 offen offset:1024 sc1
v_pk_fma_f32 v[84:85], v[144:145], v[238:239], v[252:253] op_sel:[0,0,1] op_sel_hi:[1,0,1] neg_lo:[1,0,0] neg_hi:[1,0,0]
v_pk_fma_f32 v[144:145], v[144:145], v[238:239], v[252:253] op_sel_hi:[1,0,0]
v_pk_fma_f32 v[86:87], v[146:147], v[238:239], v[252:253] op_sel:[0,0,1] op_sel_hi:[1,0,1] neg_lo:[1,0,0] neg_hi:[1,0,0]
v_pk_fma_f32 v[146:147], v[146:147], v[238:239], v[252:253] op_sel_hi:[1,0,0]
v_exp_f32 v84, v84
v_exp_f32 v144, v144
v_exp_f32 v85, v85
v_exp_f32 v145, v145
v_pk_fma_f32 v[88:89], v[148:149], v[238:239], v[252:253] op_sel:[0,0,1] op_sel_hi:[1,0,1] neg_lo:[1,0,0] neg_hi:[1,0,0]
buffer_load_dwordx4 v[60:63], v1, s[44:47], s75 offen offset:2048 sc1
v_pk_fma_f32 v[148:149], v[148:149], v[238:239], v[252:253] op_sel_hi:[1,0,0]
v_exp_f32 v86, v86
v_exp_f32 v146, v146
v_exp_f32 v87, v87
v_exp_f32 v147, v147
v_pk_fma_f32 v[90:91], v[150:151], v[238:239], v[252:253] op_sel:[0,0,1] op_sel_hi:[1,0,1] neg_lo:[1,0,0] neg_hi:[1,0,0]
v_pk_fma_f32 v[150:151], v[150:151], v[238:239], v[252:253] op_sel_hi:[1,0,0]
v_exp_f32 v88, v88
v_exp_f32 v148, v148
buffer_load_dwordx4 v[64:67], v1, s[44:47], s75 offen offset:3072 sc1
v_exp_f32 v89, v89
v_exp_f32 v149, v149
v_pk_fma_f32 v[92:93], v[152:153], v[238:239], v[252:253] op_sel:[0,0,1] op_sel_hi:[1,0,1] neg_lo:[1,0,0] neg_hi:[1,0,0]
v_pk_fma_f32 v[152:153], v[152:153], v[238:239], v[252:253] op_sel_hi:[1,0,0]
v_exp_f32 v90, v90
v_exp_f32 v150, v150
v_exp_f32 v91, v91
v_exp_f32 v151, v151
v_pk_fma_f32 v[94:95], v[154:155], v[238:239], v[252:253] op_sel:[0,0,1] op_sel_hi:[1,0,1] neg_lo:[1,0,0] neg_hi:[1,0,0]
s_add_u32 s75, s6, 0x3000
buffer_load_dwordx4 v[68:71], v1, s[44:47], s75 offen sc1
v_pk_fma_f32 v[154:155], v[154:155], v[238:239], v[252:253] op_sel_hi:[1,0,0]
v_exp_f32 v92, v92
v_exp_f32 v152, v152
v_exp_f32 v93, v93
v_exp_f32 v153, v153
v_pk_fma_f32 v[96:97], v[156:157], v[238:239], v[250:251] op_sel:[0,0,1] op_sel_hi:[1,0,1] neg_lo:[1,0,0] neg_hi:[1,0,0]
v_pk_fma_f32 v[156:157], v[156:157], v[238:239], v[250:251] op_sel_hi:[1,0,0]
v_exp_f32 v94, v94
v_exp_f32 v154, v154
buffer_load_dwordx4 v[72:75], v1, s[44:47], s75 offen offset:1024 sc1
v_exp_f32 v95, v95
v_exp_f32 v155, v155
v_pk_fma_f32 v[98:99], v[158:159], v[238:239], v[250:251] op_sel:[0,0,1] op_sel_hi:[1,0,1] neg_lo:[1,0,0] neg_hi:[1,0,0]
v_pk_fma_f32 v[158:159], v[158:159], v[238:239], v[250:251] op_sel_hi:[1,0,0]
v_exp_f32 v96, v96
v_exp_f32 v156, v156
v_exp_f32 v97, v97
v_exp_f32 v157, v157
v_exp_f32 v98, v98
buffer_load_dwordx4 v[76:79], v1, s[44:47], s75 offen offset:2048 sc1
v_exp_f32 v158, v158
v_exp_f32 v99, v99
v_exp_f32 v159, v159
v_pk_add_f32 v[232:233], v[144:145], v[146:147]
v_pk_add_f32 v[224:225], v[84:85], v[86:87]
v_pk_add_f32 v[232:233], v[232:233], v[148:149]
v_pk_add_f32 v[224:225], v[224:225], v[88:89]
v_pk_add_f32 v[232:233], v[232:233], v[150:151]
v_pk_add_f32 v[224:225], v[224:225], v[90:91]
buffer_load_dwordx4 v[80:83], v1, s[44:47], s75 offen offset:3072 sc1
v_pk_add_f32 v[232:233], v[232:233], v[152:153]
v_pk_add_f32 v[224:225], v[224:225], v[92:93]
v_pk_add_f32 v[232:233], v[232:233], v[154:155]
v_pk_add_f32 v[224:225], v[224:225], v[94:95]
v_pk_add_f32 v[232:233], v[232:233], v[156:157]
v_pk_add_f32 v[224:225], v[224:225], v[96:97]
v_pk_add_f32 v[232:233], v[232:233], v[158:159]
v_pk_add_f32 v[224:225], v[224:225], v[98:99]
v_add_f32 v232, v232, v233
s_add_u32 m0, s97, 0x18000
s_add_u32 s78, s69, 0
buffer_load_dwordx4 v227, s[36:39], s78 offen lds
v_add_f32 v224, v224, v225
s_nop 1
v_permlane32_swap_b32 v232, v224
v_add_f32 v232, v232, v224
v_mov_b32 v224, v232
s_nop 1
v_permlane32_swap_b32 v232, v224
v_rcp_f32 v232, v232
v_rcp_f32 v224, v224
v_pk_mul_f32 v[144:145], v[144:145], v[232:233] op_sel_hi:[1,0]
v_pk_mul_f32 v[84:85], v[84:85], v[224:225] op_sel_hi:[1,0]
s_add_u32 m0, s97, 0x18400
s_add_u32 s78, s69, 0x1c00
buffer_load_dwordx4 v227, s[36:39], s78 offen lds
v_pk_mul_f32 v[146:147], v[146:147], v[232:233] op_sel_hi:[1,0]
v_pk_mul_f32 v[86:87], v[86:87], v[224:225] op_sel_hi:[1,0]
v_pk_mul_f32 v[148:149], v[148:149], v[232:233] op_sel_hi:[1,0]
v_pk_mul_f32 v[88:89], v[88:89], v[224:225] op_sel_hi:[1,0]
v_pk_mul_f32 v[150:151], v[150:151], v[232:233] op_sel_hi:[1,0]
v_pk_mul_f32 v[90:91], v[90:91], v[224:225] op_sel_hi:[1,0]
v_pk_mul_f32 v[152:153], v[152:153], v[232:233] op_sel_hi:[1,0]
v_pk_mul_f32 v[92:93], v[92:93], v[224:225] op_sel_hi:[1,0]
v_pk_mul_f32 v[154:155], v[154:155], v[232:233] op_sel_hi:[1,0]
s_add_u32 m0, s97, 0x18800
s_add_u32 s78, s79, 0
buffer_load_dwordx4 v227, s[36:39], s78 offen lds
v_pk_mul_f32 v[94:95], v[94:95], v[224:225] op_sel_hi:[1,0]
v_pk_mul_f32 v[156:157], v[156:157], v[232:233] op_sel_hi:[1,0]
v_pk_mul_f32 v[96:97], v[96:97], v[224:225] op_sel_hi:[1,0]
v_pk_mul_f32 v[158:159], v[158:159], v[232:233] op_sel_hi:[1,0]
v_pk_mul_f32 v[98:99], v[98:99], v[224:225] op_sel_hi:[1,0]
v_cvt_pk_bf16_f32 v144, v144, v145
v_cvt_pk_bf16_f32 v84, v84, v85
v_cvt_pk_bf16_f32 v145, v146, v147
v_cvt_pk_bf16_f32 v85, v86, v87
s_add_u32 m0, s97, 0x18c00
s_add_u32 s78, s79, 0x1c00
buffer_load_dwordx4 v227, s[36:39], s78 offen lds
v_cvt_pk_bf16_f32 v146, v148, v149
v_cvt_pk_bf16_f32 v86, v88, v89
v_cvt_pk_bf16_f32 v147, v150, v151
v_cvt_pk_bf16_f32 v87, v90, v91
v_cvt_pk_bf16_f32 v148, v152, v153
v_cvt_pk_bf16_f32 v88, v92, v93
v_cvt_pk_bf16_f32 v149, v154, v155
v_cvt_pk_bf16_f32 v89, v94, v95
v_cvt_pk_bf16_f32 v150, v156, v157
s_add_u32 m0, s97, 0x19000
s_add_u32 s78, s87, 0
buffer_load_dwordx4 v227, s[36:39], s78 offen lds
v_cvt_pk_bf16_f32 v90, v96, v97
v_cvt_pk_bf16_f32 v151, v158, v159
v_cvt_pk_bf16_f32 v91, v98, v99
s_lshl_b32 s72, s18, 14
s_add_u32 s7, s72, 0x2c0000
s_sub_u32 s71, s18, s63
s_mul_i32 s73, s71, 0xe000
s_mul_i32 s74, s63, 0x1c00
s_add_u32 s73, s73, s74
s_add_u32 m0, s97, 0x19400
s_add_u32 s78, s87, 0x1c00
buffer_load_dwordx4 v227, s[36:39], s78 offen lds
s_add_u32 s80, s73, 0x680000
s_lshl_b32 s72, s66, 2
s_add_u32 s72, s72, s63
s_mul_i32 s73, s72, 0x7000
s_add_u32 s57, s73, 0x4c0000
s_mul_i32 s73, s72, 0x700
s_add_u32 s81, s73, 0x8000
s_mul_i32 s71, s67, 0x1c000
s_add_u32 s71, s71, s88
s_add_u32 m0, s97, 0x19800
s_add_u32 s78, s89, 0
buffer_load_dwordx4 v227, s[36:39], s78 offen lds
s_add_u32 s82, s71, 0xd80000
v_and_b32 v252, 1, v249
v_mov_b32 v253, 0x3f80
v_lshlrev_b32 v252, 4, v252
v_lshlrev_b32 v253, v252, v253
v_cmp_eq_u32 vcc, v244, v245
v_and_b32 v250, 2, v249
v_mov_b32 v14, 0
v_cndmask_b32 v253, 0, v253, vcc
s_add_u32 m0, s97, 0x19c00
s_add_u32 s78, s89, 0x1c00
buffer_load_dwordx4 v227, s[36:39], s78 offen lds
v_cmp_eq_u32 vcc, 0, v250
v_mov_b32 v15, 0
v_mov_b32 v16, 0
v_mov_b32 v17, 0
v_cndmask_b32 v12, 0, v253, vcc
v_sub_u32 v13, v253, v12
v_mov_b32 v18, v12
v_mov_b32 v19, v13
v_and_b32 v252, 0x1f0, v1
s_add_u32 m0, s97, 0x1a000
s_add_u32 s78, s96, 0
)ASM"
R"ASM(
buffer_load_dwordx4 v227, s[36:39], s78 offen lds
ds_write_b128 v252, v[14:17] offset:57344
s_lshl_b32 s71, s63, 11
s_add_u32 s71, s71, 0x26000
v_lshlrev_b32 v226, 7, v246
v_lshl_add_u32 v226, v245, 3, v226
v_add_u32 v226, s71, v226
v_add_u32 v231, 0x25000, v1
v_add_u32 v234, s88, v231
s_add_u32 m0, s97, 0x1a400
s_add_u32 s78, s96, 0x1c00
buffer_load_dwordx4 v227, s[36:39], s78 offen lds
v_lshrrev_b32 v254, 3, v249
v_and_b32 v219, 7, v249
v_mul_u32_u24 v254, 0x380, v254
v_lshl_add_u32 v254, v219, 4, v254
s_mul_i32 s76, s18, 0x3800
s_add_u32 s76, s76, s17
s_lshl_b32 s77, s63, 11
s_add_u32 m0, s77, 0x26000
s_nop 0
buffer_load_dwordx4 v254, s[20:23], s76 offen lds
s_add_u32 s76, s76, 0x1800
buffer_load_dwordx4 v254, s[20:23], s76 offen offset:1024 lds
s_mul_i32 s83, s63, 0x18800
s_add_u32 s83, s83, s17
s_add_u32 m0, s97, 0
s_add_u32 s78, s83, 0
buffer_load_dwordx4 v227, s[28:31], s78 offen lds
s_add_u32 m0, s97, 0x400
s_add_u32 s78, s83, 0x1c00
buffer_load_dwordx4 v227, s[28:31], s78 offen lds
s_add_u32 m0, s97, 0x800
s_add_u32 s78, s83, 0x3800
buffer_load_dwordx4 v227, s[28:31], s78 offen lds
s_add_u32 m0, s97, 0xc00
s_add_u32 s78, s83, 0x5400
buffer_load_dwordx4 v227, s[28:31], s78 offen lds
s_add_u32 m0, s97, 0x1000
s_add_u32 s78, s83, 0x7000
buffer_load_dwordx4 v227, s[28:31], s78 offen lds
s_add_u32 m0, s97, 0x1400
s_add_u32 s78, s83, 0x8c00
buffer_load_dwordx4 v227, s[28:31], s78 offen lds
s_add_u32 m0, s97, 0x1800
s_add_u32 s78, s83, 0xa800
buffer_load_dwordx4 v227, s[28:31], s78 offen lds
s_add_u32 s78, s13, 0x15000
buffer_store_dwordx4 v[24:27], v1, s[48:51], s78 offen offset:1024 sc1
s_add_u32 m0, s97, 0x1c00
s_add_u32 s78, s83, 0xc400
buffer_load_dwordx4 v227, s[28:31], s78 offen lds
s_add_u32 m0, s97, 0x2000
s_add_u32 s78, s83, 0xe000
buffer_load_dwordx4 v227, s[28:31], s78 offen lds
s_add_u32 m0, s97, 0x2400
s_add_u32 s78, s83, 0xfc00
buffer_load_dwordx4 v227, s[28:31], s78 offen lds
s_mov_b32 s76, 0x34000
buffer_load_dword v235, v215, s[24:27], s76 offen sc1
s_waitcnt vmcnt(14)
ds_read_b64_tr_b16 a[0:1], v242
ds_read_b64_tr_b16 a[2:3], v242 offset:1024
ds_read_b64_tr_b16 a[4:5], v241
ds_read_b64_tr_b16 a[6:7], v241 offset:1024
ds_read_b64_tr_b16 a[8:9], v242 offset:2048
ds_read_b64_tr_b16 a[10:11], v242 offset:3072
ds_read_b64_tr_b16 a[12:13], v241 offset:2048
ds_read_b64_tr_b16 a[14:15], v241 offset:3072
ds_read_b64_tr_b16 a[16:17], v242 offset:4096
ds_read_b64_tr_b16 a[18:19], v242 offset:5120
ds_read_b64_tr_b16 a[20:21], v241 offset:4096
ds_read_b64_tr_b16 a[22:23], v241 offset:5120
ds_read_b64_tr_b16 a[24:25], v242 offset:6144
ds_read_b64_tr_b16 a[26:27], v242 offset:7168
ds_read_b64_tr_b16 a[28:29], v241 offset:6144
ds_read_b64_tr_b16 a[30:31], v241 offset:7168
ds_read_b64_tr_b16 a[32:33], v242 offset:8192
ds_read_b64_tr_b16 a[34:35], v242 offset:9216
ds_read_b64_tr_b16 a[36:37], v241 offset:8192
ds_read_b64_tr_b16 a[38:39], v241 offset:9216
ds_read_b64_tr_b16 a[40:41], v242 offset:10240
ds_read_b64_tr_b16 a[42:43], v242 offset:11264
ds_read_b64_tr_b16 a[44:45], v241 offset:10240
ds_read_b64_tr_b16 a[46:47], v241 offset:11264
ds_read_b64_tr_b16 a[48:49], v242 offset:12288
ds_read_b64_tr_b16 a[50:51], v242 offset:13312
ds_read_b64_tr_b16 a[52:53], v241 offset:12288
ds_read_b64_tr_b16 a[54:55], v241 offset:13312
v_max3_u32 v240, v7, v11, v31
v_max_u32 v240, v240, v35
v_cmp_eq_u32 vcc, -1, v240
s_cbranch_vccnz CK1
CK1C:
v_mfma_f32_16x16x32_bf16 v[100:103], v[12:15], v[4:7], 0
s_add_u32 m0, s97, 0x2800
s_add_u32 s78, s83, 0x11800
buffer_load_dwordx4 v227, s[28:31], s78 offen lds
v_mfma_f32_16x16x32_bf16 v[104:107], v[16:19], v[4:7], 0
v_mfma_f32_16x16x32_bf16 v[100:103], v[12:15], v[8:11], v[100:103]
v_mfma_f32_16x16x32_bf16 v[104:107], v[16:19], v[8:11], v[104:107]
v_mfma_f32_16x16x32_bf16 v[100:103], v[12:15], v[28:31], v[100:103]
s_add_u32 m0, s97, 0x2c00
s_add_u32 s78, s83, 0x13400
buffer_load_dwordx4 v227, s[28:31], s78 offen lds
v_mfma_f32_16x16x32_bf16 v[104:107], v[16:19], v[28:31], v[104:107]
v_mfma_f32_16x16x32_bf16 v[100:103], v[12:15], v[32:35], v[100:103]
v_mfma_f32_16x16x32_bf16 v[104:107], v[16:19], v[32:35], v[104:107]
v_max3_u32 v240, v39, v43, v47
v_max_u32 v240, v240, v51
v_cmp_eq_u32 vcc, -1, v240
s_cbranch_vccnz CK2
CK2C:
v_mfma_f32_16x16x32_bf16 v[100:103], v[12:15], v[36:39], v[100:103]
s_add_u32 m0, s97, 0x3000
s_add_u32 s78, s83, 0x15000
buffer_load_dwordx4 v227, s[28:31], s78 offen lds
v_mfma_f32_16x16x32_bf16 v[104:107], v[16:19], v[36:39], v[104:107]
v_mfma_f32_16x16x32_bf16 v[100:103], v[12:15], v[40:43], v[100:103]
v_mfma_f32_16x16x32_bf16 v[104:107], v[16:19], v[40:43], v[104:107]
v_mfma_f32_16x16x32_bf16 v[100:103], v[12:15], v[44:47], v[100:103]
s_add_u32 m0, s97, 0x3400
s_add_u32 s78, s83, 0x16c00
buffer_load_dwordx4 v227, s[28:31], s78 offen lds
v_mfma_f32_16x16x32_bf16 v[104:107], v[16:19], v[44:47], v[104:107]
v_mfma_f32_16x16x32_bf16 v[100:103], v[12:15], v[48:51], v[100:103]
v_mfma_f32_16x16x32_bf16 v[104:107], v[16:19], v[48:51], v[104:107]
v_max3_u32 v240, v55, v59, v63
v_max_u32 v240, v240, v67
v_cmp_eq_u32 vcc, -1, v240
s_cbranch_vccnz CK3
CK3C:
v_mfma_f32_16x16x32_bf16 v[100:103], v[12:15], v[52:55], v[100:103]
s_waitcnt lgkmcnt(0)
s_add_u32 m0, s97, 0x17000
s_add_u32 s78, s8, 0x7000
buffer_load_dwordx4 v227, s[36:39], s78 offen lds
v_mfma_f32_16x16x32_bf16 v[104:107], v[16:19], v[52:55], v[104:107]
v_mfma_f32_16x16x32_bf16 v[100:103], v[12:15], v[56:59], v[100:103]
v_mfma_f32_16x16x32_bf16 v[104:107], v[16:19], v[56:59], v[104:107]
v_mfma_f32_16x16x32_bf16 v[100:103], v[12:15], v[60:63], v[100:103]
s_add_u32 m0, s97, 0x17400
s_add_u32 s78, s8, 0x8c00
buffer_load_dwordx4 v227, s[36:39], s78 offen lds
v_mfma_f32_16x16x32_bf16 v[104:107], v[16:19], v[60:63], v[104:107]
v_mfma_f32_16x16x32_bf16 v[100:103], v[12:15], v[64:67], v[100:103]
v_mfma_f32_16x16x32_bf16 v[104:107], v[16:19], v[64:67], v[104:107]
s_waitcnt vmcnt(18)
ds_read_b64 v[108:109], v226
ds_read_b64 v[110:111], v226 offset:32
ds_read_b64 v[112:113], v226 offset:64
ds_read_b64 v[114:115], v226 offset:96
v_max_u32 v240, v71, v75
v_cmp_eq_u32 vcc, -1, v240
s_cbranch_vccnz CK4
CK4C:
v_mfma_f32_16x16x32_bf16 v[100:103], v[12:15], v[68:71], v[100:103]
s_add_u32 m0, s97, 0x17800
s_add_u32 s78, s56, 0x7000
buffer_load_dwordx4 v227, s[36:39], s78 offen lds
v_mfma_f32_16x16x32_bf16 v[104:107], v[16:19], v[68:71], v[104:107]
v_mfma_f32_16x16x32_bf16 v[100:103], v[12:15], v[72:75], v[100:103]
v_mfma_f32_16x16x32_bf16 v[104:107], v[16:19], v[72:75], v[104:107]
v_max_u32 v240, v79, v83
v_cmp_eq_u32 vcc, -1, v240
s_cbranch_vccnz CK5
CK5C:
v_mfma_f32_16x16x32_bf16 v[100:103], v[12:15], v[76:79], v[100:103]
s_add_u32 m0, s97, 0x17c00
s_add_u32 s78, s56, 0x8c00
buffer_load_dwordx4 v227, s[36:39], s78 offen lds
v_mfma_f32_16x16x32_bf16 v[104:107], v[16:19], v[76:79], v[104:107]
v_mfma_f32_16x16x32_bf16 v[100:103], v[12:15], v[80:83], v[100:103]
v_mfma_f32_16x16x32_bf16 v[104:107], v[16:19], v[80:83], v[104:107]
s_waitcnt vmcnt(8)
v_cmp_ne_u32 vcc, s10, v235
s_cbranch_vccnz POLLG
POLLGB:
s_nop 3
v_cmp_u_f32 vcc, v100, v100
s_cbranch_vccnz PC6
PC6C:
v_cvt_pk_bf16_f32 v184, v100, v101
v_cvt_pk_bf16_f32 v185, v102, v103
v_cvt_pk_bf16_f32 v186, v104, v105
v_cvt_pk_bf16_f32 v187, v106, v107
ds_write_b128 v234, v[184:187]
s_waitcnt lgkmcnt(0)
s_barrier
ds_read_b128 v[4:7], v231
ds_read_b128 v[8:11], v231 offset:1024
ds_read_b128 v[12:15], v231 offset:2048
ds_read_b128 v[16:19], v231 offset:3072
s_add_u32 m0, s97, 0x18000
s_add_u32 s78, s69, 0x7000
buffer_load_dwordx4 v227, s[36:39], s78 offen lds
s_waitcnt lgkmcnt(3)
v_mfma_f32_32x32x16_bf16 v[28:43], v[4:7], v[144:147], 0
s_waitcnt lgkmcnt(2)
v_mfma_f32_32x32x16_bf16 v[28:43], v[8:11], v[148:151], v[28:43]
s_add_u32 m0, s97, 0x18400
s_add_u32 s78, s69, 0x8c00
buffer_load_dwordx4 v227, s[36:39], s78 offen lds
s_waitcnt lgkmcnt(1)
v_mfma_f32_32x32x16_bf16 v[28:43], v[12:15], v[84:87], v[28:43]
s_waitcnt lgkmcnt(0)
v_mfma_f32_32x32x16_bf16 v[28:43], v[16:19], v[88:91], v[28:43]
s_add_u32 m0, s97, 0x18800
s_add_u32 s78, s79, 0x7000
buffer_load_dwordx4 v227, s[36:39], s78 offen lds
v_mov_b32 v250, 0
s_nop 7
v_mov_b32 v252, v40
v_mov_b32 v253, v40
v_cndmask_b32 v40, v40, v250, s[58:59]
v_cndmask_b32 v41, v41, v250, s[58:59]
v_permlane32_swap_b32 v252, v253
v_add_f32 v253, 0x358637bd, v253
v_cndmask_b32 v42, v42, v250, s[58:59]
v_rcp_f32 v238, v253
v_cndmask_b32 v43, v43, v250, s[58:59]
v_pk_mul_f32 v[28:29], v[28:29], v[238:239] op_sel_hi:[1,0]
v_pk_mul_f32 v[30:31], v[30:31], v[238:239] op_sel_hi:[1,0]
v_pk_mul_f32 v[32:33], v[32:33], v[238:239] op_sel_hi:[1,0]
v_pk_mul_f32 v[34:35], v[34:35], v[238:239] op_sel_hi:[1,0]
v_pk_mul_f32 v[36:37], v[36:37], v[238:239] op_sel_hi:[1,0]
v_pk_mul_f32 v[38:39], v[38:39], v[238:239] op_sel_hi:[1,0]
v_pk_mul_f32 v[40:41], v[40:41], v[238:239] op_sel_hi:[1,0]
v_pk_mul_f32 v[42:43], v[42:43], v[238:239] op_sel_hi:[1,0]
v_cvt_pk_bf16_f32 v184, v28, v29
v_cvt_pk_bf16_f32 v185, v30, v31
v_cvt_pk_bf16_f32 v186, v32, v33
v_cvt_pk_bf16_f32 v187, v34, v35
v_cvt_pk_bf16_f32 v188, v36, v37
v_cvt_pk_bf16_f32 v189, v38, v39
v_cvt_pk_bf16_f32 v190, v40, v41
v_cvt_pk_bf16_f32 v191, v42, v43
buffer_store_dwordx4 v[184:187], v230, s[44:47], s11 offen sc1
buffer_store_dwordx4 v[188:191], v230, s[44:47], s11 offen offset:512 sc1
s_cmp_ge_u32 s67, 14
s_cbranch_scc1 SPARE
s_add_u32 m0, s97, 0x18c00
s_add_u32 s78, s79, 0x8c00
buffer_load_dwordx4 v227, s[36:39], s78 offen lds
v_lshrrev_b32 v252, 1, v245
v_and_b32 v253, 1, v245
v_lshl_add_u32 v253, v253, 2, v244
v_lshl_add_u32 v252, v252, 4, v253
v_and_b32 v253, 2, v252
v_bfe_u32 v250, v252, 2, 1
v_or_b32 v253, v253, v250
v_lshlrev_b32 v253, 5, v253
v_lshl_or_b32 v252, v252, 7, v243
v_or_b32 v254, v252, v253
s_add_u32 m0, s97, 0x19000
s_add_u32 s78, s87, 0x7000
buffer_load_dwordx4 v227, s[36:39], s78 offen lds
v_xor_b32 v235, 32, v254
v_xor_b32 v239, 32, v254
v_xor_b32 v238, 64, v254
v_xor_b32 v236, 0x60, v254
v_xor_b32 v234, 32, v235
v_xor_b32 v233, 64, v235
v_xor_b32 v232, 0x60, v235
s_waitcnt vmcnt(11)
s_barrier
s_add_u32 m0, s97, 0x19400
s_add_u32 s78, s87, 0x8c00
buffer_load_dwordx4 v227, s[36:39], s78 offen lds
ds_read_b64_tr_b16 v[84:85], v254
ds_read_b64_tr_b16 v[86:87], v254 offset:1024
ds_read_b64_tr_b16 v[88:89], v239
ds_read_b64_tr_b16 v[90:91], v239 offset:1024
ds_read_b64_tr_b16 v[92:93], v238
ds_read_b64_tr_b16 v[94:95], v238 offset:1024
ds_read_b64_tr_b16 v[96:97], v236
ds_read_b64_tr_b16 v[98:99], v236 offset:1024
s_add_u32 m0, s97, 0x19800
s_add_u32 s78, s89, 0x7000
buffer_load_dwordx4 v227, s[36:39], s78 offen lds
ds_read_b64_tr_b16 v[100:101], v235 offset:3584
ds_read_b64_tr_b16 v[102:103], v235 offset:4608
ds_read_b64_tr_b16 v[104:105], v234 offset:3584
ds_read_b64_tr_b16 v[106:107], v234 offset:4608
ds_read_b64_tr_b16 v[116:117], v233 offset:3584
ds_read_b64_tr_b16 v[118:119], v233 offset:4608
ds_read_b64_tr_b16 v[120:121], v232 offset:3584
ds_read_b64_tr_b16 v[122:123], v232 offset:4608
s_add_u32 m0, s97, 0x19c00
s_add_u32 s78, s89, 0x8c00
buffer_load_dwordx4 v227, s[36:39], s78 offen lds
ds_read_b64_tr_b16 v[124:125], v254 offset:7168
ds_read_b64_tr_b16 v[126:127], v254 offset:8192
ds_read_b64_tr_b16 v[128:129], v239 offset:7168
ds_read_b64_tr_b16 v[130:131], v239 offset:8192
ds_read_b64_tr_b16 v[132:133], v238 offset:7168
ds_read_b64_tr_b16 v[134:135], v238 offset:8192
ds_read_b64_tr_b16 v[136:137], v236 offset:7168
ds_read_b64_tr_b16 v[138:139], v236 offset:8192
s_add_u32 m0, s97, 0x1a000
s_add_u32 s78, s96, 0x7000
buffer_load_dwordx4 v227, s[36:39], s78 offen lds
ds_read_b64_tr_b16 v[140:141], v235 offset:10752
ds_read_b64_tr_b16 v[142:143], v235 offset:11776
ds_read_b64_tr_b16 v[144:145], v234 offset:10752
ds_read_b64_tr_b16 v[146:147], v234 offset:11776
ds_read_b64_tr_b16 v[148:149], v233 offset:10752
ds_read_b64_tr_b16 v[150:151], v233 offset:11776
ds_read_b64_tr_b16 v[152:153], v232 offset:10752
ds_read_b64_tr_b16 v[154:155], v232 offset:11776
s_add_u32 m0, s97, 0x1a400
s_add_u32 s78, s96, 0x8c00
buffer_load_dwordx4 v227, s[36:39], s78 offen lds
ds_read_b64_tr_b16 v[156:157], v254 offset:14336
ds_read_b64_tr_b16 v[158:159], v254 offset:15360
ds_read_b64_tr_b16 v[160:161], v239 offset:14336
ds_read_b64_tr_b16 v[162:163], v239 offset:15360
ds_read_b64_tr_b16 v[164:165], v238 offset:14336
ds_read_b64_tr_b16 v[166:167], v238 offset:15360
ds_read_b64_tr_b16 v[168:169], v236 offset:14336
ds_read_b64_tr_b16 v[170:171], v236 offset:15360
s_add_u32 m0, s88, 0xe200
s_add_u32 s78, s82, 0
buffer_load_dwordx4 v1, s[44:47], s78 offen sc1 lds
ds_read_b64_tr_b16 v[172:173], v235 offset:17920
ds_read_b64_tr_b16 v[174:175], v235 offset:18944
ds_read_b64_tr_b16 v[176:177], v234 offset:17920
ds_read_b64_tr_b16 v[178:179], v234 offset:18944
ds_read_b64_tr_b16 v[180:181], v233 offset:17920
ds_read_b64_tr_b16 v[182:183], v233 offset:18944
ds_read_b64_tr_b16 v[192:193], v232 offset:17920
ds_read_b64_tr_b16 v[194:195], v232 offset:18944
s_add_u32 m0, s88, 0xf200
s_add_u32 s78, s82, 0x1000
buffer_load_dwordx4 v1, s[44:47], s78 offen sc1 lds
ds_read_b64_tr_b16 v[196:197], v254 offset:21504
ds_read_b64_tr_b16 v[198:199], v254 offset:22528
ds_read_b64_tr_b16 v[200:201], v239 offset:21504
ds_read_b64_tr_b16 v[202:203], v239 offset:22528
ds_read_b64_tr_b16 v[204:205], v238 offset:21504
ds_read_b64_tr_b16 v[206:207], v238 offset:22528
ds_read_b64_tr_b16 v[208:209], v236 offset:21504
ds_read_b64_tr_b16 v[210:211], v236 offset:22528
s_add_u32 s75, s7, 0
buffer_load_dwordx4 v[20:23], v1, s[44:47], s75 offen sc1
ds_read_b64_tr_b16 a[112:113], v235 offset:25088
ds_read_b64_tr_b16 a[114:115], v235 offset:26112
ds_read_b64_tr_b16 a[116:117], v234 offset:25088
ds_read_b64_tr_b16 a[118:119], v234 offset:26112
ds_read_b64_tr_b16 a[120:121], v233 offset:25088
ds_read_b64_tr_b16 a[122:123], v233 offset:26112
ds_read_b64_tr_b16 a[124:125], v232 offset:25088
ds_read_b64_tr_b16 a[126:127], v232 offset:26112
buffer_load_dwordx4 v[24:27], v1, s[44:47], s75 offen offset:1024 sc1
ds_read_b64_tr_b16 a[128:129], v254 offset:28672
ds_read_b64_tr_b16 a[130:131], v254 offset:29696
ds_read_b64_tr_b16 a[132:133], v239 offset:28672
ds_read_b64_tr_b16 a[134:135], v239 offset:29696
ds_read_b64_tr_b16 a[136:137], v238 offset:28672
ds_read_b64_tr_b16 a[138:139], v238 offset:29696
ds_read_b64_tr_b16 a[140:141], v236 offset:28672
ds_read_b64_tr_b16 a[142:143], v236 offset:29696
s_add_u32 s75, s7, 0x800
buffer_load_dwordx4 v[28:31], v1, s[44:47], s75 offen sc1
ds_read_b64_tr_b16 a[144:145], v235 offset:32256
ds_read_b64_tr_b16 a[146:147], v235 offset:33280
ds_read_b64_tr_b16 a[148:149], v234 offset:32256
ds_read_b64_tr_b16 a[150:151], v234 offset:33280
ds_read_b64_tr_b16 a[152:153], v233 offset:32256
ds_read_b64_tr_b16 a[154:155], v233 offset:33280
ds_read_b64_tr_b16 a[156:157], v232 offset:32256
ds_read_b64_tr_b16 a[158:159], v232 offset:33280
buffer_load_dwordx4 v[32:35], v1, s[44:47], s75 offen offset:1024 sc1
ds_read_b64_tr_b16 a[160:161], v254 offset:35840
ds_read_b64_tr_b16 a[162:163], v254 offset:36864
ds_read_b64_tr_b16 a[164:165], v239 offset:35840
ds_read_b64_tr_b16 a[166:167], v239 offset:36864
ds_read_b64_tr_b16 a[168:169], v238 offset:35840
ds_read_b64_tr_b16 a[170:171], v238 offset:36864
ds_read_b64_tr_b16 a[172:173], v236 offset:35840
ds_read_b64_tr_b16 a[174:175], v236 offset:36864
s_add_u32 s75, s7, 0x1000
buffer_load_dwordx4 v[36:39], v1, s[44:47], s75 offen sc1
ds_read_b64_tr_b16 a[176:177], v235 offset:39424
ds_read_b64_tr_b16 a[178:179], v235 offset:40448
ds_read_b64_tr_b16 a[180:181], v234 offset:39424
ds_read_b64_tr_b16 a[182:183], v234 offset:40448
ds_read_b64_tr_b16 a[184:185], v233 offset:39424
ds_read_b64_tr_b16 a[186:187], v233 offset:40448
ds_read_b64_tr_b16 a[188:189], v232 offset:39424
ds_read_b64_tr_b16 a[190:191], v232 offset:40448
buffer_load_dwordx4 v[40:43], v1, s[44:47], s75 offen offset:1024 sc1
ds_read_b64_tr_b16 a[192:193], v254 offset:43008
ds_read_b64_tr_b16 a[194:195], v254 offset:44032
ds_read_b64_tr_b16 a[196:197], v239 offset:43008
ds_read_b64_tr_b16 a[198:199], v239 offset:44032
ds_read_b64_tr_b16 a[200:201], v238 offset:43008
ds_read_b64_tr_b16 a[202:203], v238 offset:44032
ds_read_b64_tr_b16 a[204:205], v236 offset:43008
ds_read_b64_tr_b16 a[206:207], v236 offset:44032
)ASM"
R"ASM(
s_add_u32 s75, s7, 0x1800
buffer_load_dwordx4 v[44:47], v1, s[44:47], s75 offen sc1
ds_read_b64_tr_b16 a[208:209], v235 offset:46592
ds_read_b64_tr_b16 a[210:211], v235 offset:47616
ds_read_b64_tr_b16 a[212:213], v234 offset:46592
ds_read_b64_tr_b16 a[214:215], v234 offset:47616
ds_read_b64_tr_b16 a[216:217], v233 offset:46592
ds_read_b64_tr_b16 a[218:219], v233 offset:47616
ds_read_b64_tr_b16 a[220:221], v232 offset:46592
ds_read_b64_tr_b16 a[222:223], v232 offset:47616
buffer_load_dwordx4 v[48:51], v1, s[44:47], s75 offen offset:1024 sc1
ds_read_b64_tr_b16 a[224:225], v254 offset:50176
ds_read_b64_tr_b16 a[226:227], v254 offset:51200
ds_read_b64_tr_b16 a[228:229], v239 offset:50176
ds_read_b64_tr_b16 a[230:231], v239 offset:51200
ds_read_b64_tr_b16 a[232:233], v238 offset:50176
ds_read_b64_tr_b16 a[234:235], v238 offset:51200
ds_read_b64_tr_b16 a[236:237], v236 offset:50176
ds_read_b64_tr_b16 a[238:239], v236 offset:51200
s_add_u32 s75, s7, 0x2000
buffer_load_dwordx4 v[52:55], v1, s[44:47], s75 offen sc1
ds_read_b64_tr_b16 a[240:241], v235 offset:53760
ds_read_b64_tr_b16 a[242:243], v235 offset:54784
ds_read_b64_tr_b16 a[244:245], v234 offset:53760
ds_read_b64_tr_b16 a[246:247], v234 offset:54784
ds_read_b64_tr_b16 a[248:249], v233 offset:53760
ds_read_b64_tr_b16 a[250:251], v233 offset:54784
ds_read_b64_tr_b16 a[252:253], v232 offset:53760
ds_read_b64_tr_b16 a[254:255], v232 offset:54784
buffer_load_dwordx4 v[56:59], v1, s[44:47], s75 offen offset:1024 sc1
v_lshlrev_b32 v4, 16, v108
v_and_b32 v5, 0xffff0000, v108
v_lshlrev_b32 v6, 16, v109
v_and_b32 v7, 0xffff0000, v109
v_lshlrev_b32 v8, 16, v110
v_and_b32 v9, 0xffff0000, v110
v_lshlrev_b32 v10, 16, v111
v_and_b32 v11, 0xffff0000, v111
s_add_u32 s75, s7, 0x2800
buffer_load_dwordx4 v[60:63], v1, s[44:47], s75 offen sc1
v_lshlrev_b32 v12, 16, v112
v_and_b32 v13, 0xffff0000, v112
v_lshlrev_b32 v14, 16, v113
v_and_b32 v15, 0xffff0000, v113
v_lshlrev_b32 v16, 16, v114
v_and_b32 v17, 0xffff0000, v114
v_lshlrev_b32 v18, 16, v115
v_and_b32 v19, 0xffff0000, v115
buffer_load_dwordx4 v[64:67], v1, s[44:47], s75 offen offset:1024 sc1
s_lshr_b32 s71, s63, 1
s_lshl_b32 s71, s71, 12
s_add_u32 s71, s71, 0x26000
v_add_u32 v231, s71, v229
s_add_u32 s75, s7, 0x3000
buffer_load_dwordx4 v[68:71], v1, s[44:47], s75 offen sc1
s_waitcnt vmcnt(11)
v_max_u32 v240, v23, v27
v_cmp_eq_u32 vcc, -1, v240
s_cbranch_vccnz CK7
CK7C:
s_waitcnt lgkmcnt(14)
v_mfma_f32_16x16x32_bf16 v[4:7], v[84:87], v[20:23], v[4:7]
v_mfma_f32_16x16x32_bf16 v[8:11], v[88:91], v[20:23], v[8:11]
buffer_load_dwordx4 v[72:75], v1, s[44:47], s75 offen offset:1024 sc1
v_mfma_f32_16x16x32_bf16 v[12:15], v[92:95], v[20:23], v[12:15]
v_mfma_f32_16x16x32_bf16 v[16:19], v[96:99], v[20:23], v[16:19]
v_mfma_f32_16x16x32_bf16 v[4:7], v[100:103], v[24:27], v[4:7]
v_mfma_f32_16x16x32_bf16 v[8:11], v[104:107], v[24:27], v[8:11]
s_add_u32 s75, s7, 0x3800
buffer_load_dwordx4 v[76:79], v1, s[44:47], s75 offen sc1
v_mfma_f32_16x16x32_bf16 v[12:15], v[116:119], v[24:27], v[12:15]
v_mfma_f32_16x16x32_bf16 v[16:19], v[120:123], v[24:27], v[16:19]
s_waitcnt vmcnt(11)
v_max_u32 v240, v31, v35
v_cmp_eq_u32 vcc, -1, v240
s_cbranch_vccnz CK8
CK8C:
v_mfma_f32_16x16x32_bf16 v[4:7], v[124:127], v[28:31], v[4:7]
ds_read_b64_tr_b16 a[56:57], v242
v_mfma_f32_16x16x32_bf16 v[8:11], v[128:131], v[28:31], v[8:11]
buffer_load_dwordx4 v[80:83], v1, s[44:47], s75 offen offset:1024 sc1
ds_read_b64_tr_b16 a[58:59], v242 offset:1024
ds_read_b64_tr_b16 a[60:61], v241
v_mfma_f32_16x16x32_bf16 v[12:15], v[132:135], v[28:31], v[12:15]
ds_read_b64_tr_b16 a[62:63], v241 offset:1024
ds_read_b64_tr_b16 a[64:65], v242 offset:2048
v_mfma_f32_16x16x32_bf16 v[16:19], v[136:139], v[28:31], v[16:19]
ds_read_b64_tr_b16 a[66:67], v242 offset:3072
ds_read_b64_tr_b16 a[68:69], v241 offset:2048
v_mfma_f32_16x16x32_bf16 v[4:7], v[140:143], v[32:35], v[4:7]
ds_read_b64_tr_b16 a[70:71], v241 offset:3072
ds_read_b64_tr_b16 a[72:73], v242 offset:4096
v_mfma_f32_16x16x32_bf16 v[8:11], v[144:147], v[32:35], v[8:11]
s_add_u32 m0, s88, 0x10200
s_add_u32 s78, s82, 0x2000
buffer_load_dwordx4 v1, s[44:47], s78 offen sc1 lds
ds_read_b64_tr_b16 a[74:75], v242 offset:5120
ds_read_b64_tr_b16 a[76:77], v241 offset:4096
v_mfma_f32_16x16x32_bf16 v[12:15], v[148:151], v[32:35], v[12:15]
ds_read_b64_tr_b16 a[78:79], v241 offset:5120
ds_read_b64_tr_b16 a[80:81], v242 offset:6144
v_mfma_f32_16x16x32_bf16 v[16:19], v[152:155], v[32:35], v[16:19]
ds_read_b64_tr_b16 a[82:83], v242 offset:7168
ds_read_b64_tr_b16 a[84:85], v241 offset:6144
s_waitcnt vmcnt(11)
v_max_u32 v240, v39, v43
v_cmp_eq_u32 vcc, -1, v240
s_cbranch_vccnz CK9
CK9C:
v_mfma_f32_16x16x32_bf16 v[4:7], v[156:159], v[36:39], v[4:7]
ds_read_b64_tr_b16 a[86:87], v241 offset:7168
ds_read_b64_tr_b16 a[88:89], v242 offset:8192
v_mfma_f32_16x16x32_bf16 v[8:11], v[160:163], v[36:39], v[8:11]
s_add_u32 m0, s88, 0x11200
s_add_u32 s78, s82, 0x3000
buffer_load_dwordx4 v1, s[44:47], s78 offen sc1 lds
ds_read_b64_tr_b16 a[90:91], v242 offset:9216
ds_read_b64_tr_b16 a[92:93], v241 offset:8192
v_mfma_f32_16x16x32_bf16 v[12:15], v[164:167], v[36:39], v[12:15]
ds_read_b64_tr_b16 a[94:95], v241 offset:9216
ds_read_b64_tr_b16 a[96:97], v242 offset:10240
v_mfma_f32_16x16x32_bf16 v[16:19], v[168:171], v[36:39], v[16:19]
ds_read_b64_tr_b16 a[98:99], v242 offset:11264
ds_read_b64_tr_b16 a[100:101], v241 offset:10240
v_mfma_f32_16x16x32_bf16 v[4:7], v[172:175], v[40:43], v[4:7]
ds_read_b64_tr_b16 a[102:103], v241 offset:11264
ds_read_b64_tr_b16 a[104:105], v242 offset:12288
v_mfma_f32_16x16x32_bf16 v[8:11], v[176:179], v[40:43], v[8:11]
s_add_u32 m0, s88, 0x12200
s_add_u32 s78, s82, 0x4000
buffer_load_dwordx4 v1, s[44:47], s78 offen sc1 lds
ds_read_b64_tr_b16 a[106:107], v242 offset:13312
ds_read_b64_tr_b16 a[108:109], v241 offset:12288
v_mfma_f32_16x16x32_bf16 v[12:15], v[180:183], v[40:43], v[12:15]
ds_read_b64_tr_b16 a[110:111], v241 offset:13312
v_mfma_f32_16x16x32_bf16 v[16:19], v[192:195], v[40:43], v[16:19]
s_waitcnt vmcnt(11)
v_max_u32 v240, v47, v51
v_cmp_eq_u32 vcc, -1, v240
s_cbranch_vccnz CK10
CK10C:
s_waitcnt lgkmcnt(0)
s_barrier
v_mfma_f32_16x16x32_bf16 v[4:7], v[196:199], v[44:47], v[4:7]
v_mfma_f32_16x16x32_bf16 v[8:11], v[200:203], v[44:47], v[8:11]
s_add_u32 m0, s88, 0x13200
s_add_u32 s78, s82, 0x5000
buffer_load_dwordx4 v1, s[44:47], s78 offen sc1 lds
v_mfma_f32_16x16x32_bf16 v[12:15], v[204:207], v[44:47], v[12:15]
v_mfma_f32_16x16x32_bf16 v[16:19], v[208:211], v[44:47], v[16:19]
v_mfma_f32_16x16x32_bf16 v[4:7], a[112:115], v[48:51], v[4:7]
v_mfma_f32_16x16x32_bf16 v[8:11], a[116:119], v[48:51], v[8:11]
s_add_u32 m0, s88, 0x14200
s_add_u32 s78, s82, 0x6000
buffer_load_dwordx4 v1, s[44:47], s78 offen sc1 lds
v_mfma_f32_16x16x32_bf16 v[12:15], a[120:123], v[48:51], v[12:15]
v_mfma_f32_16x16x32_bf16 v[16:19], a[124:127], v[48:51], v[16:19]
s_waitcnt vmcnt(11)
v_max_u32 v240, v55, v59
v_cmp_eq_u32 vcc, -1, v240
s_cbranch_vccnz CK11
CK11C:
v_mfma_f32_16x16x32_bf16 v[4:7], a[128:131], v[52:55], v[4:7]
v_mfma_f32_16x16x32_bf16 v[8:11], a[132:135], v[52:55], v[8:11]
s_add_u32 m0, s88, 0x15200
s_add_u32 s78, s82, 0x7000
buffer_load_dwordx4 v1, s[44:47], s78 offen sc1 lds
v_mfma_f32_16x16x32_bf16 v[12:15], a[136:139], v[52:55], v[12:15]
v_mfma_f32_16x16x32_bf16 v[16:19], a[140:143], v[52:55], v[16:19]
v_mfma_f32_16x16x32_bf16 v[4:7], a[144:147], v[56:59], v[4:7]
v_mfma_f32_16x16x32_bf16 v[8:11], a[148:151], v[56:59], v[8:11]
s_add_u32 m0, s88, 0x17000
s_add_u32 s78, s82, 0x8000
buffer_load_dwordx4 v1, s[44:47], s78 offen sc1 lds
v_mfma_f32_16x16x32_bf16 v[12:15], a[152:155], v[56:59], v[12:15]
v_mfma_f32_16x16x32_bf16 v[16:19], a[156:159], v[56:59], v[16:19]
s_waitcnt vmcnt(11)
v_max_u32 v240, v63, v67
v_cmp_eq_u32 vcc, -1, v240
s_cbranch_vccnz CK12
CK12C:
v_mfma_f32_16x16x32_bf16 v[4:7], a[160:163], v[60:63], v[4:7]
v_mfma_f32_16x16x32_bf16 v[8:11], a[164:167], v[60:63], v[8:11]
s_add_u32 m0, s88, 0x18000
s_add_u32 s78, s82, 0x9000
buffer_load_dwordx4 v1, s[44:47], s78 offen sc1 lds
v_mfma_f32_16x16x32_bf16 v[12:15], a[168:171], v[60:63], v[12:15]
v_mfma_f32_16x16x32_bf16 v[16:19], a[172:175], v[60:63], v[16:19]
v_mfma_f32_16x16x32_bf16 v[4:7], a[176:179], v[64:67], v[4:7]
v_mfma_f32_16x16x32_bf16 v[8:11], a[180:183], v[64:67], v[8:11]
s_add_u32 m0, s88, 0x19000
s_add_u32 s78, s82, 0xa000
buffer_load_dwordx4 v1, s[44:47], s78 offen sc1 lds
v_mfma_f32_16x16x32_bf16 v[12:15], a[184:187], v[64:67], v[12:15]
v_mfma_f32_16x16x32_bf16 v[16:19], a[188:191], v[64:67], v[16:19]
s_waitcnt vmcnt(11)
v_max_u32 v240, v71, v75
v_cmp_eq_u32 vcc, -1, v240
s_cbranch_vccnz CK13
CK13C:
v_mfma_f32_16x16x32_bf16 v[4:7], a[192:195], v[68:71], v[4:7]
v_mfma_f32_16x16x32_bf16 v[8:11], a[196:199], v[68:71], v[8:11]
s_add_u32 m0, s88, 0x1a000
s_add_u32 s78, s82, 0xb000
buffer_load_dwordx4 v1, s[44:47], s78 offen sc1 lds
v_mfma_f32_16x16x32_bf16 v[12:15], a[200:203], v[68:71], v[12:15]
v_mfma_f32_16x16x32_bf16 v[16:19], a[204:207], v[68:71], v[16:19]
v_mfma_f32_16x16x32_bf16 v[4:7], a[208:211], v[72:75], v[4:7]
v_mfma_f32_16x16x32_bf16 v[8:11], a[212:215], v[72:75], v[8:11]
s_add_u32 m0, s88, 0x1b000
s_add_u32 s78, s82, 0xc000
buffer_load_dwordx4 v1, s[44:47], s78 offen sc1 lds
v_mfma_f32_16x16x32_bf16 v[12:15], a[216:219], v[72:75], v[12:15]
v_mfma_f32_16x16x32_bf16 v[16:19], a[220:223], v[72:75], v[16:19]
s_waitcnt vmcnt(11)
v_max_u32 v240, v79, v83
v_cmp_eq_u32 vcc, -1, v240
s_cbranch_vccnz CK14
CK14C:
v_mfma_f32_16x16x32_bf16 v[4:7], a[224:227], v[76:79], v[4:7]
v_mfma_f32_16x16x32_bf16 v[8:11], a[228:231], v[76:79], v[8:11]
s_add_u32 m0, s88, 0x1c000
s_add_u32 s78, s82, 0xd000
buffer_load_dwordx4 v1, s[44:47], s78 offen sc1 lds
v_mfma_f32_16x16x32_bf16 v[12:15], a[232:235], v[76:79], v[12:15]
v_mfma_f32_16x16x32_bf16 v[16:19], a[236:239], v[76:79], v[16:19]
v_mfma_f32_16x16x32_bf16 v[4:7], a[240:243], v[80:83], v[4:7]
v_mfma_f32_16x16x32_bf16 v[8:11], a[244:247], v[80:83], v[8:11]
s_add_u32 m0, s88, 0x1d000
s_add_u32 s78, s82, 0xe000
buffer_load_dwordx4 v1, s[44:47], s78 offen sc1 lds
v_mfma_f32_16x16x32_bf16 v[12:15], a[248:251], v[80:83], v[12:15]
v_mfma_f32_16x16x32_bf16 v[16:19], a[252:255], v[80:83], v[16:19]
s_nop 1
v_cmp_u_f32 vcc, v4, v4
s_cbranch_vccnz PC15
PC15C:
v_cvt_pk_bf16_f32 v184, v4, v5
v_cvt_pk_bf16_f32 v185, v6, v7
v_cvt_pk_bf16_f32 v186, v8, v9
v_cvt_pk_bf16_f32 v187, v10, v11
v_cvt_pk_bf16_f32 v188, v12, v13
v_cvt_pk_bf16_f32 v189, v14, v15
v_cvt_pk_bf16_f32 v190, v16, v17
v_cvt_pk_bf16_f32 v191, v18, v19
v_permlane16_swap_b32 v184, v186
v_permlane16_swap_b32 v185, v187
v_permlane16_swap_b32 v188, v190
v_permlane16_swap_b32 v189, v191
buffer_store_dwordx4 v[184:187], v229, s[44:47], s12 offen sc1
buffer_store_dwordx4 v[188:191], v229, s[44:47], s12 offen offset:2048 sc1
v_pk_add_f32 v[20:21], v[4:5], v[6:7]
v_pk_mul_f32 v[22:23], v[4:5], v[4:5]
v_pk_fma_f32 v[22:23], v[6:7], v[6:7], v[22:23]
v_pk_add_f32 v[20:21], v[20:21], v[8:9]
v_pk_fma_f32 v[22:23], v[8:9], v[8:9], v[22:23]
v_pk_add_f32 v[20:21], v[20:21], v[10:11]
v_pk_fma_f32 v[22:23], v[10:11], v[10:11], v[22:23]
v_pk_add_f32 v[20:21], v[20:21], v[12:13]
v_pk_fma_f32 v[22:23], v[12:13], v[12:13], v[22:23]
v_pk_add_f32 v[20:21], v[20:21], v[14:15]
v_pk_fma_f32 v[22:23], v[14:15], v[14:15], v[22:23]
v_pk_add_f32 v[20:21], v[20:21], v[16:17]
v_pk_fma_f32 v[22:23], v[16:17], v[16:17], v[22:23]
v_pk_add_f32 v[20:21], v[20:21], v[18:19]
v_pk_fma_f32 v[22:23], v[18:19], v[18:19], v[22:23]
v_add_f32 v252, v20, v21
v_add_f32 v253, v22, v23
ds_write_b128 v231, v[184:187]
ds_write_b128 v231, v[188:191] offset:2048
v_permlane16_swap_b32 v252, v253
v_add_f32 v252, v252, v253
v_mov_b32 v253, v252
s_nop 1
v_permlane32_swap_b32 v252, v253
v_add_f32 v252, v252, v253
buffer_store_dword v252, v228, s[44:47], s14 offen sc1
s_waitcnt vmcnt(10)
s_waitcnt lgkmcnt(0)
s_barrier
s_add_u32 m0, s88, 0x1e000
s_add_u32 s78, s82, 0xf000
buffer_load_dwordx4 v1, s[44:47], s78 offen sc1 lds
v_lshlrev_b32 v232, 2, v249
v_add_u32 v231, 0xe200, v1
s_add_u32 m0, s88, 0x1f000
s_add_u32 s78, s82, 0x10000
buffer_load_dwordx4 v1, s[44:47], s78 offen sc1 lds
v_add_u32 v230, 0x17000, v1
v_add_u32 v242, 0xfffe9000, v242
s_add_u32 m0, s88, 0x20000
s_add_u32 s78, s82, 0x11000
buffer_load_dwordx4 v1, s[44:47], s78 offen sc1 lds
v_add_u32 v241, 0xfffe9000, v241
v_mov_b32 v69, 0
s_add_u32 m0, s88, 0x21000
s_add_u32 s78, s82, 0x12000
buffer_load_dwordx4 v1, s[44:47], s78 offen sc1 lds
v_mov_b32 v70, 0
v_mov_b32 v71, 0
s_add_u32 m0, s88, 0x22000
s_add_u32 s78, s82, 0x13000
buffer_load_dwordx4 v1, s[44:47], s78 offen sc1 lds
v_mov_b32 v73, 0
v_mov_b32 v74, 0
s_add_u32 m0, s88, 0x23000
s_add_u32 s78, s82, 0x14000
buffer_load_dwordx4 v1, s[44:47], s78 offen sc1 lds
v_mov_b32 v75, 0
v_mov_b32 v77, 0
s_add_u32 m0, s97, 0
s_add_u32 s78, s8, 0xe000
buffer_load_dwordx4 v227, s[36:39], s78 offen lds
v_mov_b32 v78, 0
v_mov_b32 v79, 0
s_add_u32 m0, s97, 0x400
s_add_u32 s78, s8, 0xfc00
buffer_load_dwordx4 v227, s[36:39], s78 offen lds
v_mov_b32 v81, 0
v_mov_b32 v82, 0
s_add_u32 s75, s57, 0
buffer_load_dwordx4 v[84:87], v1, s[44:47], s75 offen sc1
v_mov_b32 v83, 0
s_lshr_b32 s76, s63, 1
buffer_load_dwordx4 v[88:91], v1, s[44:47], s75 offen offset:1024 sc1
s_mul_i32 s76, s76, 0x1c000
s_add_u32 s5, s80, s76
buffer_load_dwordx4 v[92:95], v1, s[44:47], s75 offen offset:2048 sc1
s_xor_b32 s76, s76, 0x1c000
s_add_u32 s6, s80, s76
buffer_load_dwordx4 v[96:99], v1, s[44:47], s75 offen offset:3072 sc1
ds_read_b128 a[224:227], v231
ds_read_b128 a[228:231], v231 offset:1024
s_add_u32 s75, s57, 0x1000
buffer_load_dwordx4 v[100:103], v1, s[44:47], s75 offen sc1
ds_read_b128 a[232:235], v231 offset:2048
ds_read_b128 a[236:239], v231 offset:3072
buffer_load_dwordx4 v[104:107], v1, s[44:47], s75 offen offset:1024 sc1
ds_read_b128 a[240:243], v231 offset:4096
ds_read_b128 a[244:247], v231 offset:5120
buffer_load_dwordx4 v[108:111], v1, s[44:47], s75 offen offset:2048 sc1
ds_read_b128 a[248:251], v231 offset:6144
buffer_load_dwordx4 v[112:115], v1, s[44:47], s75 offen offset:3072 sc1
s_add_u32 s75, s57, 0x2000
buffer_load_dwordx4 v[116:119], v1, s[44:47], s75 offen sc1
buffer_load_dwordx4 v[120:123], v1, s[44:47], s75 offen offset:1024 sc1
buffer_load_dwordx4 v[124:127], v1, s[44:47], s75 offen offset:2048 sc1
buffer_load_dwordx4 v[128:131], v1, s[44:47], s75 offen offset:3072 sc1
s_waitcnt vmcnt(10)
v_max_u32 v240, v87, v91
v_cmp_eq_u32 vcc, -1, v240
s_cbranch_vccnz CK17
CK17C:
s_waitcnt lgkmcnt(5)
v_mfma_f32_32x32x16_bf16 v[4:19], a[224:227], v[84:87], 0
ds_read_b128 a[252:255], v231 offset:7168
v_mfma_f32_32x32x16_bf16 v[20:35], a[228:231], v[84:87], 0
ds_read_b128 a[112:115], v231 offset:8192
s_add_u32 s75, s57, 0x3000
buffer_load_dwordx4 v[132:135], v1, s[44:47], s75 offen sc1
s_waitcnt lgkmcnt(5)
v_mfma_f32_32x32x16_bf16 v[4:19], a[232:235], v[88:91], v[4:19]
ds_read_b128 a[116:119], v231 offset:9216
s_waitcnt vmcnt(9)
v_max_u32 v240, v95, v99
v_cmp_eq_u32 vcc, -1, v240
v_mfma_f32_32x32x16_bf16 v[20:35], a[236:239], v[88:91], v[20:35]
ds_read_b128 a[120:123], v231 offset:10240
buffer_load_dwordx4 v[136:139], v1, s[44:47], s75 offen offset:1024 sc1
s_cbranch_vccnz CK18
CK18C:
s_waitcnt lgkmcnt(5)
v_mfma_f32_32x32x16_bf16 v[4:19], a[240:243], v[92:95], v[4:19]
ds_read_b128 a[124:127], v231 offset:11264
v_mfma_f32_32x32x16_bf16 v[20:35], a[244:247], v[92:95], v[20:35]
ds_read_b128 a[224:227], v231 offset:12288
buffer_load_dwordx4 v[140:143], v1, s[44:47], s75 offen offset:2048 sc1
s_waitcnt lgkmcnt(5)
v_mfma_f32_32x32x16_bf16 v[4:19], a[248:251], v[96:99], v[4:19]
ds_read_b128 a[228:231], v231 offset:13312
s_waitcnt vmcnt(9)
v_max_u32 v240, v103, v107
v_cmp_eq_u32 vcc, -1, v240
v_mfma_f32_32x32x16_bf16 v[20:35], a[252:255], v[96:99], v[20:35]
ds_read_b128 a[232:235], v231 offset:14336
buffer_load_dwordx4 v[144:147], v1, s[44:47], s75 offen offset:3072 sc1
s_cbranch_vccnz CK19
CK19C:
s_waitcnt lgkmcnt(5)
v_mfma_f32_32x32x16_bf16 v[4:19], a[112:115], v[100:103], v[4:19]
ds_read_b128 a[236:239], v231 offset:15360
v_mfma_f32_32x32x16_bf16 v[20:35], a[116:119], v[100:103], v[20:35]
ds_read_b128 a[240:243], v231 offset:16384
s_add_u32 s75, s57, 0x4000
buffer_load_dwordx4 v[148:151], v1, s[44:47], s75 offen sc1
s_waitcnt lgkmcnt(5)
v_mfma_f32_32x32x16_bf16 v[4:19], a[120:123], v[104:107], v[4:19]
ds_read_b128 a[244:247], v231 offset:17408
s_waitcnt vmcnt(9)
v_max_u32 v240, v111, v115
)ASM"
R"ASM(
v_cmp_eq_u32 vcc, -1, v240
v_mfma_f32_32x32x16_bf16 v[20:35], a[124:127], v[104:107], v[20:35]
ds_read_b128 a[248:251], v231 offset:18432
buffer_load_dwordx4 v[152:155], v1, s[44:47], s75 offen offset:1024 sc1
s_cbranch_vccnz CK20
CK20C:
s_waitcnt lgkmcnt(5)
v_mfma_f32_32x32x16_bf16 v[4:19], a[224:227], v[108:111], v[4:19]
ds_read_b128 a[252:255], v231 offset:19456
v_mfma_f32_32x32x16_bf16 v[20:35], a[228:231], v[108:111], v[20:35]
ds_read_b128 a[112:115], v231 offset:20480
buffer_load_dwordx4 v[156:159], v1, s[44:47], s75 offen offset:2048 sc1
s_waitcnt lgkmcnt(5)
v_mfma_f32_32x32x16_bf16 v[4:19], a[232:235], v[112:115], v[4:19]
ds_read_b128 a[116:119], v231 offset:21504
s_waitcnt vmcnt(8)
v_max3_u32 v240, v119, v123, v127
s_waitcnt vmcnt(7)
v_max_u32 v240, v240, v131
v_cmp_eq_u32 vcc, -1, v240
v_mfma_f32_32x32x16_bf16 v[20:35], a[236:239], v[112:115], v[20:35]
ds_read_b128 a[120:123], v231 offset:22528
buffer_load_dwordx4 v[160:163], v1, s[44:47], s75 offen offset:3072 sc1
s_cbranch_vccnz CK21
CK21C:
s_waitcnt lgkmcnt(5)
v_mfma_f32_32x32x16_bf16 v[4:19], a[240:243], v[116:119], v[4:19]
ds_read_b128 a[124:127], v231 offset:23552
v_mfma_f32_32x32x16_bf16 v[20:35], a[244:247], v[116:119], v[20:35]
ds_read_b128 a[224:227], v231 offset:24576
s_add_u32 s75, s57, 0x5000
buffer_load_dwordx4 v[164:167], v1, s[44:47], s75 offen sc1
s_waitcnt lgkmcnt(5)
v_mfma_f32_32x32x16_bf16 v[4:19], a[248:251], v[120:123], v[4:19]
ds_read_b128 a[228:231], v231 offset:25600
v_mfma_f32_32x32x16_bf16 v[20:35], a[252:255], v[120:123], v[20:35]
ds_read_b128 a[232:235], v231 offset:26624
buffer_load_dwordx4 v[168:171], v1, s[44:47], s75 offen offset:1024 sc1
s_waitcnt lgkmcnt(5)
v_mfma_f32_32x32x16_bf16 v[4:19], a[112:115], v[124:127], v[4:19]
ds_read_b128 a[236:239], v231 offset:27648
v_mfma_f32_32x32x16_bf16 v[20:35], a[116:119], v[124:127], v[20:35]
ds_read_b128 a[240:243], v231 offset:28672
buffer_load_dwordx4 v[172:175], v1, s[44:47], s75 offen offset:2048 sc1
s_waitcnt lgkmcnt(5)
v_mfma_f32_32x32x16_bf16 v[4:19], a[120:123], v[128:131], v[4:19]
ds_read_b128 a[244:247], v231 offset:29696
s_waitcnt vmcnt(8)
v_max3_u32 v240, v135, v139, v143
s_waitcnt vmcnt(7)
v_max_u32 v240, v240, v147
v_cmp_eq_u32 vcc, -1, v240
v_mfma_f32_32x32x16_bf16 v[20:35], a[124:127], v[128:131], v[20:35]
ds_read_b128 a[248:251], v231 offset:30720
buffer_load_dwordx4 v[176:179], v1, s[44:47], s75 offen offset:3072 sc1
s_cbranch_vccnz CK22
CK22C:
s_waitcnt lgkmcnt(5)
v_mfma_f32_32x32x16_bf16 v[4:19], a[224:227], v[132:135], v[4:19]
ds_read_b128 a[252:255], v231 offset:31744
s_barrier
v_mfma_f32_32x32x16_bf16 v[20:35], a[228:231], v[132:135], v[20:35]
ds_read_b128 a[112:115], v230
s_add_u32 s75, s57, 0x6000
buffer_load_dwordx4 v[180:183], v1, s[44:47], s75 offen sc1
s_waitcnt lgkmcnt(5)
v_mfma_f32_32x32x16_bf16 v[4:19], a[232:235], v[136:139], v[4:19]
ds_read_b128 a[116:119], v230 offset:1024
v_mfma_f32_32x32x16_bf16 v[20:35], a[236:239], v[136:139], v[20:35]
ds_read_b128 a[120:123], v230 offset:2048
buffer_load_dwordx4 v[192:195], v1, s[44:47], s75 offen offset:1024 sc1
s_waitcnt lgkmcnt(5)
v_mfma_f32_32x32x16_bf16 v[4:19], a[240:243], v[140:143], v[4:19]
ds_read_b128 a[124:127], v230 offset:3072
v_mfma_f32_32x32x16_bf16 v[20:35], a[244:247], v[140:143], v[20:35]
ds_read_b128 a[224:227], v230 offset:4096
buffer_load_dwordx4 v[196:199], v1, s[44:47], s75 offen offset:2048 sc1
s_waitcnt lgkmcnt(5)
v_mfma_f32_32x32x16_bf16 v[4:19], a[248:251], v[144:147], v[4:19]
ds_read_b128 a[228:231], v230 offset:5120
s_waitcnt vmcnt(8)
v_max3_u32 v240, v151, v155, v159
s_waitcnt vmcnt(7)
v_max_u32 v240, v240, v163
v_cmp_eq_u32 vcc, -1, v240
v_mfma_f32_32x32x16_bf16 v[20:35], a[252:255], v[144:147], v[20:35]
ds_read_b128 a[232:235], v230 offset:6144
buffer_load_dwordx4 v[200:203], v1, s[44:47], s75 offen offset:3072 sc1
s_cbranch_vccnz CK23
CK23C:
s_waitcnt lgkmcnt(5)
v_mfma_f32_32x32x16_bf16 v[4:19], a[112:115], v[148:151], v[4:19]
ds_read_b128 a[236:239], v230 offset:7168
s_barrier
v_mfma_f32_32x32x16_bf16 v[20:35], a[116:119], v[148:151], v[20:35]
ds_read_b128 a[240:243], v230 offset:8192
buffer_load_dword v254, v232, s[44:47], s81 offen sc1
buffer_load_dword v239, v232, s[44:47], s81 offen offset:256 sc1
buffer_load_dword v238, v232, s[44:47], s81 offen offset:512 sc1
buffer_load_dword v236, v232, s[44:47], s81 offen offset:768 sc1
buffer_load_dword v235, v232, s[44:47], s81 offen offset:1024 sc1
buffer_load_dword v234, v232, s[44:47], s81 offen offset:1280 sc1
buffer_load_dword v233, v232, s[44:47], s81 offen offset:1536 sc1
s_lshl_b32 s76, s67, 9
s_add_u32 s76, s76, 0x4000
buffer_load_dword v68, v237, s[44:47], s76 offen sc1
buffer_load_dword v72, v237, s[44:47], s76 offen offset:128 sc1
buffer_load_dword v76, v237, s[44:47], s76 offen offset:256 sc1
buffer_load_dword v80, v237, s[44:47], s76 offen offset:384 sc1
s_add_u32 m0, s97, 0x800
s_add_u32 s78, s56, 0xe000
buffer_load_dwordx4 v227, s[36:39], s78 offen lds
s_waitcnt lgkmcnt(5)
v_mfma_f32_32x32x16_bf16 v[4:19], a[120:123], v[152:155], v[4:19]
ds_read_b128 a[244:247], v230 offset:9216
v_mfma_f32_32x32x16_bf16 v[20:35], a[124:127], v[152:155], v[20:35]
ds_read_b128 a[248:251], v230 offset:10240
s_add_u32 m0, s88, 0xe200
s_add_u32 s78, s82, 0x15000
buffer_load_dwordx4 v1, s[44:47], s78 offen sc1 lds
s_waitcnt lgkmcnt(5)
v_mfma_f32_32x32x16_bf16 v[4:19], a[224:227], v[156:159], v[4:19]
ds_read_b128 a[252:255], v230 offset:11264
v_mfma_f32_32x32x16_bf16 v[20:35], a[228:231], v[156:159], v[20:35]
ds_read_b128 a[112:115], v230 offset:12288
s_add_u32 m0, s88, 0xf200
s_add_u32 s78, s82, 0x16000
buffer_load_dwordx4 v1, s[44:47], s78 offen sc1 lds
s_waitcnt lgkmcnt(5)
v_mfma_f32_32x32x16_bf16 v[4:19], a[232:235], v[160:163], v[4:19]
ds_read_b128 a[116:119], v230 offset:13312
s_waitcnt vmcnt(19)
v_max3_u32 v240, v167, v171, v175
s_waitcnt vmcnt(18)
v_max_u32 v240, v240, v179
v_cmp_eq_u32 vcc, -1, v240
v_mfma_f32_32x32x16_bf16 v[20:35], a[236:239], v[160:163], v[20:35]
ds_read_b128 a[120:123], v230 offset:14336
s_add_u32 m0, s88, 0x10200
s_add_u32 s78, s82, 0x17000
buffer_load_dwordx4 v1, s[44:47], s78 offen sc1 lds
s_cbranch_vccnz CK24
CK24C:
s_waitcnt lgkmcnt(5)
v_mfma_f32_32x32x16_bf16 v[4:19], a[240:243], v[164:167], v[4:19]
ds_read_b128 a[124:127], v230 offset:15360
v_mfma_f32_32x32x16_bf16 v[20:35], a[244:247], v[164:167], v[20:35]
ds_read_b128 a[224:227], v230 offset:16384
s_add_u32 m0, s88, 0x11200
s_add_u32 s78, s82, 0x18000
buffer_load_dwordx4 v1, s[44:47], s78 offen sc1 lds
s_waitcnt lgkmcnt(5)
v_mfma_f32_32x32x16_bf16 v[4:19], a[248:251], v[168:171], v[4:19]
ds_read_b128 a[228:231], v230 offset:17408
s_waitcnt vmcnt(13)
v_max3_u32 v240, v254, v239, v238
s_waitcnt vmcnt(11)
v_max3_u32 v240, v240, v236, v235
s_waitcnt vmcnt(9)
v_max3_u32 v240, v240, v234, v233
s_waitcnt vmcnt(7)
v_max3_u32 v240, v240, v68, v72
s_waitcnt vmcnt(5)
v_max3_u32 v240, v240, v76, v80
v_cmp_eq_u32 vcc, -1, v240
s_cbranch_vccnz CK16
CK16C:
v_add_f32 v252, v254, v239
v_mfma_f32_32x32x16_bf16 v[20:35], a[252:255], v[168:171], v[20:35]
ds_read_b128 a[232:235], v230 offset:18432
s_add_u32 m0, s88, 0x12200
s_add_u32 s78, s82, 0x19000
buffer_load_dwordx4 v1, s[44:47], s78 offen sc1 lds
v_add_f32 v253, v238, v236
v_add_f32 v250, v235, v234
v_add_f32 v252, v252, v233
s_waitcnt lgkmcnt(5)
v_mfma_f32_32x32x16_bf16 v[4:19], a[112:115], v[172:175], v[4:19]
ds_read_b128 a[236:239], v230 offset:19456
v_add_f32 v253, v253, v250
v_add_f32 v252, v252, v253
v_mul_f32 v252, 0x3b124925, v252
v_mfma_f32_32x32x16_bf16 v[20:35], a[116:119], v[172:175], v[20:35]
ds_read_b128 a[240:243], v230 offset:20480
s_add_u32 m0, s88, 0x13200
s_add_u32 s78, s82, 0x1a000
buffer_load_dwordx4 v1, s[44:47], s78 offen sc1 lds
v_mov_b32 v253, v252
s_nop 1
v_permlane32_swap_b32 v252, v253
s_waitcnt lgkmcnt(5)
v_mfma_f32_32x32x16_bf16 v[4:19], a[120:123], v[176:179], v[4:19]
ds_read_b128 a[244:247], v230 offset:21504
v_max3_u32 v240, v183, v195, v199
v_max_u32 v240, v240, v203
v_cmp_eq_u32 vcc, -1, v240
v_mfma_f32_32x32x16_bf16 v[20:35], a[124:127], v[176:179], v[20:35]
ds_read_b128 a[248:251], v230 offset:22528
s_add_u32 m0, s88, 0x14200
s_add_u32 s78, s82, 0x1b000
buffer_load_dwordx4 v1, s[44:47], s78 offen sc1 lds
s_cbranch_vccnz CK25
CK25C:
v_cvt_pk_bf16_f32 v204, -v252, -v252
v_fma_f32 v253, -v252, v252, v253
v_mov_b32 v205, v204
s_waitcnt lgkmcnt(5)
v_mfma_f32_32x32x16_bf16 v[4:19], a[224:227], v[180:183], v[4:19]
ds_read_b128 a[252:255], v230 offset:23552
v_max_f32 v253, 0, v253
v_mov_b32 v206, v204
v_add_f32 v253, 0x3727c5ac, v253
v_mfma_f32_32x32x16_bf16 v[20:35], a[228:231], v[180:183], v[20:35]
ds_read_b128 a[112:115], v230 offset:24576
s_add_u32 m0, s97, 0xc00
s_add_u32 s78, s56, 0xfc00
buffer_load_dwordx4 v227, s[36:39], s78 offen lds
v_mov_b32 v207, v204
v_rsq_f32 v252, v253
s_waitcnt lgkmcnt(5)
v_mfma_f32_32x32x16_bf16 v[4:19], a[232:235], v[192:195], v[4:19]
ds_read_b128 a[116:119], v230 offset:26624
v_mul_f32 v228, v253, v252
v_mul_f32 v224, 0xbfb8aa3b, v252
v_mfma_f32_32x32x16_bf16 v[20:35], a[236:239], v[192:195], v[20:35]
ds_read_b128 a[120:123], v230 offset:28672
s_add_u32 m0, s97, 0x1000
s_add_u32 s78, s69, 0xe000
buffer_load_dwordx4 v227, s[36:39], s78 offen lds
s_waitcnt lgkmcnt(5)
v_mfma_f32_32x32x16_bf16 v[4:19], a[240:243], v[196:199], v[4:19]
ds_read_b128 a[124:127], v230 offset:30720
v_mfma_f32_32x32x16_bf16 v[20:35], a[244:247], v[196:199], v[20:35]
ds_read_b128 a[224:227], v230 offset:32768
s_add_u32 m0, s97, 0x1400
s_add_u32 s78, s69, 0xfc00
buffer_load_dwordx4 v227, s[36:39], s78 offen lds
s_waitcnt lgkmcnt(5)
v_mfma_f32_32x32x16_bf16 v[4:19], a[248:251], v[200:203], v[4:19]
ds_read_b128 a[228:231], v230 offset:34816
v_mfma_f32_32x32x16_bf16 v[20:35], a[252:255], v[200:203], v[20:35]
ds_read_b128 a[232:235], v230 offset:36864
s_add_u32 m0, s97, 0x1800
s_add_u32 s78, s79, 0xe000
buffer_load_dwordx4 v227, s[36:39], s78 offen lds
v_mfma_f32_32x32x16_bf16 v[4:19], v[68:71], v[204:207], v[4:19]
v_mfma_f32_32x32x16_bf16 v[20:35], v[72:75], v[204:207], v[20:35]
s_waitcnt lgkmcnt(5)
v_mfma_f32_32x32x16_bf16 v[36:51], a[112:115], v[84:87], 0
ds_read_b128 a[236:239], v230 offset:38912
v_mfma_f32_32x32x16_bf16 v[36:51], a[116:119], v[88:91], v[36:51]
ds_read_b128 a[240:243], v230 offset:40960
s_add_u32 m0, s97, 0x1c00
s_add_u32 s78, s79, 0xfc00
buffer_load_dwordx4 v227, s[36:39], s78 offen lds
s_nop 2
v_cmp_u_f32 vcc, v4, v4
s_cbranch_vccnz PC26
PC26C:
s_waitcnt lgkmcnt(5)
v_mfma_f32_32x32x16_bf16 v[36:51], a[120:123], v[92:95], v[36:51]
ds_read_b128 a[244:247], v230 offset:43008
v_mul_f32 v222, v4, v224
v_mul_f32 v223, v5, v224
v_mul_f32 v220, v6, v224
v_mul_f32 v221, v7, v224
v_mfma_f32_32x32x16_bf16 v[36:51], a[124:127], v[96:99], v[36:51]
ds_read_b128 a[248:251], v230 offset:45056
s_add_u32 m0, s97, 0x2000
s_add_u32 s78, s87, 0xe000
buffer_load_dwordx4 v227, s[36:39], s78 offen lds
v_exp_f32 v222, v222
v_exp_f32 v223, v223
v_exp_f32 v220, v220
s_waitcnt lgkmcnt(5)
v_mfma_f32_32x32x16_bf16 v[36:51], a[224:227], v[100:103], v[36:51]
ds_read_b128 a[252:255], v230 offset:47104
v_exp_f32 v221, v221
v_fma_f32 v222, v222, v228, v228
v_fma_f32 v223, v223, v228, v228
v_fma_f32 v220, v220, v228, v228
v_mfma_f32_32x32x16_bf16 v[36:51], a[228:231], v[104:107], v[36:51]
ds_read_b128 a[112:115], v230 offset:49152
s_add_u32 m0, s97, 0x2400
s_add_u32 s78, s87, 0xfc00
buffer_load_dwordx4 v227, s[36:39], s78 offen lds
v_fma_f32 v221, v221, v228, v228
v_rcp_f32 v222, v222
v_rcp_f32 v223, v223
s_waitcnt lgkmcnt(5)
v_mfma_f32_32x32x16_bf16 v[36:51], a[232:235], v[108:111], v[36:51]
ds_read_b128 a[116:119], v230 offset:51200
v_rcp_f32 v220, v220
v_rcp_f32 v221, v221
v_mul_f32 v222, v4, v222
v_mul_f32 v223, v5, v223
v_mfma_f32_32x32x16_bf16 v[36:51], a[236:239], v[112:115], v[36:51]
ds_read_b128 a[120:123], v230 offset:25600
s_add_u32 m0, s97, 0x2800
s_add_u32 s78, s89, 0xe000
buffer_load_dwordx4 v227, s[36:39], s78 offen lds
v_mul_f32 v220, v6, v220
v_mul_f32 v221, v7, v221
v_cvt_pk_bf16_f32 v184, v222, v223
s_waitcnt lgkmcnt(5)
v_mfma_f32_32x32x16_bf16 v[36:51], a[240:243], v[116:119], v[36:51]
ds_read_b128 a[124:127], v230 offset:27648
v_cvt_pk_bf16_f32 v185, v220, v221
v_mul_f32 v222, v8, v224
v_mul_f32 v223, v9, v224
v_mul_f32 v220, v10, v224
v_mfma_f32_32x32x16_bf16 v[36:51], a[244:247], v[120:123], v[36:51]
ds_read_b128 a[224:227], v230 offset:29696
s_add_u32 m0, s97, 0x2c00
s_add_u32 s78, s89, 0xfc00
buffer_load_dwordx4 v227, s[36:39], s78 offen lds
v_mul_f32 v221, v11, v224
v_exp_f32 v222, v222
v_exp_f32 v223, v223
s_waitcnt lgkmcnt(5)
v_mfma_f32_32x32x16_bf16 v[36:51], a[248:251], v[124:127], v[36:51]
ds_read_b128 a[228:231], v230 offset:31744
v_exp_f32 v220, v220
v_exp_f32 v221, v221
v_fma_f32 v222, v222, v228, v228
v_fma_f32 v223, v223, v228, v228
v_mfma_f32_32x32x16_bf16 v[36:51], a[252:255], v[128:131], v[36:51]
ds_read_b128 a[232:235], v230 offset:33792
s_add_u32 m0, s97, 0x3000
s_add_u32 s78, s96, 0xe000
buffer_load_dwordx4 v227, s[36:39], s78 offen lds
v_fma_f32 v220, v220, v228, v228
v_fma_f32 v221, v221, v228, v228
v_rcp_f32 v222, v222
s_waitcnt lgkmcnt(5)
v_mfma_f32_32x32x16_bf16 v[36:51], a[112:115], v[132:135], v[36:51]
ds_read_b128 a[236:239], v230 offset:35840
v_rcp_f32 v223, v223
v_rcp_f32 v220, v220
v_rcp_f32 v221, v221
v_mul_f32 v222, v8, v222
v_mfma_f32_32x32x16_bf16 v[36:51], a[116:119], v[136:139], v[36:51]
ds_read_b128 a[240:243], v230 offset:37888
s_add_u32 m0, s97, 0x3400
s_add_u32 s78, s96, 0xfc00
buffer_load_dwordx4 v227, s[36:39], s78 offen lds
v_mul_f32 v223, v9, v223
v_mul_f32 v220, v10, v220
v_mul_f32 v221, v11, v221
s_waitcnt lgkmcnt(5)
v_mfma_f32_32x32x16_bf16 v[52:67], a[120:123], v[84:87], 0
ds_read_b128 a[244:247], v230 offset:39936
v_cvt_pk_bf16_f32 v186, v222, v223
v_cvt_pk_bf16_f32 v187, v220, v221
s_add_u32 s75, s13, 0
buffer_store_dwordx4 v[184:187], v1, s[44:47], s75 offen sc1
v_mul_f32 v222, v12, v224
v_mfma_f32_32x32x16_bf16 v[52:67], a[124:127], v[88:91], v[52:67]
ds_read_b128 a[248:251], v230 offset:41984
s_mul_i32 s78, s68, 28
s_mul_i32 s77, s63, 7
s_add_u32 s77, s77, s78
s_lshl_b32 s77, s77, 11
s_add_u32 s77, s77, 0xf08000
buffer_load_dwordx4 a[168:171], v1, s[44:47], s77 offen sc1
v_mul_f32 v223, v13, v224
v_mul_f32 v220, v14, v224
v_mul_f32 v221, v15, v224
s_waitcnt lgkmcnt(5)
v_mfma_f32_32x32x16_bf16 v[52:67], a[224:227], v[92:95], v[52:67]
ds_read_b128 a[252:255], v230 offset:44032
v_exp_f32 v222, v222
v_exp_f32 v223, v223
v_exp_f32 v220, v220
v_exp_f32 v221, v221
v_mfma_f32_32x32x16_bf16 v[52:67], a[228:231], v[96:99], v[52:67]
ds_read_b128 a[112:115], v230 offset:46080
buffer_load_dwordx4 a[172:175], v1, s[44:47], s77 offen offset:1024 sc1
v_fma_f32 v222, v222, v228, v228
v_fma_f32 v223, v223, v228, v228
v_fma_f32 v220, v220, v228, v228
s_waitcnt lgkmcnt(5)
v_mfma_f32_32x32x16_bf16 v[52:67], a[232:235], v[100:103], v[52:67]
ds_read_b128 a[116:119], v230 offset:48128
v_fma_f32 v221, v221, v228, v228
v_rcp_f32 v222, v222
v_rcp_f32 v223, v223
v_rcp_f32 v220, v220
v_mfma_f32_32x32x16_bf16 v[52:67], a[236:239], v[104:107], v[52:67]
ds_read_b128 a[120:123], v230 offset:50176
buffer_load_dwordx4 a[176:179], v1, s[44:47], s77 offen offset:2048 sc1
v_rcp_f32 v221, v221
v_mul_f32 v222, v12, v222
v_mul_f32 v223, v13, v223
s_waitcnt lgkmcnt(5)
v_mfma_f32_32x32x16_bf16 v[52:67], a[240:243], v[108:111], v[52:67]
ds_read_b128 a[124:127], v230 offset:52224
v_mul_f32 v220, v14, v220
v_mul_f32 v221, v15, v221
v_cvt_pk_bf16_f32 v188, v222, v223
)ASM"
R"ASM(
v_cvt_pk_bf16_f32 v189, v220, v221
s_waitcnt vmcnt(15)
s_barrier
v_mfma_f32_32x32x16_bf16 v[52:67], a[244:247], v[112:115], v[52:67]
ds_read_b128 a[224:227], v231
buffer_load_dwordx4 a[180:183], v1, s[44:47], s77 offen offset:3072 sc1
v_mul_f32 v222, v16, v224
v_mul_f32 v223, v17, v224
v_mul_f32 v220, v18, v224
s_waitcnt lgkmcnt(5)
v_mfma_f32_32x32x16_bf16 v[52:67], a[248:251], v[116:119], v[52:67]
ds_read_b128 a[228:231], v231 offset:2048
v_mul_f32 v221, v19, v224
v_exp_f32 v222, v222
v_exp_f32 v223, v223
v_exp_f32 v220, v220
v_mfma_f32_32x32x16_bf16 v[52:67], a[252:255], v[120:123], v[52:67]
ds_read_b128 a[232:235], v231 offset:4096
s_add_u32 s77, s77, 0x1000
buffer_load_dwordx4 a[184:187], v1, s[44:47], s77 offen sc1
v_exp_f32 v221, v221
v_fma_f32 v222, v222, v228, v228
v_fma_f32 v223, v223, v228, v228
s_waitcnt lgkmcnt(5)
v_mfma_f32_32x32x16_bf16 v[52:67], a[112:115], v[124:127], v[52:67]
ds_read_b128 a[236:239], v231 offset:6144
v_fma_f32 v220, v220, v228, v228
v_fma_f32 v221, v221, v228, v228
v_rcp_f32 v222, v222
v_rcp_f32 v223, v223
v_mfma_f32_32x32x16_bf16 v[52:67], a[116:119], v[128:131], v[52:67]
ds_read_b128 a[240:243], v231 offset:8192
buffer_load_dwordx4 a[188:191], v1, s[44:47], s77 offen offset:1024 sc1
v_rcp_f32 v220, v220
v_rcp_f32 v221, v221
v_mul_f32 v222, v16, v222
s_waitcnt lgkmcnt(5)
v_mfma_f32_32x32x16_bf16 v[52:67], a[120:123], v[132:135], v[52:67]
ds_read_b128 a[244:247], v231 offset:10240
v_mul_f32 v223, v17, v223
v_mul_f32 v220, v18, v220
v_mul_f32 v221, v19, v221
v_cvt_pk_bf16_f32 v190, v222, v223
v_mfma_f32_32x32x16_bf16 v[52:67], a[124:127], v[136:139], v[52:67]
ds_read_b128 a[248:251], v231 offset:12288
buffer_load_dwordx4 a[192:195], v1, s[44:47], s77 offen offset:2048 sc1
v_cvt_pk_bf16_f32 v191, v220, v221
s_add_u32 s75, s13, 0
buffer_store_dwordx4 v[188:191], v1, s[44:47], s75 offen offset:1024 sc1
v_mul_f32 v222, v20, v224
s_waitcnt lgkmcnt(5)
v_mfma_f32_32x32x16_bf16 v[36:51], a[224:227], v[140:143], v[36:51]
ds_read_b128 a[252:255], v231 offset:14336
v_mul_f32 v223, v21, v224
v_mul_f32 v220, v22, v224
v_mul_f32 v221, v23, v224
v_exp_f32 v222, v222
v_mfma_f32_32x32x16_bf16 v[36:51], a[228:231], v[144:147], v[36:51]
ds_read_b128 a[112:115], v231 offset:16384
buffer_load_dwordx4 a[196:199], v1, s[44:47], s77 offen offset:3072 sc1
v_exp_f32 v223, v223
v_exp_f32 v220, v220
v_exp_f32 v221, v221
s_waitcnt lgkmcnt(5)
v_mfma_f32_32x32x16_bf16 v[36:51], a[232:235], v[148:151], v[36:51]
ds_read_b128 a[116:119], v231 offset:18432
v_fma_f32 v222, v222, v228, v228
v_fma_f32 v223, v223, v228, v228
v_fma_f32 v220, v220, v228, v228
v_fma_f32 v221, v221, v228, v228
v_mfma_f32_32x32x16_bf16 v[36:51], a[236:239], v[152:155], v[36:51]
ds_read_b128 a[120:123], v231 offset:20480
s_add_u32 s77, s77, 0x1000
buffer_load_dwordx4 a[200:203], v1, s[44:47], s77 offen sc1
v_rcp_f32 v222, v222
v_rcp_f32 v223, v223
v_rcp_f32 v220, v220
s_waitcnt lgkmcnt(5)
v_mfma_f32_32x32x16_bf16 v[36:51], a[240:243], v[156:159], v[36:51]
ds_read_b128 a[124:127], v231 offset:22528
v_rcp_f32 v221, v221
v_mul_f32 v222, v20, v222
v_mul_f32 v223, v21, v223
v_mul_f32 v220, v22, v220
v_mfma_f32_32x32x16_bf16 v[36:51], a[244:247], v[160:163], v[36:51]
ds_read_b128 a[224:227], v231 offset:24576
buffer_load_dwordx4 a[204:207], v1, s[44:47], s77 offen offset:1024 sc1
v_mul_f32 v221, v23, v221
v_cvt_pk_bf16_f32 v184, v222, v223
v_cvt_pk_bf16_f32 v185, v220, v221
s_waitcnt lgkmcnt(5)
v_mfma_f32_32x32x16_bf16 v[36:51], a[248:251], v[164:167], v[36:51]
ds_read_b128 a[228:231], v231 offset:26624
v_mul_f32 v222, v24, v224
v_mul_f32 v223, v25, v224
v_mul_f32 v220, v26, v224
v_mul_f32 v221, v27, v224
v_mfma_f32_32x32x16_bf16 v[36:51], a[252:255], v[168:171], v[36:51]
ds_read_b128 a[232:235], v231 offset:1024
buffer_load_dwordx4 a[208:211], v1, s[44:47], s77 offen offset:2048 sc1
v_exp_f32 v222, v222
v_exp_f32 v223, v223
v_exp_f32 v220, v220
s_waitcnt lgkmcnt(5)
v_mfma_f32_32x32x16_bf16 v[36:51], a[112:115], v[172:175], v[36:51]
ds_read_b128 a[236:239], v231 offset:3072
v_exp_f32 v221, v221
v_fma_f32 v222, v222, v228, v228
v_fma_f32 v223, v223, v228, v228
v_fma_f32 v220, v220, v228, v228
v_mfma_f32_32x32x16_bf16 v[36:51], a[116:119], v[176:179], v[36:51]
ds_read_b128 a[240:243], v231 offset:5120
buffer_load_dwordx4 a[212:215], v1, s[44:47], s77 offen offset:3072 sc1
v_fma_f32 v221, v221, v228, v228
v_rcp_f32 v222, v222
v_rcp_f32 v223, v223
s_waitcnt lgkmcnt(5)
v_mfma_f32_32x32x16_bf16 v[36:51], a[120:123], v[180:183], v[36:51]
ds_read_b128 a[244:247], v231 offset:7168
v_rcp_f32 v220, v220
v_rcp_f32 v221, v221
v_mul_f32 v222, v24, v222
v_mul_f32 v223, v25, v223
v_mfma_f32_32x32x16_bf16 v[36:51], a[124:127], v[192:195], v[36:51]
ds_read_b128 a[248:251], v231 offset:9216
s_add_u32 s77, s77, 0x1000
buffer_load_dwordx4 a[216:219], v1, s[44:47], s77 offen sc1
v_mul_f32 v220, v26, v220
v_mul_f32 v221, v27, v221
v_cvt_pk_bf16_f32 v186, v222, v223
s_waitcnt lgkmcnt(5)
v_mfma_f32_32x32x16_bf16 v[36:51], a[224:227], v[196:199], v[36:51]
ds_read_b128 a[252:255], v231 offset:11264
v_cvt_pk_bf16_f32 v187, v220, v221
s_add_u32 s75, s13, 0x7000
buffer_store_dwordx4 v[184:187], v1, s[44:47], s75 offen sc1
v_mfma_f32_32x32x16_bf16 v[36:51], a[228:231], v[200:203], v[36:51]
v_mfma_f32_32x32x16_bf16 v[36:51], v[76:79], v[204:207], v[36:51]
ds_read_b128 a[112:115], v231 offset:13312
buffer_load_dwordx4 a[220:223], v1, s[44:47], s77 offen offset:1024 sc1
s_waitcnt lgkmcnt(5)
v_mfma_f32_32x32x16_bf16 v[52:67], a[232:235], v[140:143], v[52:67]
ds_read_b128 a[116:119], v231 offset:15360
v_mfma_f32_32x32x16_bf16 v[52:67], a[236:239], v[144:147], v[52:67]
ds_read_b128 a[120:123], v231 offset:17408
s_waitcnt lgkmcnt(5)
v_mfma_f32_32x32x16_bf16 v[52:67], a[240:243], v[148:151], v[52:67]
ds_read_b128 a[124:127], v231 offset:19456
v_mfma_f32_32x32x16_bf16 v[52:67], a[244:247], v[152:155], v[52:67]
ds_read_b128 a[224:227], v231 offset:21504
s_waitcnt lgkmcnt(5)
v_mfma_f32_32x32x16_bf16 v[52:67], a[248:251], v[156:159], v[52:67]
ds_read_b128 a[228:231], v231 offset:23552
v_mfma_f32_32x32x16_bf16 v[52:67], a[252:255], v[160:163], v[52:67]
ds_read_b128 a[232:235], v231 offset:25600
s_waitcnt lgkmcnt(5)
v_mfma_f32_32x32x16_bf16 v[52:67], a[112:115], v[164:167], v[52:67]
ds_read_b128 a[236:239], v231 offset:27648
v_mfma_f32_32x32x16_bf16 v[52:67], a[116:119], v[168:171], v[52:67]
s_waitcnt lgkmcnt(4)
v_mfma_f32_32x32x16_bf16 v[52:67], a[120:123], v[172:175], v[52:67]
v_mfma_f32_32x32x16_bf16 v[52:67], a[124:127], v[176:179], v[52:67]
s_waitcnt lgkmcnt(3)
v_mfma_f32_32x32x16_bf16 v[52:67], a[224:227], v[180:183], v[52:67]
s_waitcnt lgkmcnt(2)
v_mfma_f32_32x32x16_bf16 v[52:67], a[228:231], v[192:195], v[52:67]
s_waitcnt lgkmcnt(1)
v_mfma_f32_32x32x16_bf16 v[52:67], a[232:235], v[196:199], v[52:67]
s_waitcnt lgkmcnt(0)
v_mfma_f32_32x32x16_bf16 v[52:67], a[236:239], v[200:203], v[52:67]
v_mfma_f32_32x32x16_bf16 v[52:67], v[80:83], v[204:207], v[52:67]
s_add_u32 s10, s5, 0
buffer_load_dwordx4 v[100:103], v1, s[44:47], s10 offen sc1
v_pk_mul_f32 v[222:223], v[28:29], v[224:225] op_sel_hi:[1,0]
v_pk_mul_f32 v[220:221], v[30:31], v[224:225] op_sel_hi:[1,0]
v_exp_f32 v222, v222
v_exp_f32 v223, v223
v_exp_f32 v220, v220
v_exp_f32 v221, v221
v_pk_fma_f32 v[222:223], v[222:223], v[228:229], v[228:229] op_sel_hi:[1,0,0]
v_pk_fma_f32 v[220:221], v[220:221], v[228:229], v[228:229] op_sel_hi:[1,0,0]
v_rcp_f32 v222, v222
v_rcp_f32 v223, v223
v_rcp_f32 v220, v220
s_add_u32 s15, s6, 0
buffer_load_dwordx4 v[104:107], v1, s[44:47], s15 offen sc1
v_rcp_f32 v221, v221
v_pk_mul_f32 v[222:223], v[222:223], v[28:29]
v_pk_mul_f32 v[220:221], v[220:221], v[30:31]
v_cvt_pk_bf16_f32 v188, v222, v223
v_cvt_pk_bf16_f32 v189, v220, v221
v_pk_mul_f32 v[222:223], v[32:33], v[224:225] op_sel_hi:[1,0]
v_pk_mul_f32 v[220:221], v[34:35], v[224:225] op_sel_hi:[1,0]
v_exp_f32 v222, v222
v_exp_f32 v223, v223
v_exp_f32 v220, v220
v_exp_f32 v221, v221
buffer_load_dwordx4 v[108:111], v1, s[44:47], s10 offen offset:1024 sc1
v_pk_fma_f32 v[222:223], v[222:223], v[228:229], v[228:229] op_sel_hi:[1,0,0]
v_pk_fma_f32 v[220:221], v[220:221], v[228:229], v[228:229] op_sel_hi:[1,0,0]
v_rcp_f32 v222, v222
v_rcp_f32 v223, v223
v_rcp_f32 v220, v220
v_rcp_f32 v221, v221
v_pk_mul_f32 v[222:223], v[222:223], v[32:33]
v_pk_mul_f32 v[220:221], v[220:221], v[34:35]
v_cvt_pk_bf16_f32 v190, v222, v223
v_cvt_pk_bf16_f32 v191, v220, v221
s_add_u32 s75, s13, 0x7000
buffer_store_dwordx4 v[188:191], v1, s[44:47], s75 offen offset:1024 sc1
buffer_load_dwordx4 v[112:115], v1, s[44:47], s15 offen offset:1024 sc1
v_pk_mul_f32 v[222:223], v[36:37], v[224:225] op_sel_hi:[1,0]
v_pk_mul_f32 v[220:221], v[38:39], v[224:225] op_sel_hi:[1,0]
v_exp_f32 v222, v222
v_exp_f32 v223, v223
v_exp_f32 v220, v220
v_exp_f32 v221, v221
v_pk_fma_f32 v[222:223], v[222:223], v[228:229], v[228:229] op_sel_hi:[1,0,0]
v_pk_fma_f32 v[220:221], v[220:221], v[228:229], v[228:229] op_sel_hi:[1,0,0]
v_rcp_f32 v222, v222
v_rcp_f32 v223, v223
v_rcp_f32 v220, v220
buffer_load_dwordx4 v[116:119], v1, s[44:47], s10 offen offset:2048 sc1
v_rcp_f32 v221, v221
v_pk_mul_f32 v[222:223], v[222:223], v[36:37]
v_pk_mul_f32 v[220:221], v[220:221], v[38:39]
v_cvt_pk_bf16_f32 v184, v222, v223
v_cvt_pk_bf16_f32 v185, v220, v221
v_pk_mul_f32 v[222:223], v[40:41], v[224:225] op_sel_hi:[1,0]
v_pk_mul_f32 v[220:221], v[42:43], v[224:225] op_sel_hi:[1,0]
v_exp_f32 v222, v222
v_exp_f32 v223, v223
v_exp_f32 v220, v220
v_exp_f32 v221, v221
buffer_load_dwordx4 v[120:123], v1, s[44:47], s15 offen offset:2048 sc1
v_pk_fma_f32 v[222:223], v[222:223], v[228:229], v[228:229] op_sel_hi:[1,0,0]
v_pk_fma_f32 v[220:221], v[220:221], v[228:229], v[228:229] op_sel_hi:[1,0,0]
v_rcp_f32 v222, v222
v_rcp_f32 v223, v223
v_rcp_f32 v220, v220
v_rcp_f32 v221, v221
v_pk_mul_f32 v[222:223], v[222:223], v[40:41]
v_pk_mul_f32 v[220:221], v[220:221], v[42:43]
v_cvt_pk_bf16_f32 v186, v222, v223
v_cvt_pk_bf16_f32 v187, v220, v221
s_add_u32 s75, s13, 0xe000
buffer_store_dwordx4 v[184:187], v1, s[44:47], s75 offen sc1
buffer_load_dwordx4 v[124:127], v1, s[44:47], s10 offen offset:3072 sc1
v_pk_mul_f32 v[222:223], v[44:45], v[224:225] op_sel_hi:[1,0]
v_pk_mul_f32 v[220:221], v[46:47], v[224:225] op_sel_hi:[1,0]
v_exp_f32 v222, v222
v_exp_f32 v223, v223
v_exp_f32 v220, v220
v_exp_f32 v221, v221
v_pk_fma_f32 v[222:223], v[222:223], v[228:229], v[228:229] op_sel_hi:[1,0,0]
v_pk_fma_f32 v[220:221], v[220:221], v[228:229], v[228:229] op_sel_hi:[1,0,0]
v_rcp_f32 v222, v222
v_rcp_f32 v223, v223
v_rcp_f32 v220, v220
buffer_load_dwordx4 v[128:131], v1, s[44:47], s15 offen offset:3072 sc1
v_rcp_f32 v221, v221
v_pk_mul_f32 v[222:223], v[222:223], v[44:45]
v_pk_mul_f32 v[220:221], v[220:221], v[46:47]
v_cvt_pk_bf16_f32 v188, v222, v223
v_cvt_pk_bf16_f32 v189, v220, v221
v_pk_mul_f32 v[222:223], v[48:49], v[224:225] op_sel_hi:[1,0]
v_pk_mul_f32 v[220:221], v[50:51], v[224:225] op_sel_hi:[1,0]
v_exp_f32 v222, v222
v_exp_f32 v223, v223
v_exp_f32 v220, v220
v_exp_f32 v221, v221
s_add_u32 s11, s5, 0x1000
buffer_load_dwordx4 v[132:135], v1, s[44:47], s11 offen sc1
v_pk_fma_f32 v[222:223], v[222:223], v[228:229], v[228:229] op_sel_hi:[1,0,0]
v_pk_fma_f32 v[220:221], v[220:221], v[228:229], v[228:229] op_sel_hi:[1,0,0]
v_rcp_f32 v222, v222
v_rcp_f32 v223, v223
v_rcp_f32 v220, v220
v_rcp_f32 v221, v221
v_pk_mul_f32 v[222:223], v[222:223], v[48:49]
v_pk_mul_f32 v[220:221], v[220:221], v[50:51]
v_cvt_pk_bf16_f32 v190, v222, v223
v_cvt_pk_bf16_f32 v191, v220, v221
s_add_u32 s75, s13, 0xe000
buffer_store_dwordx4 v[188:191], v1, s[44:47], s75 offen offset:1024 sc1
s_add_u32 s16, s6, 0x1000
buffer_load_dwordx4 v[136:139], v1, s[44:47], s16 offen sc1
s_waitcnt vmcnt(30)
ds_read_b64_tr_b16 a[112:113], v242
ds_read_b64_tr_b16 a[114:115], v242 offset:1024
ds_read_b64_tr_b16 a[116:117], v241
ds_read_b64_tr_b16 a[118:119], v241 offset:1024
ds_read_b64_tr_b16 a[120:121], v242 offset:2048
ds_read_b64_tr_b16 a[122:123], v242 offset:3072
ds_read_b64_tr_b16 a[124:125], v241 offset:2048
ds_read_b64_tr_b16 a[126:127], v241 offset:3072
ds_read_b64_tr_b16 a[128:129], v242 offset:4096
ds_read_b64_tr_b16 a[130:131], v242 offset:5120
buffer_load_dwordx4 v[140:143], v1, s[44:47], s11 offen offset:1024 sc1
ds_read_b64_tr_b16 a[132:133], v241 offset:4096
ds_read_b64_tr_b16 a[134:135], v241 offset:5120
ds_read_b64_tr_b16 a[136:137], v242 offset:6144
ds_read_b64_tr_b16 a[138:139], v242 offset:7168
ds_read_b64_tr_b16 a[140:141], v241 offset:6144
ds_read_b64_tr_b16 a[142:143], v241 offset:7168
ds_read_b64_tr_b16 a[144:145], v242 offset:8192
ds_read_b64_tr_b16 a[146:147], v242 offset:9216
ds_read_b64_tr_b16 a[148:149], v241 offset:8192
ds_read_b64_tr_b16 a[150:151], v241 offset:9216
ds_read_b64_tr_b16 a[152:153], v242 offset:10240
buffer_load_dwordx4 v[144:147], v1, s[44:47], s16 offen offset:1024 sc1
ds_read_b64_tr_b16 a[154:155], v242 offset:11264
ds_read_b64_tr_b16 a[156:157], v241 offset:10240
ds_read_b64_tr_b16 a[158:159], v241 offset:11264
ds_read_b64_tr_b16 a[160:161], v242 offset:12288
ds_read_b64_tr_b16 a[162:163], v242 offset:13312
ds_read_b64_tr_b16 a[164:165], v241 offset:12288
ds_read_b64_tr_b16 a[166:167], v241 offset:13312
s_waitcnt vmcnt(12)
v_max3_u32 v240, v103, v107, v111
v_cmp_eq_u32 vcc, -1, v240
s_cbranch_vccnz CK27
CK27C:
v_mfma_f32_32x32x16_bf16 v[20:35], a[4:7], v[100:103], 0
v_mul_f32 v222, v52, v224
v_mul_f32 v223, v53, v224
v_mul_f32 v220, v54, v224
v_mfma_f32_32x32x16_bf16 v[4:19], a[0:3], v[100:103], 0
buffer_load_dwordx4 v[148:151], v1, s[44:47], s11 offen offset:2048 sc1
v_mul_f32 v221, v55, v224
v_exp_f32 v222, v222
v_exp_f32 v223, v223
v_mfma_f32_32x32x16_bf16 v[84:99], a[4:7], v[104:107], 0
v_exp_f32 v220, v220
v_exp_f32 v221, v221
v_fma_f32 v222, v222, v228, v228
v_mfma_f32_32x32x16_bf16 v[68:83], a[0:3], v[104:107], 0
buffer_load_dwordx4 v[152:155], v1, s[44:47], s16 offen offset:2048 sc1
v_fma_f32 v223, v223, v228, v228
v_fma_f32 v220, v220, v228, v228
v_fma_f32 v221, v221, v228, v228
v_mfma_f32_32x32x16_bf16 v[20:35], a[12:15], v[108:111], v[20:35]
s_waitcnt vmcnt(11)
v_max_u32 v240, v115, v119
v_cmp_eq_u32 vcc, -1, v240
v_rcp_f32 v222, v222
v_rcp_f32 v223, v223
v_mfma_f32_32x32x16_bf16 v[4:19], a[8:11], v[108:111], v[4:19]
s_add_u32 s10, s5, 0x7000
buffer_load_dwordx4 v[156:159], v1, s[44:47], s10 offen sc1
s_cbranch_vccnz CK28
CK28C:
v_rcp_f32 v220, v220
v_rcp_f32 v221, v221
v_mfma_f32_32x32x16_bf16 v[84:99], a[12:15], v[112:115], v[84:99]
v_mul_f32 v222, v52, v222
v_mul_f32 v223, v53, v223
v_mul_f32 v220, v54, v220
v_mfma_f32_32x32x16_bf16 v[68:83], a[8:11], v[112:115], v[68:83]
s_add_u32 s15, s6, 0x7000
buffer_load_dwordx4 v[160:163], v1, s[44:47], s15 offen sc1
v_mul_f32 v221, v55, v221
v_cvt_pk_bf16_f32 v184, v222, v223
v_cvt_pk_bf16_f32 v185, v220, v221
v_mfma_f32_32x32x16_bf16 v[20:35], a[20:23], v[116:119], v[20:35]
s_waitcnt vmcnt(10)
v_max_u32 v240, v123, v127
v_cmp_eq_u32 vcc, -1, v240
v_mul_f32 v222, v56, v224
v_mul_f32 v223, v57, v224
v_mfma_f32_32x32x16_bf16 v[4:19], a[16:19], v[116:119], v[4:19]
buffer_load_dwordx4 v[164:167], v1, s[44:47], s10 offen offset:1024 sc1
s_cbranch_vccnz CK29
CK29C:
v_mul_f32 v220, v58, v224
v_mul_f32 v221, v59, v224
v_mfma_f32_32x32x16_bf16 v[84:99], a[20:23], v[120:123], v[84:99]
v_exp_f32 v222, v222
v_exp_f32 v223, v223
v_exp_f32 v220, v220
v_mfma_f32_32x32x16_bf16 v[68:83], a[16:19], v[120:123], v[68:83]
buffer_load_dwordx4 v[168:171], v1, s[44:47], s15 offen offset:1024 sc1
v_exp_f32 v221, v221
v_fma_f32 v222, v222, v228, v228
v_fma_f32 v223, v223, v228, v228
v_mfma_f32_32x32x16_bf16 v[20:35], a[28:31], v[124:127], v[20:35]
s_waitcnt vmcnt(10)
v_max_u32 v240, v131, v135
v_cmp_eq_u32 vcc, -1, v240
v_fma_f32 v220, v220, v228, v228
v_fma_f32 v221, v221, v228, v228
v_mfma_f32_32x32x16_bf16 v[4:19], a[24:27], v[124:127], v[4:19]
buffer_load_dwordx4 v[172:175], v1, s[44:47], s10 offen offset:2048 sc1
s_cbranch_vccnz CK30
CK30C:
v_rcp_f32 v222, v222
v_rcp_f32 v223, v223
v_mfma_f32_32x32x16_bf16 v[84:99], a[28:31], v[128:131], v[84:99]
v_rcp_f32 v220, v220
v_rcp_f32 v221, v221
)ASM"
R"ASM(
v_mul_f32 v222, v56, v222
v_mfma_f32_32x32x16_bf16 v[68:83], a[24:27], v[128:131], v[68:83]
buffer_load_dwordx4 v[176:179], v1, s[44:47], s15 offen offset:2048 sc1
v_mul_f32 v223, v57, v223
v_mul_f32 v220, v58, v220
v_mul_f32 v221, v59, v221
v_mfma_f32_32x32x16_bf16 v[20:35], a[36:39], v[132:135], v[20:35]
s_waitcnt vmcnt(9)
v_max_u32 v240, v139, v143
v_cmp_eq_u32 vcc, -1, v240
v_cvt_pk_bf16_f32 v186, v222, v223
v_cvt_pk_bf16_f32 v187, v220, v221
v_mfma_f32_32x32x16_bf16 v[4:19], a[32:35], v[132:135], v[4:19]
buffer_load_dwordx4 v[100:103], v1, s[44:47], s10 offen offset:3072 sc1
s_cbranch_vccnz CK31
CK31C:
s_add_u32 s75, s13, 0x15000
buffer_store_dwordx4 v[184:187], v1, s[44:47], s75 offen sc1
v_mul_f32 v222, v60, v224
v_mfma_f32_32x32x16_bf16 v[84:99], a[36:39], v[136:139], v[84:99]
v_mul_f32 v223, v61, v224
v_mul_f32 v220, v62, v224
v_mul_f32 v221, v63, v224
v_mfma_f32_32x32x16_bf16 v[68:83], a[32:35], v[136:139], v[68:83]
buffer_load_dwordx4 v[104:107], v1, s[44:47], s15 offen offset:3072 sc1
v_exp_f32 v222, v222
v_exp_f32 v223, v223
v_exp_f32 v220, v220
v_mfma_f32_32x32x16_bf16 v[20:35], a[44:47], v[140:143], v[20:35]
s_waitcnt vmcnt(10)
v_max_u32 v240, v147, v151
v_cmp_eq_u32 vcc, -1, v240
v_exp_f32 v221, v221
v_fma_f32 v222, v222, v228, v228
v_mfma_f32_32x32x16_bf16 v[4:19], a[40:43], v[140:143], v[4:19]
s_add_u32 s11, s5, 0x8000
buffer_load_dwordx4 v[108:111], v1, s[44:47], s11 offen sc1
s_cbranch_vccnz CK32
CK32C:
v_fma_f32 v223, v223, v228, v228
v_fma_f32 v220, v220, v228, v228
v_mfma_f32_32x32x16_bf16 v[84:99], a[44:47], v[144:147], v[84:99]
v_fma_f32 v221, v221, v228, v228
v_rcp_f32 v222, v222
v_rcp_f32 v223, v223
v_mfma_f32_32x32x16_bf16 v[68:83], a[40:43], v[144:147], v[68:83]
s_add_u32 s16, s6, 0x8000
buffer_load_dwordx4 v[112:115], v1, s[44:47], s16 offen sc1
v_rcp_f32 v220, v220
v_rcp_f32 v221, v221
v_mul_f32 v222, v60, v222
v_mfma_f32_32x32x16_bf16 v[20:35], a[52:55], v[148:151], v[20:35]
s_waitcnt vmcnt(10)
v_max_u32 v240, v155, v159
v_cmp_eq_u32 vcc, -1, v240
v_mul_f32 v223, v61, v223
v_mul_f32 v220, v62, v220
v_mfma_f32_32x32x16_bf16 v[4:19], a[48:51], v[148:151], v[4:19]
buffer_load_dwordx4 v[116:119], v1, s[44:47], s11 offen offset:1024 sc1
s_cbranch_vccnz CK33
CK33C:
v_mul_f32 v221, v63, v221
v_cvt_pk_bf16_f32 v188, v222, v223
v_mfma_f32_32x32x16_bf16 v[84:99], a[52:55], v[152:155], v[84:99]
v_cvt_pk_bf16_f32 v189, v220, v221
v_mul_f32 v222, v64, v224
v_mul_f32 v223, v65, v224
v_mfma_f32_32x32x16_bf16 v[68:83], a[48:51], v[152:155], v[68:83]
buffer_load_dwordx4 v[120:123], v1, s[44:47], s16 offen offset:1024 sc1
v_mul_f32 v220, v66, v224
v_mul_f32 v221, v67, v224
v_exp_f32 v222, v222
v_mfma_f32_32x32x16_bf16 v[20:35], a[60:63], v[156:159], v[20:35]
s_waitcnt vmcnt(10)
v_max_u32 v240, v163, v167
v_cmp_eq_u32 vcc, -1, v240
v_exp_f32 v223, v223
v_exp_f32 v220, v220
v_mfma_f32_32x32x16_bf16 v[4:19], a[56:59], v[156:159], v[4:19]
buffer_load_dwordx4 v[124:127], v1, s[44:47], s11 offen offset:2048 sc1
s_cbranch_vccnz CK34
CK34C:
v_exp_f32 v221, v221
v_fma_f32 v222, v222, v228, v228
v_mfma_f32_32x32x16_bf16 v[84:99], a[60:63], v[160:163], v[84:99]
v_fma_f32 v223, v223, v228, v228
v_fma_f32 v220, v220, v228, v228
v_fma_f32 v221, v221, v228, v228
v_mfma_f32_32x32x16_bf16 v[68:83], a[56:59], v[160:163], v[68:83]
buffer_load_dwordx4 v[128:131], v1, s[44:47], s16 offen offset:2048 sc1
v_rcp_f32 v222, v222
v_rcp_f32 v223, v223
v_rcp_f32 v220, v220
v_mfma_f32_32x32x16_bf16 v[20:35], a[68:71], v[164:167], v[20:35]
s_waitcnt vmcnt(10)
v_max_u32 v240, v171, v175
v_cmp_eq_u32 vcc, -1, v240
v_rcp_f32 v221, v221
v_mul_f32 v222, v64, v222
v_mfma_f32_32x32x16_bf16 v[4:19], a[64:67], v[164:167], v[4:19]
s_add_u32 s10, s5, 0xe000
buffer_load_dwordx4 v[132:135], v1, s[44:47], s10 offen sc1
s_cbranch_vccnz CK35
CK35C:
v_mul_f32 v223, v65, v223
v_mul_f32 v220, v66, v220
v_mfma_f32_32x32x16_bf16 v[84:99], a[68:71], v[168:171], v[84:99]
v_mul_f32 v221, v67, v221
v_cvt_pk_bf16_f32 v190, v222, v223
v_mfma_f32_32x32x16_bf16 v[68:83], a[64:67], v[168:171], v[68:83]
s_add_u32 s15, s6, 0xe000
buffer_load_dwordx4 v[136:139], v1, s[44:47], s15 offen sc1
v_cvt_pk_bf16_f32 v191, v220, v221
s_add_u32 s75, s13, 0x15000
buffer_store_dwordx4 v[188:191], v1, s[44:47], s75 offen offset:1024 sc1
v_mfma_f32_32x32x16_bf16 v[20:35], a[76:79], v[172:175], v[20:35]
s_waitcnt vmcnt(11)
v_max_u32 v240, v179, v103
v_cmp_eq_u32 vcc, -1, v240
v_mfma_f32_32x32x16_bf16 v[4:19], a[72:75], v[172:175], v[4:19]
buffer_load_dwordx4 v[140:143], v1, s[44:47], s10 offen offset:1024 sc1
s_cbranch_vccnz CK36
CK36C:
s_xor_b32 s71, s63, 0
s_mul_i32 s71, s71, 0x3000
v_add_u32 v254, s71, v1
v_mfma_f32_32x32x16_bf16 v[84:99], a[76:79], v[176:179], v[84:99]
v_mfma_f32_32x32x16_bf16 v[68:83], a[72:75], v[176:179], v[68:83]
buffer_load_dwordx4 v[144:147], v1, s[44:47], s15 offen offset:1024 sc1
s_xor_b32 s71, s63, 1
s_mul_i32 s71, s71, 0x3000
v_add_u32 v239, s71, v1
v_mfma_f32_32x32x16_bf16 v[20:35], a[84:87], v[100:103], v[20:35]
s_waitcnt vmcnt(10)
v_max_u32 v240, v107, v111
v_cmp_eq_u32 vcc, -1, v240
v_mfma_f32_32x32x16_bf16 v[4:19], a[80:83], v[100:103], v[4:19]
buffer_load_dwordx4 v[148:151], v1, s[44:47], s10 offen offset:2048 sc1
s_cbranch_vccnz CK37
CK37C:
s_xor_b32 s71, s63, 2
s_mul_i32 s71, s71, 0x3000
v_add_u32 v238, s71, v1
v_mfma_f32_32x32x16_bf16 v[84:99], a[84:87], v[104:107], v[84:99]
v_mfma_f32_32x32x16_bf16 v[68:83], a[80:83], v[104:107], v[68:83]
buffer_load_dwordx4 v[152:155], v1, s[44:47], s15 offen offset:2048 sc1
s_xor_b32 s71, s63, 3
s_mul_i32 s71, s71, 0x3000
v_add_u32 v236, s71, v1
v_mfma_f32_32x32x16_bf16 v[20:35], a[92:95], v[108:111], v[20:35]
s_waitcnt vmcnt(10)
v_max_u32 v240, v115, v119
v_cmp_eq_u32 vcc, -1, v240
v_mfma_f32_32x32x16_bf16 v[4:19], a[88:91], v[108:111], v[4:19]
buffer_load_dwordx4 v[156:159], v1, s[44:47], s10 offen offset:3072 sc1
s_cbranch_vccnz CK38
CK38C:
s_lshr_b32 s72, s63, 1
s_lshl_b32 s72, s72, 12
s_and_b32 s73, s63, 1
s_lshl_b32 s74, s73, 11
v_mfma_f32_32x32x16_bf16 v[84:99], a[92:95], v[112:115], v[84:99]
v_mfma_f32_32x32x16_bf16 v[68:83], a[88:91], v[112:115], v[68:83]
buffer_load_dwordx4 v[160:163], v1, s[44:47], s15 offen offset:3072 sc1
s_add_u32 s72, s72, s74
s_add_u32 s72, s72, 0x26000
v_lshlrev_b32 v235, 4, v248
v_mfma_f32_32x32x16_bf16 v[20:35], a[100:103], v[116:119], v[20:35]
s_waitcnt vmcnt(10)
v_max_u32 v240, v123, v127
v_cmp_eq_u32 vcc, -1, v240
v_mfma_f32_32x32x16_bf16 v[4:19], a[96:99], v[116:119], v[4:19]
s_add_u32 s11, s5, 0xf000
buffer_load_dwordx4 v[164:167], v1, s[44:47], s11 offen sc1
s_cbranch_vccnz CK39
CK39C:
v_lshl_add_u32 v235, v247, 3, v235
v_add_u32 v235, s72, v235
v_mfma_f32_32x32x16_bf16 v[84:99], a[100:103], v[120:123], v[84:99]
v_mfma_f32_32x32x16_bf16 v[68:83], a[96:99], v[120:123], v[68:83]
s_add_u32 s16, s6, 0xf000
buffer_load_dwordx4 v[168:171], v1, s[44:47], s16 offen sc1
v_mul_u32_u24 v234, 0x380, v248
v_lshl_add_u32 v234, v247, 4, v234
v_mfma_f32_32x32x16_bf16 v[20:35], a[108:111], v[124:127], v[20:35]
s_waitcnt vmcnt(10)
v_max_u32 v240, v131, v135
v_cmp_eq_u32 vcc, -1, v240
v_mfma_f32_32x32x16_bf16 v[4:19], a[104:107], v[124:127], v[4:19]
buffer_load_dwordx4 v[172:175], v1, s[44:47], s11 offen offset:1024 sc1
s_cbranch_vccnz CK40
CK40C:
s_lshr_b32 s72, s18, 1
s_mul_i32 s72, s72, 0x7000
s_and_b32 s73, s63, 1
v_mfma_f32_32x32x16_bf16 v[84:99], a[108:111], v[128:131], v[84:99]
v_mfma_f32_32x32x16_bf16 v[68:83], a[104:107], v[128:131], v[68:83]
buffer_load_dwordx4 v[176:179], v1, s[44:47], s16 offen offset:1024 sc1
s_lshl_b32 s73, s73, 6
s_add_u32 s72, s72, s73
s_add_u32 s19, s72, s17
s_waitcnt lgkmcnt(14)
v_mfma_f32_32x32x16_bf16 v[20:35], a[116:119], v[132:135], v[20:35]
s_waitcnt vmcnt(9)
v_max_u32 v240, v139, v143
v_cmp_eq_u32 vcc, -1, v240
v_mfma_f32_32x32x16_bf16 v[4:19], a[112:115], v[132:135], v[4:19]
buffer_load_dwordx4 v[100:103], v1, s[44:47], s11 offen offset:2048 sc1
s_cbranch_vccnz CK41
CK41C:
s_barrier
v_mfma_f32_32x32x16_bf16 v[84:99], a[116:119], v[136:139], v[84:99]
v_mfma_f32_32x32x16_bf16 v[68:83], a[112:115], v[136:139], v[68:83]
buffer_load_dwordx4 v[104:107], v1, s[44:47], s16 offen offset:2048 sc1
ds_read_b64 v[192:193], v235
ds_read_b64 v[194:195], v235 offset:512
ds_read_b64 v[196:197], v235 offset:1024
ds_read_b64 v[198:199], v235 offset:1536
v_mfma_f32_32x32x16_bf16 v[20:35], a[124:127], v[140:143], v[20:35]
s_waitcnt vmcnt(9)
v_max_u32 v240, v147, v151
v_cmp_eq_u32 vcc, -1, v240
v_mfma_f32_32x32x16_bf16 v[4:19], a[120:123], v[140:143], v[4:19]
s_add_u32 s15, s6, 0x15000
buffer_load_dwordx4 v[108:111], v1, s[44:47], s15 offen sc1
s_cbranch_vccnz CK42
CK42C:
s_waitcnt lgkmcnt(3)
v_lshlrev_b32 v36, 16, v192
v_and_b32 v37, 0xffff0000, v192
v_mfma_f32_32x32x16_bf16 v[84:99], a[124:127], v[144:147], v[84:99]
v_mfma_f32_32x32x16_bf16 v[68:83], a[120:123], v[144:147], v[68:83]
buffer_load_dwordx4 v[112:115], v1, s[44:47], s15 offen offset:1024 sc1
v_lshlrev_b32 v38, 16, v193
v_and_b32 v39, 0xffff0000, v193
v_mfma_f32_32x32x16_bf16 v[20:35], a[132:135], v[148:151], v[20:35]
s_waitcnt vmcnt(9)
v_max_u32 v240, v155, v159
v_cmp_eq_u32 vcc, -1, v240
v_mfma_f32_32x32x16_bf16 v[4:19], a[128:131], v[148:151], v[4:19]
buffer_load_dwordx4 v[116:119], v1, s[44:47], s15 offen offset:2048 sc1
s_cbranch_vccnz CK43
CK43C:
s_waitcnt lgkmcnt(2)
v_lshlrev_b32 v40, 16, v194
v_and_b32 v41, 0xffff0000, v194
v_mfma_f32_32x32x16_bf16 v[84:99], a[132:135], v[152:155], v[84:99]
v_mfma_f32_32x32x16_bf16 v[68:83], a[128:131], v[152:155], v[68:83]
buffer_load_dwordx4 v[120:123], v1, s[44:47], s15 offen offset:3072 sc1
v_lshlrev_b32 v42, 16, v195
v_and_b32 v43, 0xffff0000, v195
v_mfma_f32_32x32x16_bf16 v[20:35], a[140:143], v[156:159], v[20:35]
s_waitcnt vmcnt(9)
v_max_u32 v240, v163, v167
v_cmp_eq_u32 vcc, -1, v240
v_mfma_f32_32x32x16_bf16 v[4:19], a[136:139], v[156:159], v[4:19]
s_add_u32 s16, s6, 0x16000
buffer_load_dwordx4 v[124:127], v1, s[44:47], s16 offen sc1
s_cbranch_vccnz CK44
CK44C:
s_waitcnt lgkmcnt(1)
v_lshlrev_b32 v44, 16, v196
v_and_b32 v45, 0xffff0000, v196
v_mfma_f32_32x32x16_bf16 v[84:99], a[140:143], v[160:163], v[84:99]
v_mfma_f32_32x32x16_bf16 v[68:83], a[136:139], v[160:163], v[68:83]
buffer_load_dwordx4 v[128:131], v1, s[44:47], s16 offen offset:1024 sc1
v_lshlrev_b32 v46, 16, v197
v_and_b32 v47, 0xffff0000, v197
v_mfma_f32_32x32x16_bf16 v[20:35], a[148:151], v[164:167], v[20:35]
s_waitcnt vmcnt(9)
v_max_u32 v240, v171, v175
v_cmp_eq_u32 vcc, -1, v240
v_mfma_f32_32x32x16_bf16 v[4:19], a[144:147], v[164:167], v[4:19]
buffer_load_dwordx4 v[132:135], v1, s[44:47], s16 offen offset:2048 sc1
s_cbranch_vccnz CK45
CK45C:
s_waitcnt lgkmcnt(0)
v_lshlrev_b32 v48, 16, v198
v_and_b32 v49, 0xffff0000, v198
v_mfma_f32_32x32x16_bf16 v[84:99], a[148:151], v[168:171], v[84:99]
v_mfma_f32_32x32x16_bf16 v[68:83], a[144:147], v[168:171], v[68:83]
s_add_u32 s10, s5, 0x15000
buffer_load_dwordx4 v[136:139], v1, s[44:47], s10 offen sc1
v_lshlrev_b32 v50, 16, v199
v_and_b32 v51, 0xffff0000, v199
v_mfma_f32_32x32x16_bf16 v[20:35], a[156:159], v[172:175], v[20:35]
s_waitcnt vmcnt(9)
v_max_u32 v240, v179, v103
v_cmp_eq_u32 vcc, -1, v240
v_mfma_f32_32x32x16_bf16 v[4:19], a[152:155], v[172:175], v[4:19]
buffer_load_dwordx4 v[140:143], v1, s[44:47], s10 offen offset:1024 sc1
s_cbranch_vccnz CK46
CK46C:
v_mfma_f32_32x32x16_bf16 v[84:99], a[156:159], v[176:179], v[84:99]
v_mfma_f32_32x32x16_bf16 v[68:83], a[152:155], v[176:179], v[68:83]
buffer_load_dwordx4 v[144:147], v1, s[44:47], s10 offen offset:2048 sc1
v_mfma_f32_32x32x16_bf16 v[20:35], a[164:167], v[100:103], v[20:35]
s_waitcnt vmcnt(9)
v_max_u32 v240, v107, v111
v_cmp_eq_u32 vcc, -1, v240
v_mfma_f32_32x32x16_bf16 v[4:19], a[160:163], v[100:103], v[4:19]
buffer_load_dwordx4 v[148:151], v1, s[44:47], s10 offen offset:3072 sc1
s_cbranch_vccnz CK47
CK47C:
v_mfma_f32_32x32x16_bf16 v[84:99], a[164:167], v[104:107], v[84:99]
v_mfma_f32_32x32x16_bf16 v[68:83], a[160:163], v[104:107], v[68:83]
s_add_u32 s11, s5, 0x16000
buffer_load_dwordx4 v[152:155], v1, s[44:47], s11 offen sc1
v_mfma_f32_32x32x16_bf16 v[84:99], a[172:175], v[108:111], v[84:99]
s_waitcnt vmcnt(9)
v_max_u32 v240, v115, v119
v_cmp_eq_u32 vcc, -1, v240
v_mfma_f32_32x32x16_bf16 v[68:83], a[168:171], v[108:111], v[68:83]
buffer_load_dwordx4 v[156:159], v1, s[44:47], s11 offen offset:1024 sc1
s_cbranch_vccnz CK48
CK48C:
v_mfma_f32_32x32x16_bf16 v[84:99], a[180:183], v[112:115], v[84:99]
v_mfma_f32_32x32x16_bf16 v[68:83], a[176:179], v[112:115], v[68:83]
buffer_load_dwordx4 v[160:163], v1, s[44:47], s11 offen offset:2048 sc1
v_mfma_f32_32x32x16_bf16 v[84:99], a[188:191], v[116:119], v[84:99]
s_waitcnt vmcnt(9)
v_max_u32 v240, v123, v127
v_cmp_eq_u32 vcc, -1, v240
v_mfma_f32_32x32x16_bf16 v[68:83], a[184:187], v[116:119], v[68:83]
s_cbranch_vccnz CK49
CK49C:
v_mfma_f32_32x32x16_bf16 v[84:99], a[196:199], v[120:123], v[84:99]
v_mfma_f32_32x32x16_bf16 v[68:83], a[192:195], v[120:123], v[68:83]
v_mfma_f32_32x32x16_bf16 v[84:99], a[204:207], v[124:127], v[84:99]
s_waitcnt vmcnt(7)
v_max_u32 v240, v131, v135
v_cmp_eq_u32 vcc, -1, v240
v_mfma_f32_32x32x16_bf16 v[68:83], a[200:203], v[124:127], v[68:83]
s_cbranch_vccnz CK50
CK50C:
v_mfma_f32_32x32x16_bf16 v[84:99], a[212:215], v[128:131], v[84:99]
v_mfma_f32_32x32x16_bf16 v[68:83], a[208:211], v[128:131], v[68:83]
v_mfma_f32_32x32x16_bf16 v[84:99], a[220:223], v[132:135], v[84:99]
s_waitcnt vmcnt(5)
v_max_u32 v240, v139, v143
v_cmp_eq_u32 vcc, -1, v240
v_mfma_f32_32x32x16_bf16 v[68:83], a[216:219], v[132:135], v[68:83]
s_cbranch_vccnz CK51
CK51C:
v_mfma_f32_32x32x16_bf16 v[20:35], a[172:175], v[136:139], v[20:35]
v_mfma_f32_32x32x16_bf16 v[4:19], a[168:171], v[136:139], v[4:19]
v_mfma_f32_32x32x16_bf16 v[20:35], a[180:183], v[140:143], v[20:35]
s_waitcnt vmcnt(3)
v_max_u32 v240, v147, v151
v_cmp_eq_u32 vcc, -1, v240
v_mfma_f32_32x32x16_bf16 v[4:19], a[176:179], v[140:143], v[4:19]
s_cbranch_vccnz CK52
CK52C:
v_mfma_f32_32x32x16_bf16 v[20:35], a[188:191], v[144:147], v[20:35]
s_nop 1
v_cmp_u_f32 vcc, v68, v68
s_cbranch_vccnz REDO
v_mfma_f32_32x32x16_bf16 v[4:19], a[184:187], v[144:147], v[4:19]
ds_write_b128 v238, v[68:71] offset:4096
v_mfma_f32_32x32x16_bf16 v[20:35], a[196:199], v[148:151], v[20:35]
s_waitcnt vmcnt(1)
v_max_u32 v240, v155, v159
v_cmp_eq_u32 vcc, -1, v240
ds_write_b128 v238, v[72:75] offset:5120
v_mfma_f32_32x32x16_bf16 v[4:19], a[192:195], v[148:151], v[4:19]
s_cbranch_vccnz CK53
CK53C:
ds_write_b128 v238, v[76:79] offset:6144
v_mfma_f32_32x32x16_bf16 v[20:35], a[204:207], v[152:155], v[20:35]
ds_write_b128 v238, v[80:83] offset:7168
v_mfma_f32_32x32x16_bf16 v[4:19], a[200:203], v[152:155], v[4:19]
ds_write_b128 v236, v[84:87] offset:8192
v_mfma_f32_32x32x16_bf16 v[20:35], a[212:215], v[156:159], v[20:35]
s_waitcnt vmcnt(0)
v_mov_b32 v240, v163
v_cmp_eq_u32 vcc, -1, v240
ds_write_b128 v236, v[88:91] offset:9216
v_mfma_f32_32x32x16_bf16 v[4:19], a[208:211], v[156:159], v[4:19]
s_cbranch_vccnz CK54
CK54C:
ds_write_b128 v236, v[92:95] offset:10240
v_mfma_f32_32x32x16_bf16 v[20:35], a[220:223], v[160:163], v[20:35]
ds_write_b128 v236, v[96:99] offset:11264
v_mfma_f32_32x32x16_bf16 v[4:19], a[216:219], v[160:163], v[4:19]
s_nop 9
ds_write_b128 v239, v[20:23]
ds_write_b128 v239, v[24:27] offset:1024
ds_write_b128 v239, v[28:31] offset:2048
ds_write_b128 v239, v[32:35] offset:3072
v_cmp_u_f32 vcc, v4, v4
s_cbranch_vccnz REDO
REDOB:
v_pk_add_f32 v[4:5], v[4:5], v[36:37]
v_pk_add_f32 v[6:7], v[6:7], v[38:39]
v_pk_add_f32 v[8:9], v[8:9], v[40:41]
v_pk_add_f32 v[10:11], v[10:11], v[42:43]
v_pk_add_f32 v[12:13], v[12:13], v[44:45]
v_pk_add_f32 v[14:15], v[14:15], v[46:47]
v_pk_add_f32 v[16:17], v[16:17], v[48:49]
v_pk_add_f32 v[18:19], v[18:19], v[50:51]
)ASM"
R"ASM(
s_waitcnt lgkmcnt(0)
s_barrier
ds_read_b128 v[52:55], v254
ds_read_b128 v[100:103], v254 offset:4096
ds_read_b128 v[116:119], v254 offset:8192
ds_read_b128 v[56:59], v254 offset:1024
ds_read_b128 v[104:107], v254 offset:5120
ds_read_b128 v[120:123], v254 offset:9216
ds_read_b128 v[60:63], v254 offset:2048
ds_read_b128 v[108:111], v254 offset:6144
ds_read_b128 v[124:127], v254 offset:10240
ds_read_b128 v[64:67], v254 offset:3072
ds_read_b128 v[112:115], v254 offset:7168
ds_read_b128 v[128:131], v254 offset:11264
s_waitcnt lgkmcnt(10)
v_pk_add_f32 v[100:101], v[100:101], v[52:53]
v_pk_add_f32 v[102:103], v[102:103], v[54:55]
s_waitcnt lgkmcnt(9)
v_pk_add_f32 v[4:5], v[4:5], v[116:117]
v_pk_add_f32 v[6:7], v[6:7], v[118:119]
v_pk_add_f32 v[4:5], v[4:5], v[100:101]
v_pk_add_f32 v[6:7], v[6:7], v[102:103]
v_cvt_pk_bf16_f32 v184, v4, v5
v_cvt_pk_bf16_f32 v185, v6, v7
s_waitcnt lgkmcnt(7)
v_pk_add_f32 v[104:105], v[104:105], v[56:57]
v_pk_add_f32 v[106:107], v[106:107], v[58:59]
s_waitcnt lgkmcnt(6)
v_pk_add_f32 v[8:9], v[8:9], v[120:121]
v_pk_add_f32 v[10:11], v[10:11], v[122:123]
v_pk_add_f32 v[8:9], v[8:9], v[104:105]
v_pk_add_f32 v[10:11], v[10:11], v[106:107]
v_cvt_pk_bf16_f32 v186, v8, v9
v_cvt_pk_bf16_f32 v187, v10, v11
s_nop 0
v_permlane32_swap_b32 v184, v186
v_permlane32_swap_b32 v185, v187
buffer_store_dwordx4 v[184:187], v234, s[40:43], s19 offen
s_waitcnt lgkmcnt(4)
v_pk_add_f32 v[108:109], v[108:109], v[60:61]
v_pk_add_f32 v[110:111], v[110:111], v[62:63]
s_waitcnt lgkmcnt(3)
v_pk_add_f32 v[12:13], v[12:13], v[124:125]
v_pk_add_f32 v[14:15], v[14:15], v[126:127]
v_pk_add_f32 v[12:13], v[12:13], v[108:109]
v_pk_add_f32 v[14:15], v[14:15], v[110:111]
v_cvt_pk_bf16_f32 v188, v12, v13
v_cvt_pk_bf16_f32 v189, v14, v15
s_waitcnt lgkmcnt(1)
v_pk_add_f32 v[112:113], v[112:113], v[64:65]
v_pk_add_f32 v[114:115], v[114:115], v[66:67]
s_waitcnt lgkmcnt(0)
v_pk_add_f32 v[16:17], v[16:17], v[128:129]
v_pk_add_f32 v[18:19], v[18:19], v[130:131]
v_pk_add_f32 v[16:17], v[16:17], v[112:113]
v_pk_add_f32 v[18:19], v[18:19], v[114:115]
v_cvt_pk_bf16_f32 v190, v16, v17
v_cvt_pk_bf16_f32 v191, v18, v19
s_nop 0
v_permlane32_swap_b32 v188, v190
v_permlane32_swap_b32 v189, v191
buffer_store_dwordx4 v[188:191], v234, s[40:43], s19 offen offset:32
s_cmp_eq_u32 s62, 0
s_cselect_b32 s54, 0xf6a000, 0
v_mov_b32 v253, s70
buffer_store_dword v253, v214, s[52:55], 0 offen sc1
DONE:
s_endpgm
POLLA:
s_sleep 1
s_lshl_b32 s71, s65, 12
s_add_u32 s71, s71, 0x24000
buffer_load_dword v235, v241, s[24:27], s71 offen sc1
s_waitcnt vmcnt(0)
s_sub_u32 s9, s9, 1
s_cbranch_scc1 POLLAG
v_cmp_ne_u32 vcc, s10, v235
s_cbranch_vccnz POLLA
s_branch POLLBACK
POLLAG:
s_mov_b32 s9, 0
s_branch POLLBACK
POLLR:
s_sleep 1
buffer_load_dwordx2 v[12:13], v240, s[44:47], s57 offen sc1
s_waitcnt vmcnt(0)
s_sub_u32 s9, s9, 1
s_cbranch_scc1 POLLRG
v_cmp_ne_u32 vcc, s70, v13
s_cbranch_vccnz POLLR
s_branch POLLRB
POLLRG:
s_mov_b32 s9, 0
s_branch POLLRB
CK1:
s_nop 3
s_add_u32 s75, s6, 0
buffer_load_dwordx4 v[4:7], v1, s[44:47], s75 offen sc1
buffer_load_dwordx4 v[8:11], v1, s[44:47], s75 offen offset:1024 sc1
buffer_load_dwordx4 v[28:31], v1, s[44:47], s75 offen offset:2048 sc1
buffer_load_dwordx4 v[32:35], v1, s[44:47], s75 offen offset:3072 sc1
s_waitcnt vmcnt(0)
s_sub_u32 s9, s9, 1
s_cbranch_scc1 CK1G
v_max3_u32 v240, v7, v11, v31
v_max_u32 v240, v240, v35
v_cmp_eq_u32 vcc, -1, v240
s_cbranch_vccnz CK1
s_branch CK1D
CK1G:
s_mov_b32 s9, 0
CK1D:
v_max3_u32 v240, v39, v43, v47
v_max3_u32 v240, v240, v51, v55
v_max3_u32 v240, v240, v59, v63
v_max3_u32 v240, v240, v67, v71
v_max3_u32 v240, v240, v75, v79
v_max_u32 v240, v240, v83
v_cmp_eq_u32 vcc, -1, v240
s_cbranch_vccz CK1C
s_add_u32 s75, s6, 0x1000
buffer_load_dwordx4 v[36:39], v1, s[44:47], s75 offen sc1
buffer_load_dwordx4 v[40:43], v1, s[44:47], s75 offen offset:1024 sc1
buffer_load_dword v240, off, s[44:47], 0
buffer_load_dwordx4 v[44:47], v1, s[44:47], s75 offen offset:2048 sc1
buffer_load_dwordx4 v[48:51], v1, s[44:47], s75 offen offset:3072 sc1
s_add_u32 s75, s6, 0x2000
buffer_load_dwordx4 v[52:55], v1, s[44:47], s75 offen sc1
buffer_load_dwordx4 v[56:59], v1, s[44:47], s75 offen offset:1024 sc1
buffer_load_dwordx4 v[60:63], v1, s[44:47], s75 offen offset:2048 sc1
buffer_load_dwordx4 v[64:67], v1, s[44:47], s75 offen offset:3072 sc1
s_add_u32 s75, s6, 0x3000
buffer_load_dwordx4 v[68:71], v1, s[44:47], s75 offen sc1
buffer_load_dwordx4 v[72:75], v1, s[44:47], s75 offen offset:1024 sc1
buffer_load_dwordx4 v[76:79], v1, s[44:47], s75 offen offset:2048 sc1
buffer_load_dwordx4 v[80:83], v1, s[44:47], s75 offen offset:3072 sc1
buffer_load_dword v240, off, s[44:47], 0
buffer_load_dword v240, off, s[44:47], 0
buffer_load_dword v240, off, s[44:47], 0
buffer_load_dword v240, off, s[44:47], 0
buffer_load_dword v240, off, s[44:47], 0
buffer_load_dword v240, off, s[44:47], 0
buffer_load_dword v240, off, s[44:47], 0
buffer_load_dword v240, off, s[44:47], 0
buffer_load_dword v240, off, s[44:47], 0
buffer_load_dword v240, off, s[44:47], 0
buffer_load_dword v240, off, s[44:47], 0
buffer_load_dword v240, off, s[44:47], 0
buffer_load_dword v240, off, s[44:47], 0
buffer_load_dword v240, off, s[44:47], 0
buffer_load_dword v240, off, s[44:47], 0
buffer_load_dword v240, off, s[44:47], 0
buffer_load_dword v240, off, s[44:47], 0
buffer_load_dword v240, off, s[44:47], 0
buffer_load_dword v240, off, s[44:47], 0
buffer_load_dword v240, off, s[44:47], 0
buffer_load_dword v240, off, s[44:47], 0
buffer_load_dword v240, off, s[44:47], 0
buffer_load_dword v240, off, s[44:47], 0
buffer_load_dword v240, off, s[44:47], 0
s_waitcnt vmcnt(0)
s_branch CK1C
CK2:
s_nop 3
s_add_u32 s75, s6, 0x1000
buffer_load_dwordx4 v[36:39], v1, s[44:47], s75 offen sc1
buffer_load_dwordx4 v[40:43], v1, s[44:47], s75 offen offset:1024 sc1
buffer_load_dwordx4 v[44:47], v1, s[44:47], s75 offen offset:2048 sc1
buffer_load_dwordx4 v[48:51], v1, s[44:47], s75 offen offset:3072 sc1
s_waitcnt vmcnt(0)
s_sub_u32 s9, s9, 1
s_cbranch_scc1 CK2G
v_max3_u32 v240, v39, v43, v47
v_max_u32 v240, v240, v51
v_cmp_eq_u32 vcc, -1, v240
s_cbranch_vccnz CK2
s_branch CK2D
CK2G:
s_mov_b32 s9, 0
CK2D:
v_max3_u32 v240, v55, v59, v63
v_max3_u32 v240, v240, v67, v71
v_max3_u32 v240, v240, v75, v79
v_max_u32 v240, v240, v83
v_cmp_eq_u32 vcc, -1, v240
s_cbranch_vccz CK2C
s_add_u32 s75, s6, 0x2000
buffer_load_dwordx4 v[52:55], v1, s[44:47], s75 offen sc1
buffer_load_dwordx4 v[56:59], v1, s[44:47], s75 offen offset:1024 sc1
buffer_load_dwordx4 v[60:63], v1, s[44:47], s75 offen offset:2048 sc1
buffer_load_dwordx4 v[64:67], v1, s[44:47], s75 offen offset:3072 sc1
s_add_u32 s75, s6, 0x3000
buffer_load_dwordx4 v[68:71], v1, s[44:47], s75 offen sc1
buffer_load_dwordx4 v[72:75], v1, s[44:47], s75 offen offset:1024 sc1
buffer_load_dwordx4 v[76:79], v1, s[44:47], s75 offen offset:2048 sc1
buffer_load_dwordx4 v[80:83], v1, s[44:47], s75 offen offset:3072 sc1
buffer_load_dword v240, off, s[44:47], 0
buffer_load_dword v240, off, s[44:47], 0
buffer_load_dword v240, off, s[44:47], 0
buffer_load_dword v240, off, s[44:47], 0
buffer_load_dword v240, off, s[44:47], 0
buffer_load_dword v240, off, s[44:47], 0
buffer_load_dword v240, off, s[44:47], 0
buffer_load_dword v240, off, s[44:47], 0
buffer_load_dword v240, off, s[44:47], 0
buffer_load_dword v240, off, s[44:47], 0
buffer_load_dword v240, off, s[44:47], 0
buffer_load_dword v240, off, s[44:47], 0
buffer_load_dword v240, off, s[44:47], 0
buffer_load_dword v240, off, s[44:47], 0
buffer_load_dword v240, off, s[44:47], 0
buffer_load_dword v240, off, s[44:47], 0
buffer_load_dword v240, off, s[44:47], 0
buffer_load_dword v240, off, s[44:47], 0
buffer_load_dword v240, off, s[44:47], 0
buffer_load_dword v240, off, s[44:47], 0
buffer_load_dword v240, off, s[44:47], 0
buffer_load_dword v240, off, s[44:47], 0
buffer_load_dword v240, off, s[44:47], 0
buffer_load_dword v240, off, s[44:47], 0
buffer_load_dword v240, off, s[44:47], 0
buffer_load_dword v240, off, s[44:47], 0
s_waitcnt vmcnt(0)
s_branch CK2C
CK3:
s_nop 3
s_add_u32 s75, s6, 0x2000
buffer_load_dwordx4 v[52:55], v1, s[44:47], s75 offen sc1
buffer_load_dwordx4 v[56:59], v1, s[44:47], s75 offen offset:1024 sc1
buffer_load_dwordx4 v[60:63], v1, s[44:47], s75 offen offset:2048 sc1
buffer_load_dwordx4 v[64:67], v1, s[44:47], s75 offen offset:3072 sc1
s_waitcnt vmcnt(0)
s_sub_u32 s9, s9, 1
s_cbranch_scc1 CK3G
v_max3_u32 v240, v55, v59, v63
v_max_u32 v240, v240, v67
v_cmp_eq_u32 vcc, -1, v240
s_cbranch_vccnz CK3
s_branch CK3D
CK3G:
s_mov_b32 s9, 0
CK3D:
v_max3_u32 v240, v71, v75, v79
v_max_u32 v240, v240, v83
v_cmp_eq_u32 vcc, -1, v240
s_cbranch_vccz CK3C
s_add_u32 s75, s6, 0x3000
buffer_load_dwordx4 v[68:71], v1, s[44:47], s75 offen sc1
buffer_load_dwordx4 v[72:75], v1, s[44:47], s75 offen offset:1024 sc1
buffer_load_dwordx4 v[76:79], v1, s[44:47], s75 offen offset:2048 sc1
buffer_load_dwordx4 v[80:83], v1, s[44:47], s75 offen offset:3072 sc1
buffer_load_dword v240, off, s[44:47], 0
buffer_load_dword v240, off, s[44:47], 0
buffer_load_dword v240, off, s[44:47], 0
buffer_load_dword v240, off, s[44:47], 0
buffer_load_dword v240, off, s[44:47], 0
buffer_load_dword v240, off, s[44:47], 0
buffer_load_dword v240, off, s[44:47], 0
buffer_load_dword v240, off, s[44:47], 0
buffer_load_dword v240, off, s[44:47], 0
buffer_load_dword v240, off, s[44:47], 0
buffer_load_dword v240, off, s[44:47], 0
buffer_load_dword v240, off, s[44:47], 0
buffer_load_dword v240, off, s[44:47], 0
buffer_load_dword v240, off, s[44:47], 0
buffer_load_dword v240, off, s[44:47], 0
buffer_load_dword v240, off, s[44:47], 0
buffer_load_dword v240, off, s[44:47], 0
buffer_load_dword v240, off, s[44:47], 0
buffer_load_dword v240, off, s[44:47], 0
buffer_load_dword v240, off, s[44:47], 0
buffer_load_dword v240, off, s[44:47], 0
buffer_load_dword v240, off, s[44:47], 0
buffer_load_dword v240, off, s[44:47], 0
buffer_load_dword v240, off, s[44:47], 0
buffer_load_dword v240, off, s[44:47], 0
buffer_load_dword v240, off, s[44:47], 0
buffer_load_dword v240, off, s[44:47], 0
buffer_load_dword v240, off, s[44:47], 0
s_waitcnt vmcnt(0)
s_branch CK3C
CK4:
s_nop 3
s_add_u32 s75, s6, 0x3000
buffer_load_dwordx4 v[68:71], v1, s[44:47], s75 offen sc1
buffer_load_dwordx4 v[72:75], v1, s[44:47], s75 offen offset:1024 sc1
s_waitcnt vmcnt(0)
s_sub_u32 s9, s9, 1
s_cbranch_scc1 CK4G
v_max_u32 v240, v71, v75
v_cmp_eq_u32 vcc, -1, v240
s_cbranch_vccnz CK4
s_branch CK4D
CK4G:
s_mov_b32 s9, 0
CK4D:
v_max_u32 v240, v79, v83
v_cmp_eq_u32 vcc, -1, v240
s_cbranch_vccz CK4C
s_add_u32 s75, s6, 0x3000
buffer_load_dwordx4 v[76:79], v1, s[44:47], s75 offen offset:2048 sc1
buffer_load_dwordx4 v[80:83], v1, s[44:47], s75 offen offset:3072 sc1
buffer_load_dword v240, off, s[44:47], 0
buffer_load_dword v240, off, s[44:47], 0
buffer_load_dword v240, off, s[44:47], 0
buffer_load_dword v240, off, s[44:47], 0
buffer_load_dword v240, off, s[44:47], 0
buffer_load_dword v240, off, s[44:47], 0
buffer_load_dword v240, off, s[44:47], 0
buffer_load_dword v240, off, s[44:47], 0
buffer_load_dword v240, off, s[44:47], 0
buffer_load_dword v240, off, s[44:47], 0
buffer_load_dword v240, off, s[44:47], 0
buffer_load_dword v240, off, s[44:47], 0
buffer_load_dword v240, off, s[44:47], 0
buffer_load_dword v240, off, s[44:47], 0
buffer_load_dword v240, off, s[44:47], 0
buffer_load_dword v240, off, s[44:47], 0
buffer_load_dword v240, off, s[44:47], 0
buffer_load_dword v240, off, s[44:47], 0
buffer_load_dword v240, off, s[44:47], 0
buffer_load_dword v240, off, s[44:47], 0
buffer_load_dword v240, off, s[44:47], 0
buffer_load_dword v240, off, s[44:47], 0
buffer_load_dword v240, off, s[44:47], 0
buffer_load_dword v240, off, s[44:47], 0
buffer_load_dword v240, off, s[44:47], 0
buffer_load_dword v240, off, s[44:47], 0
buffer_load_dword v240, off, s[44:47], 0
buffer_load_dword v240, off, s[44:47], 0
buffer_load_dword v240, off, s[44:47], 0
buffer_load_dword v240, off, s[44:47], 0
s_waitcnt vmcnt(0)
s_branch CK4C
CK5:
s_nop 3
s_add_u32 s75, s6, 0x3000
buffer_load_dwordx4 v[76:79], v1, s[44:47], s75 offen offset:2048 sc1
buffer_load_dwordx4 v[80:83], v1, s[44:47], s75 offen offset:3072 sc1
s_waitcnt vmcnt(0)
s_sub_u32 s9, s9, 1
s_cbranch_scc1 CK5G
v_max_u32 v240, v79, v83
v_cmp_eq_u32 vcc, -1, v240
s_cbranch_vccnz CK5
s_branch CK5D
CK5G:
s_mov_b32 s9, 0
CK5D:
s_branch CK5C
POLLG:
s_sleep 1
s_mov_b32 s76, 0x34000
buffer_load_dword v235, v215, s[24:27], s76 offen sc1
s_waitcnt vmcnt(0)
s_sub_u32 s9, s9, 1
s_cbranch_scc1 POLLGG
v_cmp_ne_u32 vcc, s10, v235
s_cbranch_vccnz POLLG
s_branch POLLGB
POLLGG:
s_mov_b32 s9, 0
s_branch POLLGB
SPARE:
s_cmp_ge_u32 s66, 14
s_cbranch_scc1 DONE
s_cmp_ge_u32 s63, 2
s_cbranch_scc1 DONE
s_lshl_b32 s71, s66, 1
s_add_u32 s71, s71, s67
s_sub_u32 s71, s71, 14
s_mul_i32 s71, s71, 0xe000
s_add_u32 s71, s71, 0xd80000
s_add_u32 s71, s71, s88
buffer_load_dwordx4 v[24:27], v1, s[44:47], s71 offen sc1
buffer_load_dwordx4 v[28:31], v1, s[44:47], s71 offen offset:2048 sc1
s_add_u32 s71, s71, 0x1000
buffer_load_dwordx4 v[32:35], v1, s[44:47], s71 offen sc1
buffer_load_dwordx4 v[36:39], v1, s[44:47], s71 offen offset:2048 sc1
s_add_u32 s71, s71, 0x1000
buffer_load_dwordx4 v[40:43], v1, s[44:47], s71 offen sc1
buffer_load_dwordx4 v[44:47], v1, s[44:47], s71 offen offset:2048 sc1
s_add_u32 s71, s71, 0x1000
buffer_load_dwordx4 v[48:51], v1, s[44:47], s71 offen sc1
buffer_load_dwordx4 v[52:55], v1, s[44:47], s71 offen offset:2048 sc1
s_add_u32 s71, s71, 0x1000
buffer_load_dwordx4 v[56:59], v1, s[44:47], s71 offen sc1
buffer_load_dwordx4 v[60:63], v1, s[44:47], s71 offen offset:2048 sc1
s_add_u32 s71, s71, 0x1000
buffer_load_dwordx4 v[64:67], v1, s[44:47], s71 offen sc1
buffer_load_dwordx4 v[68:71], v1, s[44:47], s71 offen offset:2048 sc1
s_add_u32 s71, s71, 0x1000
buffer_load_dwordx4 v[72:75], v1, s[44:47], s71 offen sc1
buffer_load_dwordx4 v[76:79], v1, s[44:47], s71 offen offset:2048 sc1
s_add_u32 s71, s71, 0x1000
buffer_load_dwordx4 v[80:83], v1, s[44:47], s71 offen sc1
buffer_load_dwordx4 v[84:87], v1, s[44:47], s71 offen offset:2048 sc1
s_add_u32 s71, s71, 0x1000
buffer_load_dwordx4 v[88:91], v1, s[44:47], s71 offen sc1
buffer_load_dwordx4 v[92:95], v1, s[44:47], s71 offen offset:2048 sc1
)ASM"
R"ASM(
s_add_u32 s71, s71, 0x1000
buffer_load_dwordx4 v[96:99], v1, s[44:47], s71 offen sc1
buffer_load_dwordx4 v[100:103], v1, s[44:47], s71 offen offset:2048 sc1
s_add_u32 s71, s71, 0x1000
buffer_load_dwordx4 v[104:107], v1, s[44:47], s71 offen sc1
buffer_load_dwordx4 v[116:119], v1, s[44:47], s71 offen offset:2048 sc1
s_add_u32 s71, s71, 0x1000
buffer_load_dwordx4 v[120:123], v1, s[44:47], s71 offen sc1
buffer_load_dwordx4 v[124:127], v1, s[44:47], s71 offen offset:2048 sc1
s_add_u32 s71, s71, 0x1000
buffer_load_dwordx4 v[128:131], v1, s[44:47], s71 offen sc1
buffer_load_dwordx4 v[132:135], v1, s[44:47], s71 offen offset:2048 sc1
s_add_u32 s71, s71, 0x1000
buffer_load_dwordx4 v[136:139], v1, s[44:47], s71 offen sc1
buffer_load_dwordx4 v[140:143], v1, s[44:47], s71 offen offset:2048 sc1
s_waitcnt vmcnt(27)
v_mfma_f32_32x32x16_bf16 v[4:19], v[20:23], v[24:27], 0
s_waitcnt vmcnt(26)
v_mfma_f32_32x32x16_bf16 v[4:19], v[20:23], v[28:31], v[4:19]
s_waitcnt vmcnt(25)
v_mfma_f32_32x32x16_bf16 v[4:19], v[20:23], v[32:35], v[4:19]
s_waitcnt vmcnt(24)
v_mfma_f32_32x32x16_bf16 v[4:19], v[20:23], v[36:39], v[4:19]
s_waitcnt vmcnt(23)
v_mfma_f32_32x32x16_bf16 v[4:19], v[20:23], v[40:43], v[4:19]
s_waitcnt vmcnt(22)
v_mfma_f32_32x32x16_bf16 v[4:19], v[20:23], v[44:47], v[4:19]
s_waitcnt vmcnt(21)
v_mfma_f32_32x32x16_bf16 v[4:19], v[20:23], v[48:51], v[4:19]
s_waitcnt vmcnt(20)
v_mfma_f32_32x32x16_bf16 v[4:19], v[20:23], v[52:55], v[4:19]
s_waitcnt vmcnt(19)
v_mfma_f32_32x32x16_bf16 v[4:19], v[20:23], v[56:59], v[4:19]
s_waitcnt vmcnt(18)
v_mfma_f32_32x32x16_bf16 v[4:19], v[20:23], v[60:63], v[4:19]
s_waitcnt vmcnt(17)
v_mfma_f32_32x32x16_bf16 v[4:19], v[20:23], v[64:67], v[4:19]
s_waitcnt vmcnt(16)
v_mfma_f32_32x32x16_bf16 v[4:19], v[20:23], v[68:71], v[4:19]
s_waitcnt vmcnt(15)
v_mfma_f32_32x32x16_bf16 v[4:19], v[20:23], v[72:75], v[4:19]
s_waitcnt vmcnt(14)
v_mfma_f32_32x32x16_bf16 v[4:19], v[20:23], v[76:79], v[4:19]
s_waitcnt vmcnt(13)
v_mfma_f32_32x32x16_bf16 v[4:19], v[20:23], v[80:83], v[4:19]
s_waitcnt vmcnt(12)
v_mfma_f32_32x32x16_bf16 v[4:19], v[20:23], v[84:87], v[4:19]
s_waitcnt vmcnt(11)
v_mfma_f32_32x32x16_bf16 v[4:19], v[20:23], v[88:91], v[4:19]
s_waitcnt vmcnt(10)
v_mfma_f32_32x32x16_bf16 v[4:19], v[20:23], v[92:95], v[4:19]
s_waitcnt vmcnt(9)
v_mfma_f32_32x32x16_bf16 v[4:19], v[20:23], v[96:99], v[4:19]
s_waitcnt vmcnt(8)
v_mfma_f32_32x32x16_bf16 v[4:19], v[20:23], v[100:103], v[4:19]
s_waitcnt vmcnt(7)
v_mfma_f32_32x32x16_bf16 v[4:19], v[20:23], v[104:107], v[4:19]
s_waitcnt vmcnt(6)
v_mfma_f32_32x32x16_bf16 v[4:19], v[20:23], v[116:119], v[4:19]
s_waitcnt vmcnt(5)
v_mfma_f32_32x32x16_bf16 v[4:19], v[20:23], v[120:123], v[4:19]
s_waitcnt vmcnt(4)
v_mfma_f32_32x32x16_bf16 v[4:19], v[20:23], v[124:127], v[4:19]
s_waitcnt vmcnt(3)
v_mfma_f32_32x32x16_bf16 v[4:19], v[20:23], v[128:131], v[4:19]
s_waitcnt vmcnt(2)
v_mfma_f32_32x32x16_bf16 v[4:19], v[20:23], v[132:135], v[4:19]
s_waitcnt vmcnt(1)
v_mfma_f32_32x32x16_bf16 v[4:19], v[20:23], v[136:139], v[4:19]
s_waitcnt vmcnt(0)
v_mfma_f32_32x32x16_bf16 v[4:19], v[20:23], v[140:143], v[4:19]
s_nop 11
v_cvt_pk_bf16_f32 v252, v4, 0
v_lshlrev_b32 v253, 16, v252
v_sub_f32 v253, v4, v253
v_cvt_pk_bf16_f32 v253, v253, 0
v_and_b32 v252, 0xffff, v252
v_lshl_or_b32 v252, v253, 16, v252
buffer_store_dword v252, v237, s[52:55], s15 offen sc1
s_branch DONE
CK7:
s_nop 3
s_add_u32 s4, s7, 0
buffer_load_dwordx4 v[20:23], v1, s[44:47], s4 offen sc1
buffer_load_dwordx4 v[24:27], v1, s[44:47], s4 offen offset:1024 sc1
s_waitcnt vmcnt(0)
s_sub_u32 s9, s9, 1
s_cbranch_scc1 CK7G
v_max_u32 v240, v23, v27
v_cmp_eq_u32 vcc, -1, v240
s_cbranch_vccnz CK7
s_branch CK7D
CK7G:
s_mov_b32 s9, 0
CK7D:
v_max3_u32 v240, v31, v35, v39
v_max3_u32 v240, v240, v43, v47
v_max3_u32 v240, v240, v51, v55
v_max3_u32 v240, v240, v59, v63
v_max3_u32 v240, v240, v67, v71
v_cmp_eq_u32 vcc, -1, v240
s_cbranch_vccz CK7C
s_add_u32 s4, s7, 0x800
buffer_load_dwordx4 v[28:31], v1, s[44:47], s4 offen sc1
buffer_load_dwordx4 v[32:35], v1, s[44:47], s4 offen offset:1024 sc1
s_add_u32 s4, s7, 0x1000
buffer_load_dwordx4 v[36:39], v1, s[44:47], s4 offen sc1
buffer_load_dwordx4 v[40:43], v1, s[44:47], s4 offen offset:1024 sc1
s_add_u32 s4, s7, 0x1800
buffer_load_dwordx4 v[44:47], v1, s[44:47], s4 offen sc1
buffer_load_dwordx4 v[48:51], v1, s[44:47], s4 offen offset:1024 sc1
s_add_u32 s4, s7, 0x2000
buffer_load_dwordx4 v[52:55], v1, s[44:47], s4 offen sc1
buffer_load_dwordx4 v[56:59], v1, s[44:47], s4 offen offset:1024 sc1
s_add_u32 s4, s7, 0x2800
buffer_load_dwordx4 v[60:63], v1, s[44:47], s4 offen sc1
buffer_load_dwordx4 v[64:67], v1, s[44:47], s4 offen offset:1024 sc1
s_add_u32 s4, s7, 0x3000
buffer_load_dwordx4 v[68:71], v1, s[44:47], s4 offen sc1
s_branch CK7C
CK8:
s_nop 3
s_add_u32 s4, s7, 0x800
buffer_load_dwordx4 v[28:31], v1, s[44:47], s4 offen sc1
buffer_load_dwordx4 v[32:35], v1, s[44:47], s4 offen offset:1024 sc1
s_waitcnt vmcnt(0)
s_sub_u32 s9, s9, 1
s_cbranch_scc1 CK8G
v_max_u32 v240, v31, v35
v_cmp_eq_u32 vcc, -1, v240
s_cbranch_vccnz CK8
s_branch CK8D
CK8G:
s_mov_b32 s9, 0
CK8D:
v_max3_u32 v240, v39, v43, v47
v_max3_u32 v240, v240, v51, v55
v_max3_u32 v240, v240, v59, v63
v_max3_u32 v240, v240, v67, v71
v_max3_u32 v240, v240, v75, v79
v_cmp_eq_u32 vcc, -1, v240
s_cbranch_vccz CK8C
s_add_u32 s4, s7, 0x1000
buffer_load_dwordx4 v[36:39], v1, s[44:47], s4 offen sc1
buffer_load_dwordx4 v[40:43], v1, s[44:47], s4 offen offset:1024 sc1
s_add_u32 s4, s7, 0x1800
buffer_load_dwordx4 v[44:47], v1, s[44:47], s4 offen sc1
buffer_load_dwordx4 v[48:51], v1, s[44:47], s4 offen offset:1024 sc1
s_add_u32 s4, s7, 0x2000
buffer_load_dwordx4 v[52:55], v1, s[44:47], s4 offen sc1
buffer_load_dwordx4 v[56:59], v1, s[44:47], s4 offen offset:1024 sc1
s_add_u32 s4, s7, 0x2800
buffer_load_dwordx4 v[60:63], v1, s[44:47], s4 offen sc1
buffer_load_dwordx4 v[64:67], v1, s[44:47], s4 offen offset:1024 sc1
s_add_u32 s4, s7, 0x3000
buffer_load_dwordx4 v[68:71], v1, s[44:47], s4 offen sc1
buffer_load_dwordx4 v[72:75], v1, s[44:47], s4 offen offset:1024 sc1
s_add_u32 s4, s7, 0x3800
buffer_load_dwordx4 v[76:79], v1, s[44:47], s4 offen sc1
s_branch CK8C
CK9:
s_nop 3
s_add_u32 s4, s7, 0x1000
buffer_load_dwordx4 v[36:39], v1, s[44:47], s4 offen sc1
buffer_load_dwordx4 v[40:43], v1, s[44:47], s4 offen offset:1024 sc1
s_waitcnt vmcnt(0)
s_sub_u32 s9, s9, 1
s_cbranch_scc1 CK9G
v_max_u32 v240, v39, v43
v_cmp_eq_u32 vcc, -1, v240
s_cbranch_vccnz CK9
s_branch CK9D
CK9G:
s_mov_b32 s9, 0
CK9D:
v_max3_u32 v240, v47, v51, v55
v_max3_u32 v240, v240, v59, v63
v_max3_u32 v240, v240, v67, v71
v_max3_u32 v240, v240, v75, v79
v_max_u32 v240, v240, v83
v_cmp_eq_u32 vcc, -1, v240
s_cbranch_vccz CK9C
s_add_u32 s4, s7, 0x1800
buffer_load_dwordx4 v[44:47], v1, s[44:47], s4 offen sc1
buffer_load_dwordx4 v[48:51], v1, s[44:47], s4 offen offset:1024 sc1
s_add_u32 s4, s7, 0x2000
buffer_load_dwordx4 v[52:55], v1, s[44:47], s4 offen sc1
buffer_load_dwordx4 v[56:59], v1, s[44:47], s4 offen offset:1024 sc1
s_add_u32 s4, s7, 0x2800
buffer_load_dwordx4 v[60:63], v1, s[44:47], s4 offen sc1
buffer_load_dwordx4 v[64:67], v1, s[44:47], s4 offen offset:1024 sc1
s_add_u32 s4, s7, 0x3000
buffer_load_dwordx4 v[68:71], v1, s[44:47], s4 offen sc1
buffer_load_dwordx4 v[72:75], v1, s[44:47], s4 offen offset:1024 sc1
s_add_u32 s4, s7, 0x3800
buffer_load_dwordx4 v[76:79], v1, s[44:47], s4 offen sc1
buffer_load_dwordx4 v[80:83], v1, s[44:47], s4 offen offset:1024 sc1
buffer_load_dword v240, off, s[44:47], 0
s_branch CK9C
CK10:
s_nop 3
s_add_u32 s4, s7, 0x1800
buffer_load_dwordx4 v[44:47], v1, s[44:47], s4 offen sc1
buffer_load_dwordx4 v[48:51], v1, s[44:47], s4 offen offset:1024 sc1
s_waitcnt vmcnt(0)
s_sub_u32 s9, s9, 1
s_cbranch_scc1 CK10G
v_max_u32 v240, v47, v51
v_cmp_eq_u32 vcc, -1, v240
s_cbranch_vccnz CK10
s_branch CK10D
CK10G:
s_mov_b32 s9, 0
CK10D:
v_max3_u32 v240, v55, v59, v63
v_max3_u32 v240, v240, v67, v71
v_max3_u32 v240, v240, v75, v79
v_max_u32 v240, v240, v83
v_cmp_eq_u32 vcc, -1, v240
s_cbranch_vccz CK10C
s_add_u32 s4, s7, 0x2000
buffer_load_dwordx4 v[52:55], v1, s[44:47], s4 offen sc1
buffer_load_dwordx4 v[56:59], v1, s[44:47], s4 offen offset:1024 sc1
s_add_u32 s4, s7, 0x2800
buffer_load_dwordx4 v[60:63], v1, s[44:47], s4 offen sc1
buffer_load_dwordx4 v[64:67], v1, s[44:47], s4 offen offset:1024 sc1
s_add_u32 s4, s7, 0x3000
buffer_load_dwordx4 v[68:71], v1, s[44:47], s4 offen sc1
buffer_load_dwordx4 v[72:75], v1, s[44:47], s4 offen offset:1024 sc1
s_add_u32 s4, s7, 0x3800
buffer_load_dwordx4 v[76:79], v1, s[44:47], s4 offen sc1
buffer_load_dwordx4 v[80:83], v1, s[44:47], s4 offen offset:1024 sc1
buffer_load_dword v240, off, s[44:47], 0
buffer_load_dword v240, off, s[44:47], 0
buffer_load_dword v240, off, s[44:47], 0
s_branch CK10C
CK11:
s_nop 3
s_add_u32 s4, s7, 0x2000
buffer_load_dwordx4 v[52:55], v1, s[44:47], s4 offen sc1
buffer_load_dwordx4 v[56:59], v1, s[44:47], s4 offen offset:1024 sc1
s_waitcnt vmcnt(0)
s_sub_u32 s9, s9, 1
s_cbranch_scc1 CK11G
v_max_u32 v240, v55, v59
v_cmp_eq_u32 vcc, -1, v240
s_cbranch_vccnz CK11
s_branch CK11D
CK11G:
s_mov_b32 s9, 0
CK11D:
v_max3_u32 v240, v63, v67, v71
v_max3_u32 v240, v240, v75, v79
v_max_u32 v240, v240, v83
v_cmp_eq_u32 vcc, -1, v240
s_cbranch_vccz CK11C
s_add_u32 s4, s7, 0x2800
buffer_load_dwordx4 v[60:63], v1, s[44:47], s4 offen sc1
buffer_load_dwordx4 v[64:67], v1, s[44:47], s4 offen offset:1024 sc1
s_add_u32 s4, s7, 0x3000
buffer_load_dwordx4 v[68:71], v1, s[44:47], s4 offen sc1
buffer_load_dwordx4 v[72:75], v1, s[44:47], s4 offen offset:1024 sc1
s_add_u32 s4, s7, 0x3800
buffer_load_dwordx4 v[76:79], v1, s[44:47], s4 offen sc1
buffer_load_dwordx4 v[80:83], v1, s[44:47], s4 offen offset:1024 sc1
buffer_load_dword v240, off, s[44:47], 0
buffer_load_dword v240, off, s[44:47], 0
buffer_load_dword v240, off, s[44:47], 0
buffer_load_dword v240, off, s[44:47], 0
buffer_load_dword v240, off, s[44:47], 0
s_branch CK11C
CK12:
s_nop 3
s_add_u32 s4, s7, 0x2800
buffer_load_dwordx4 v[60:63], v1, s[44:47], s4 offen sc1
buffer_load_dwordx4 v[64:67], v1, s[44:47], s4 offen offset:1024 sc1
s_waitcnt vmcnt(0)
s_sub_u32 s9, s9, 1
s_cbranch_scc1 CK12G
v_max_u32 v240, v63, v67
v_cmp_eq_u32 vcc, -1, v240
s_cbranch_vccnz CK12
s_branch CK12D
CK12G:
s_mov_b32 s9, 0
CK12D:
v_max3_u32 v240, v71, v75, v79
v_max_u32 v240, v240, v83
v_cmp_eq_u32 vcc, -1, v240
s_cbranch_vccz CK12C
s_add_u32 s4, s7, 0x3000
buffer_load_dwordx4 v[68:71], v1, s[44:47], s4 offen sc1
buffer_load_dwordx4 v[72:75], v1, s[44:47], s4 offen offset:1024 sc1
s_add_u32 s4, s7, 0x3800
buffer_load_dwordx4 v[76:79], v1, s[44:47], s4 offen sc1
buffer_load_dwordx4 v[80:83], v1, s[44:47], s4 offen offset:1024 sc1
buffer_load_dword v240, off, s[44:47], 0
buffer_load_dword v240, off, s[44:47], 0
buffer_load_dword v240, off, s[44:47], 0
buffer_load_dword v240, off, s[44:47], 0
buffer_load_dword v240, off, s[44:47], 0
buffer_load_dword v240, off, s[44:47], 0
buffer_load_dword v240, off, s[44:47], 0
s_branch CK12C
CK13:
s_nop 3
s_add_u32 s4, s7, 0x3000
buffer_load_dwordx4 v[68:71], v1, s[44:47], s4 offen sc1
buffer_load_dwordx4 v[72:75], v1, s[44:47], s4 offen offset:1024 sc1
s_waitcnt vmcnt(0)
s_sub_u32 s9, s9, 1
s_cbranch_scc1 CK13G
v_max_u32 v240, v71, v75
v_cmp_eq_u32 vcc, -1, v240
s_cbranch_vccnz CK13
s_branch CK13D
CK13G:
s_mov_b32 s9, 0
CK13D:
v_max_u32 v240, v79, v83
v_cmp_eq_u32 vcc, -1, v240
s_cbranch_vccz CK13C
s_add_u32 s4, s7, 0x3800
buffer_load_dwordx4 v[76:79], v1, s[44:47], s4 offen sc1
buffer_load_dwordx4 v[80:83], v1, s[44:47], s4 offen offset:1024 sc1
buffer_load_dword v240, off, s[44:47], 0
buffer_load_dword v240, off, s[44:47], 0
buffer_load_dword v240, off, s[44:47], 0
buffer_load_dword v240, off, s[44:47], 0
buffer_load_dword v240, off, s[44:47], 0
buffer_load_dword v240, off, s[44:47], 0
buffer_load_dword v240, off, s[44:47], 0
buffer_load_dword v240, off, s[44:47], 0
buffer_load_dword v240, off, s[44:47], 0
s_branch CK13C
CK14:
s_nop 3
s_add_u32 s4, s7, 0x3800
buffer_load_dwordx4 v[76:79], v1, s[44:47], s4 offen sc1
buffer_load_dwordx4 v[80:83], v1, s[44:47], s4 offen offset:1024 sc1
s_waitcnt vmcnt(0)
s_sub_u32 s9, s9, 1
s_cbranch_scc1 CK14G
v_max_u32 v240, v79, v83
v_cmp_eq_u32 vcc, -1, v240
s_cbranch_vccnz CK14
s_branch CK14D
CK14G:
s_mov_b32 s9, 0
CK14D:
s_branch CK14C
CK16:
s_nop 3
buffer_load_dword v254, v232, s[44:47], s81 offen sc1
buffer_load_dword v239, v232, s[44:47], s81 offen offset:256 sc1
buffer_load_dword v238, v232, s[44:47], s81 offen offset:512 sc1
buffer_load_dword v236, v232, s[44:47], s81 offen offset:768 sc1
buffer_load_dword v235, v232, s[44:47], s81 offen offset:1024 sc1
buffer_load_dword v234, v232, s[44:47], s81 offen offset:1280 sc1
buffer_load_dword v233, v232, s[44:47], s81 offen offset:1536 sc1
s_lshl_b32 s76, s67, 9
s_add_u32 s76, s76, 0x4000
buffer_load_dword v68, v237, s[44:47], s76 offen sc1
buffer_load_dword v72, v237, s[44:47], s76 offen offset:128 sc1
buffer_load_dword v76, v237, s[44:47], s76 offen offset:256 sc1
buffer_load_dword v80, v237, s[44:47], s76 offen offset:384 sc1
s_waitcnt vmcnt(0)
s_sub_u32 s9, s9, 1
s_cbranch_scc1 CK16G
v_max3_u32 v240, v254, v239, v238
v_max3_u32 v240, v240, v236, v235
v_max3_u32 v240, v240, v234, v233
v_max3_u32 v240, v240, v68, v72
v_max3_u32 v240, v240, v76, v80
v_cmp_eq_u32 vcc, -1, v240
s_cbranch_vccnz CK16
s_branch CK16D
CK16G:
s_mov_b32 s9, 0
CK16D:
s_branch CK16C
CK17:
s_nop 3
s_add_u32 s4, s57, 0
buffer_load_dwordx4 v[84:87], v1, s[44:47], s4 offen sc1
buffer_load_dwordx4 v[88:91], v1, s[44:47], s4 offen offset:1024 sc1
s_waitcnt vmcnt(0)
s_sub_u32 s9, s9, 1
s_cbranch_scc1 CK17G
v_max_u32 v240, v87, v91
v_cmp_eq_u32 vcc, -1, v240
s_cbranch_vccnz CK17
s_branch CK17D
CK17G:
s_mov_b32 s9, 0
CK17D:
v_max3_u32 v240, v95, v99, v103
v_max3_u32 v240, v240, v107, v111
v_max3_u32 v240, v240, v115, v119
v_max3_u32 v240, v240, v123, v127
)ASM"
R"ASM(
v_max_u32 v240, v240, v131
v_cmp_eq_u32 vcc, -1, v240
s_cbranch_vccz CK17C
s_add_u32 s4, s57, 0
buffer_load_dwordx4 v[92:95], v1, s[44:47], s4 offen offset:2048 sc1
buffer_load_dwordx4 v[96:99], v1, s[44:47], s4 offen offset:3072 sc1
s_add_u32 s4, s57, 0x1000
buffer_load_dwordx4 v[100:103], v1, s[44:47], s4 offen sc1
buffer_load_dwordx4 v[104:107], v1, s[44:47], s4 offen offset:1024 sc1
buffer_load_dwordx4 v[108:111], v1, s[44:47], s4 offen offset:2048 sc1
buffer_load_dwordx4 v[112:115], v1, s[44:47], s4 offen offset:3072 sc1
s_add_u32 s4, s57, 0x2000
buffer_load_dwordx4 v[116:119], v1, s[44:47], s4 offen sc1
buffer_load_dwordx4 v[120:123], v1, s[44:47], s4 offen offset:1024 sc1
buffer_load_dwordx4 v[124:127], v1, s[44:47], s4 offen offset:2048 sc1
buffer_load_dwordx4 v[128:131], v1, s[44:47], s4 offen offset:3072 sc1
s_branch CK17C
CK18:
s_nop 3
s_add_u32 s4, s57, 0
buffer_load_dwordx4 v[92:95], v1, s[44:47], s4 offen offset:2048 sc1
buffer_load_dwordx4 v[96:99], v1, s[44:47], s4 offen offset:3072 sc1
s_waitcnt vmcnt(0)
s_sub_u32 s9, s9, 1
s_cbranch_scc1 CK18G
v_max_u32 v240, v95, v99
v_cmp_eq_u32 vcc, -1, v240
s_cbranch_vccnz CK18
s_branch CK18D
CK18G:
s_mov_b32 s9, 0
CK18D:
v_max3_u32 v240, v103, v107, v111
v_max3_u32 v240, v240, v115, v119
v_max3_u32 v240, v240, v123, v127
v_max3_u32 v240, v240, v131, v135
v_max_u32 v240, v240, v139
v_cmp_eq_u32 vcc, -1, v240
s_cbranch_vccz CK18C
s_add_u32 s4, s57, 0x1000
buffer_load_dwordx4 v[100:103], v1, s[44:47], s4 offen sc1
buffer_load_dwordx4 v[104:107], v1, s[44:47], s4 offen offset:1024 sc1
buffer_load_dwordx4 v[108:111], v1, s[44:47], s4 offen offset:2048 sc1
buffer_load_dwordx4 v[112:115], v1, s[44:47], s4 offen offset:3072 sc1
s_add_u32 s4, s57, 0x2000
buffer_load_dwordx4 v[116:119], v1, s[44:47], s4 offen sc1
buffer_load_dwordx4 v[120:123], v1, s[44:47], s4 offen offset:1024 sc1
buffer_load_dwordx4 v[124:127], v1, s[44:47], s4 offen offset:2048 sc1
buffer_load_dwordx4 v[128:131], v1, s[44:47], s4 offen offset:3072 sc1
s_add_u32 s4, s57, 0x3000
buffer_load_dwordx4 v[132:135], v1, s[44:47], s4 offen sc1
buffer_load_dwordx4 v[136:139], v1, s[44:47], s4 offen offset:1024 sc1
s_branch CK18C
CK19:
s_nop 3
s_add_u32 s4, s57, 0x1000
buffer_load_dwordx4 v[100:103], v1, s[44:47], s4 offen sc1
buffer_load_dwordx4 v[104:107], v1, s[44:47], s4 offen offset:1024 sc1
s_waitcnt vmcnt(0)
s_sub_u32 s9, s9, 1
s_cbranch_scc1 CK19G
v_max_u32 v240, v103, v107
v_cmp_eq_u32 vcc, -1, v240
s_cbranch_vccnz CK19
s_branch CK19D
CK19G:
s_mov_b32 s9, 0
CK19D:
v_max3_u32 v240, v111, v115, v119
v_max3_u32 v240, v240, v123, v127
v_max3_u32 v240, v240, v131, v135
v_max3_u32 v240, v240, v139, v143
v_max_u32 v240, v240, v147
v_cmp_eq_u32 vcc, -1, v240
s_cbranch_vccz CK19C
s_add_u32 s4, s57, 0x1000
buffer_load_dwordx4 v[108:111], v1, s[44:47], s4 offen offset:2048 sc1
buffer_load_dwordx4 v[112:115], v1, s[44:47], s4 offen offset:3072 sc1
s_add_u32 s4, s57, 0x2000
buffer_load_dwordx4 v[116:119], v1, s[44:47], s4 offen sc1
buffer_load_dwordx4 v[120:123], v1, s[44:47], s4 offen offset:1024 sc1
buffer_load_dwordx4 v[124:127], v1, s[44:47], s4 offen offset:2048 sc1
buffer_load_dwordx4 v[128:131], v1, s[44:47], s4 offen offset:3072 sc1
s_add_u32 s4, s57, 0x3000
buffer_load_dwordx4 v[132:135], v1, s[44:47], s4 offen sc1
buffer_load_dwordx4 v[136:139], v1, s[44:47], s4 offen offset:1024 sc1
buffer_load_dwordx4 v[140:143], v1, s[44:47], s4 offen offset:2048 sc1
buffer_load_dwordx4 v[144:147], v1, s[44:47], s4 offen offset:3072 sc1
s_branch CK19C
CK20:
s_nop 3
s_add_u32 s4, s57, 0x1000
buffer_load_dwordx4 v[108:111], v1, s[44:47], s4 offen offset:2048 sc1
buffer_load_dwordx4 v[112:115], v1, s[44:47], s4 offen offset:3072 sc1
s_waitcnt vmcnt(0)
s_sub_u32 s9, s9, 1
s_cbranch_scc1 CK20G
v_max_u32 v240, v111, v115
v_cmp_eq_u32 vcc, -1, v240
s_cbranch_vccnz CK20
s_branch CK20D
CK20G:
s_mov_b32 s9, 0
CK20D:
v_max3_u32 v240, v119, v123, v127
v_max3_u32 v240, v240, v131, v135
v_max3_u32 v240, v240, v139, v143
v_max3_u32 v240, v240, v147, v151
v_max_u32 v240, v240, v155
v_cmp_eq_u32 vcc, -1, v240
s_cbranch_vccz CK20C
s_add_u32 s4, s57, 0x2000
buffer_load_dwordx4 v[116:119], v1, s[44:47], s4 offen sc1
buffer_load_dwordx4 v[120:123], v1, s[44:47], s4 offen offset:1024 sc1
buffer_load_dwordx4 v[124:127], v1, s[44:47], s4 offen offset:2048 sc1
buffer_load_dwordx4 v[128:131], v1, s[44:47], s4 offen offset:3072 sc1
s_add_u32 s4, s57, 0x3000
buffer_load_dwordx4 v[132:135], v1, s[44:47], s4 offen sc1
buffer_load_dwordx4 v[136:139], v1, s[44:47], s4 offen offset:1024 sc1
buffer_load_dwordx4 v[140:143], v1, s[44:47], s4 offen offset:2048 sc1
buffer_load_dwordx4 v[144:147], v1, s[44:47], s4 offen offset:3072 sc1
s_add_u32 s4, s57, 0x4000
buffer_load_dwordx4 v[148:151], v1, s[44:47], s4 offen sc1
buffer_load_dwordx4 v[152:155], v1, s[44:47], s4 offen offset:1024 sc1
s_branch CK20C
CK21:
s_nop 3
s_add_u32 s4, s57, 0x2000
buffer_load_dwordx4 v[116:119], v1, s[44:47], s4 offen sc1
buffer_load_dwordx4 v[120:123], v1, s[44:47], s4 offen offset:1024 sc1
buffer_load_dwordx4 v[124:127], v1, s[44:47], s4 offen offset:2048 sc1
buffer_load_dwordx4 v[128:131], v1, s[44:47], s4 offen offset:3072 sc1
s_waitcnt vmcnt(0)
s_sub_u32 s9, s9, 1
s_cbranch_scc1 CK21G
v_max3_u32 v240, v119, v123, v127
v_max_u32 v240, v240, v131
v_cmp_eq_u32 vcc, -1, v240
s_cbranch_vccnz CK21
s_branch CK21D
CK21G:
s_mov_b32 s9, 0
CK21D:
v_max3_u32 v240, v135, v139, v143
v_max3_u32 v240, v240, v147, v151
v_max3_u32 v240, v240, v155, v159
v_max_u32 v240, v240, v163
v_cmp_eq_u32 vcc, -1, v240
s_cbranch_vccz CK21C
s_add_u32 s4, s57, 0x3000
buffer_load_dwordx4 v[132:135], v1, s[44:47], s4 offen sc1
buffer_load_dwordx4 v[136:139], v1, s[44:47], s4 offen offset:1024 sc1
buffer_load_dwordx4 v[140:143], v1, s[44:47], s4 offen offset:2048 sc1
buffer_load_dwordx4 v[144:147], v1, s[44:47], s4 offen offset:3072 sc1
s_add_u32 s4, s57, 0x4000
buffer_load_dwordx4 v[148:151], v1, s[44:47], s4 offen sc1
buffer_load_dwordx4 v[152:155], v1, s[44:47], s4 offen offset:1024 sc1
buffer_load_dwordx4 v[156:159], v1, s[44:47], s4 offen offset:2048 sc1
buffer_load_dwordx4 v[160:163], v1, s[44:47], s4 offen offset:3072 sc1
s_branch CK21C
CK22:
s_nop 3
s_add_u32 s4, s57, 0x3000
buffer_load_dwordx4 v[132:135], v1, s[44:47], s4 offen sc1
buffer_load_dwordx4 v[136:139], v1, s[44:47], s4 offen offset:1024 sc1
buffer_load_dwordx4 v[140:143], v1, s[44:47], s4 offen offset:2048 sc1
buffer_load_dwordx4 v[144:147], v1, s[44:47], s4 offen offset:3072 sc1
s_waitcnt vmcnt(0)
s_sub_u32 s9, s9, 1
s_cbranch_scc1 CK22G
v_max3_u32 v240, v135, v139, v143
v_max_u32 v240, v240, v147
v_cmp_eq_u32 vcc, -1, v240
s_cbranch_vccnz CK22
s_branch CK22D
CK22G:
s_mov_b32 s9, 0
CK22D:
v_max3_u32 v240, v151, v155, v159
v_max3_u32 v240, v240, v163, v167
v_max3_u32 v240, v240, v171, v175
v_max_u32 v240, v240, v179
v_cmp_eq_u32 vcc, -1, v240
s_cbranch_vccz CK22C
s_add_u32 s4, s57, 0x4000
buffer_load_dwordx4 v[148:151], v1, s[44:47], s4 offen sc1
buffer_load_dwordx4 v[152:155], v1, s[44:47], s4 offen offset:1024 sc1
buffer_load_dwordx4 v[156:159], v1, s[44:47], s4 offen offset:2048 sc1
buffer_load_dwordx4 v[160:163], v1, s[44:47], s4 offen offset:3072 sc1
s_add_u32 s4, s57, 0x5000
buffer_load_dwordx4 v[164:167], v1, s[44:47], s4 offen sc1
buffer_load_dwordx4 v[168:171], v1, s[44:47], s4 offen offset:1024 sc1
buffer_load_dwordx4 v[172:175], v1, s[44:47], s4 offen offset:2048 sc1
buffer_load_dwordx4 v[176:179], v1, s[44:47], s4 offen offset:3072 sc1
s_branch CK22C
CK23:
s_nop 3
s_add_u32 s4, s57, 0x4000
buffer_load_dwordx4 v[148:151], v1, s[44:47], s4 offen sc1
buffer_load_dwordx4 v[152:155], v1, s[44:47], s4 offen offset:1024 sc1
buffer_load_dwordx4 v[156:159], v1, s[44:47], s4 offen offset:2048 sc1
buffer_load_dwordx4 v[160:163], v1, s[44:47], s4 offen offset:3072 sc1
s_waitcnt vmcnt(0)
s_sub_u32 s9, s9, 1
s_cbranch_scc1 CK23G
v_max3_u32 v240, v151, v155, v159
v_max_u32 v240, v240, v163
v_cmp_eq_u32 vcc, -1, v240
s_cbranch_vccnz CK23
s_branch CK23D
CK23G:
s_mov_b32 s9, 0
CK23D:
v_max3_u32 v240, v167, v171, v175
v_max3_u32 v240, v240, v179, v183
v_max3_u32 v240, v240, v195, v199
v_max_u32 v240, v240, v203
v_cmp_eq_u32 vcc, -1, v240
s_cbranch_vccz CK23C
s_add_u32 s4, s57, 0x5000
buffer_load_dwordx4 v[164:167], v1, s[44:47], s4 offen sc1
buffer_load_dwordx4 v[168:171], v1, s[44:47], s4 offen offset:1024 sc1
buffer_load_dwordx4 v[172:175], v1, s[44:47], s4 offen offset:2048 sc1
buffer_load_dwordx4 v[176:179], v1, s[44:47], s4 offen offset:3072 sc1
s_add_u32 s4, s57, 0x6000
buffer_load_dwordx4 v[180:183], v1, s[44:47], s4 offen sc1
buffer_load_dwordx4 v[192:195], v1, s[44:47], s4 offen offset:1024 sc1
buffer_load_dwordx4 v[196:199], v1, s[44:47], s4 offen offset:2048 sc1
buffer_load_dwordx4 v[200:203], v1, s[44:47], s4 offen offset:3072 sc1
s_branch CK23C
CK24:
s_nop 3
s_add_u32 s4, s57, 0x5000
buffer_load_dwordx4 v[164:167], v1, s[44:47], s4 offen sc1
buffer_load_dwordx4 v[168:171], v1, s[44:47], s4 offen offset:1024 sc1
buffer_load_dwordx4 v[172:175], v1, s[44:47], s4 offen offset:2048 sc1
buffer_load_dwordx4 v[176:179], v1, s[44:47], s4 offen offset:3072 sc1
s_waitcnt vmcnt(0)
s_sub_u32 s9, s9, 1
s_cbranch_scc1 CK24G
v_max3_u32 v240, v167, v171, v175
v_max_u32 v240, v240, v179
v_cmp_eq_u32 vcc, -1, v240
s_cbranch_vccnz CK24
s_branch CK24D
CK24G:
s_mov_b32 s9, 0
CK24D:
v_max3_u32 v240, v183, v195, v199
v_max_u32 v240, v240, v203
v_cmp_eq_u32 vcc, -1, v240
s_cbranch_vccz CK24C
s_add_u32 s4, s57, 0x6000
buffer_load_dwordx4 v[180:183], v1, s[44:47], s4 offen sc1
buffer_load_dwordx4 v[192:195], v1, s[44:47], s4 offen offset:1024 sc1
buffer_load_dwordx4 v[196:199], v1, s[44:47], s4 offen offset:2048 sc1
buffer_load_dwordx4 v[200:203], v1, s[44:47], s4 offen offset:3072 sc1
buffer_load_dword v240, off, s[44:47], 0
buffer_load_dword v240, off, s[44:47], 0
buffer_load_dword v240, off, s[44:47], 0
buffer_load_dword v240, off, s[44:47], 0
buffer_load_dword v240, off, s[44:47], 0
buffer_load_dword v240, off, s[44:47], 0
buffer_load_dword v240, off, s[44:47], 0
buffer_load_dword v240, off, s[44:47], 0
buffer_load_dword v240, off, s[44:47], 0
buffer_load_dword v240, off, s[44:47], 0
buffer_load_dword v240, off, s[44:47], 0
buffer_load_dword v240, off, s[44:47], 0
buffer_load_dword v240, off, s[44:47], 0
buffer_load_dword v240, off, s[44:47], 0
buffer_load_dword v240, off, s[44:47], 0
s_branch CK24C
CK25:
s_nop 3
s_add_u32 s4, s57, 0x6000
buffer_load_dwordx4 v[180:183], v1, s[44:47], s4 offen sc1
buffer_load_dwordx4 v[192:195], v1, s[44:47], s4 offen offset:1024 sc1
buffer_load_dwordx4 v[196:199], v1, s[44:47], s4 offen offset:2048 sc1
buffer_load_dwordx4 v[200:203], v1, s[44:47], s4 offen offset:3072 sc1
s_waitcnt vmcnt(0)
s_sub_u32 s9, s9, 1
s_cbranch_scc1 CK25G
v_max3_u32 v240, v183, v195, v199
v_max_u32 v240, v240, v203
v_cmp_eq_u32 vcc, -1, v240
s_cbranch_vccnz CK25
s_branch CK25D
CK25G:
s_mov_b32 s9, 0
CK25D:
s_branch CK25C
CK27:
s_nop 3
s_add_u32 s4, s5, 0
buffer_load_dwordx4 v[100:103], v1, s[44:47], s4 offen sc1
s_add_u32 s4, s6, 0
buffer_load_dwordx4 v[104:107], v1, s[44:47], s4 offen sc1
s_add_u32 s4, s5, 0x400
buffer_load_dwordx4 v[108:111], v1, s[44:47], s4 offen sc1
s_waitcnt vmcnt(0)
s_sub_u32 s9, s9, 1
s_cbranch_scc1 CK27G
v_max3_u32 v240, v103, v107, v111
v_cmp_eq_u32 vcc, -1, v240
s_cbranch_vccnz CK27
s_branch CK27D
CK27G:
s_mov_b32 s9, 0
CK27D:
v_max3_u32 v240, v115, v119, v123
v_max3_u32 v240, v240, v127, v131
v_max3_u32 v240, v240, v135, v139
v_max3_u32 v240, v240, v143, v147
v_cmp_eq_u32 vcc, -1, v240
s_cbranch_vccz CK27C
s_add_u32 s4, s6, 0x400
buffer_load_dwordx4 v[112:115], v1, s[44:47], s4 offen sc1
s_add_u32 s4, s5, 0x800
buffer_load_dwordx4 v[116:119], v1, s[44:47], s4 offen sc1
s_add_u32 s4, s6, 0x800
buffer_load_dwordx4 v[120:123], v1, s[44:47], s4 offen sc1
buffer_load_dword v240, off, s[44:47], 0
s_add_u32 s4, s5, 0xc00
buffer_load_dwordx4 v[124:127], v1, s[44:47], s4 offen sc1
s_add_u32 s4, s6, 0xc00
buffer_load_dwordx4 v[128:131], v1, s[44:47], s4 offen sc1
s_add_u32 s4, s5, 0x1000
buffer_load_dwordx4 v[132:135], v1, s[44:47], s4 offen sc1
buffer_load_dword v240, off, s[44:47], 0
s_add_u32 s4, s6, 0x1000
buffer_load_dwordx4 v[136:139], v1, s[44:47], s4 offen sc1
s_add_u32 s4, s5, 0x1400
buffer_load_dwordx4 v[140:143], v1, s[44:47], s4 offen sc1
s_add_u32 s4, s6, 0x1400
buffer_load_dwordx4 v[144:147], v1, s[44:47], s4 offen sc1
s_branch CK27C
CK28:
s_nop 3
s_add_u32 s4, s6, 0x400
buffer_load_dwordx4 v[112:115], v1, s[44:47], s4 offen sc1
s_add_u32 s4, s5, 0x800
buffer_load_dwordx4 v[116:119], v1, s[44:47], s4 offen sc1
s_waitcnt vmcnt(0)
s_sub_u32 s9, s9, 1
s_cbranch_scc1 CK28G
v_max_u32 v240, v115, v119
v_cmp_eq_u32 vcc, -1, v240
s_cbranch_vccnz CK28
s_branch CK28D
CK28G:
s_mov_b32 s9, 0
CK28D:
v_max3_u32 v240, v123, v127, v131
v_max3_u32 v240, v240, v135, v139
v_max3_u32 v240, v240, v143, v147
v_max3_u32 v240, v240, v151, v155
v_max_u32 v240, v240, v159
v_cmp_eq_u32 vcc, -1, v240
s_cbranch_vccz CK28C
s_add_u32 s4, s6, 0x800
buffer_load_dwordx4 v[120:123], v1, s[44:47], s4 offen sc1
buffer_load_dword v240, off, s[44:47], 0
s_add_u32 s4, s5, 0xc00
buffer_load_dwordx4 v[124:127], v1, s[44:47], s4 offen sc1
s_add_u32 s4, s6, 0xc00
buffer_load_dwordx4 v[128:131], v1, s[44:47], s4 offen sc1
s_add_u32 s4, s5, 0x1000
buffer_load_dwordx4 v[132:135], v1, s[44:47], s4 offen sc1
buffer_load_dword v240, off, s[44:47], 0
s_add_u32 s4, s6, 0x1000
buffer_load_dwordx4 v[136:139], v1, s[44:47], s4 offen sc1
s_add_u32 s4, s5, 0x1400
buffer_load_dwordx4 v[140:143], v1, s[44:47], s4 offen sc1
s_add_u32 s4, s6, 0x1400
buffer_load_dwordx4 v[144:147], v1, s[44:47], s4 offen sc1
s_add_u32 s4, s5, 0x1800
buffer_load_dwordx4 v[148:151], v1, s[44:47], s4 offen sc1
s_add_u32 s4, s6, 0x1800
buffer_load_dwordx4 v[152:155], v1, s[44:47], s4 offen sc1
s_add_u32 s4, s5, 0x7000
buffer_load_dwordx4 v[156:159], v1, s[44:47], s4 offen sc1
s_branch CK28C
CK29:
s_nop 3
s_add_u32 s4, s6, 0x800
buffer_load_dwordx4 v[120:123], v1, s[44:47], s4 offen sc1
s_add_u32 s4, s5, 0xc00
buffer_load_dwordx4 v[124:127], v1, s[44:47], s4 offen sc1
s_waitcnt vmcnt(0)
s_sub_u32 s9, s9, 1
s_cbranch_scc1 CK29G
v_max_u32 v240, v123, v127
v_cmp_eq_u32 vcc, -1, v240
s_cbranch_vccnz CK29
s_branch CK29D
CK29G:
s_mov_b32 s9, 0
CK29D:
v_max3_u32 v240, v131, v135, v139
)ASM"
R"ASM(
v_max3_u32 v240, v240, v143, v147
v_max3_u32 v240, v240, v151, v155
v_max3_u32 v240, v240, v159, v163
v_max_u32 v240, v240, v167
v_cmp_eq_u32 vcc, -1, v240
s_cbranch_vccz CK29C
s_add_u32 s4, s6, 0xc00
buffer_load_dwordx4 v[128:131], v1, s[44:47], s4 offen sc1
s_add_u32 s4, s5, 0x1000
buffer_load_dwordx4 v[132:135], v1, s[44:47], s4 offen sc1
buffer_load_dword v240, off, s[44:47], 0
s_add_u32 s4, s6, 0x1000
buffer_load_dwordx4 v[136:139], v1, s[44:47], s4 offen sc1
s_add_u32 s4, s5, 0x1400
buffer_load_dwordx4 v[140:143], v1, s[44:47], s4 offen sc1
s_add_u32 s4, s6, 0x1400
buffer_load_dwordx4 v[144:147], v1, s[44:47], s4 offen sc1
s_add_u32 s4, s5, 0x1800
buffer_load_dwordx4 v[148:151], v1, s[44:47], s4 offen sc1
s_add_u32 s4, s6, 0x1800
buffer_load_dwordx4 v[152:155], v1, s[44:47], s4 offen sc1
s_add_u32 s4, s5, 0x7000
buffer_load_dwordx4 v[156:159], v1, s[44:47], s4 offen sc1
s_add_u32 s4, s6, 0x7000
buffer_load_dwordx4 v[160:163], v1, s[44:47], s4 offen sc1
s_add_u32 s4, s5, 0x7400
buffer_load_dwordx4 v[164:167], v1, s[44:47], s4 offen sc1
s_branch CK29C
CK30:
s_nop 3
s_add_u32 s4, s6, 0xc00
buffer_load_dwordx4 v[128:131], v1, s[44:47], s4 offen sc1
s_add_u32 s4, s5, 0x1000
buffer_load_dwordx4 v[132:135], v1, s[44:47], s4 offen sc1
s_waitcnt vmcnt(0)
s_sub_u32 s9, s9, 1
s_cbranch_scc1 CK30G
v_max_u32 v240, v131, v135
v_cmp_eq_u32 vcc, -1, v240
s_cbranch_vccnz CK30
s_branch CK30D
CK30G:
s_mov_b32 s9, 0
CK30D:
v_max3_u32 v240, v139, v143, v147
v_max3_u32 v240, v240, v151, v155
v_max3_u32 v240, v240, v159, v163
v_max3_u32 v240, v240, v167, v171
v_max_u32 v240, v240, v175
v_cmp_eq_u32 vcc, -1, v240
s_cbranch_vccz CK30C
s_add_u32 s4, s6, 0x1000
buffer_load_dwordx4 v[136:139], v1, s[44:47], s4 offen sc1
s_add_u32 s4, s5, 0x1400
buffer_load_dwordx4 v[140:143], v1, s[44:47], s4 offen sc1
s_add_u32 s4, s6, 0x1400
buffer_load_dwordx4 v[144:147], v1, s[44:47], s4 offen sc1
s_add_u32 s4, s5, 0x1800
buffer_load_dwordx4 v[148:151], v1, s[44:47], s4 offen sc1
s_add_u32 s4, s6, 0x1800
buffer_load_dwordx4 v[152:155], v1, s[44:47], s4 offen sc1
s_add_u32 s4, s5, 0x7000
buffer_load_dwordx4 v[156:159], v1, s[44:47], s4 offen sc1
s_add_u32 s4, s6, 0x7000
buffer_load_dwordx4 v[160:163], v1, s[44:47], s4 offen sc1
s_add_u32 s4, s5, 0x7400
buffer_load_dwordx4 v[164:167], v1, s[44:47], s4 offen sc1
s_add_u32 s4, s6, 0x7400
buffer_load_dwordx4 v[168:171], v1, s[44:47], s4 offen sc1
s_add_u32 s4, s5, 0x7800
buffer_load_dwordx4 v[172:175], v1, s[44:47], s4 offen sc1
s_branch CK30C
CK31:
s_nop 3
s_add_u32 s4, s6, 0x1000
buffer_load_dwordx4 v[136:139], v1, s[44:47], s4 offen sc1
s_add_u32 s4, s5, 0x1400
buffer_load_dwordx4 v[140:143], v1, s[44:47], s4 offen sc1
s_waitcnt vmcnt(0)
s_sub_u32 s9, s9, 1
s_cbranch_scc1 CK31G
v_max_u32 v240, v139, v143
v_cmp_eq_u32 vcc, -1, v240
s_cbranch_vccnz CK31
s_branch CK31D
CK31G:
s_mov_b32 s9, 0
CK31D:
v_max3_u32 v240, v147, v151, v155
v_max3_u32 v240, v240, v159, v163
v_max3_u32 v240, v240, v167, v171
v_max3_u32 v240, v240, v175, v179
v_max_u32 v240, v240, v103
v_cmp_eq_u32 vcc, -1, v240
s_cbranch_vccz CK31C
s_add_u32 s4, s6, 0x1400
buffer_load_dwordx4 v[144:147], v1, s[44:47], s4 offen sc1
s_add_u32 s4, s5, 0x1800
buffer_load_dwordx4 v[148:151], v1, s[44:47], s4 offen sc1
s_add_u32 s4, s6, 0x1800
buffer_load_dwordx4 v[152:155], v1, s[44:47], s4 offen sc1
s_add_u32 s4, s5, 0x7000
buffer_load_dwordx4 v[156:159], v1, s[44:47], s4 offen sc1
s_add_u32 s4, s6, 0x7000
buffer_load_dwordx4 v[160:163], v1, s[44:47], s4 offen sc1
s_add_u32 s4, s5, 0x7400
buffer_load_dwordx4 v[164:167], v1, s[44:47], s4 offen sc1
s_add_u32 s4, s6, 0x7400
buffer_load_dwordx4 v[168:171], v1, s[44:47], s4 offen sc1
s_add_u32 s4, s5, 0x7800
buffer_load_dwordx4 v[172:175], v1, s[44:47], s4 offen sc1
s_add_u32 s4, s6, 0x7800
buffer_load_dwordx4 v[176:179], v1, s[44:47], s4 offen sc1
s_add_u32 s4, s5, 0x7c00
buffer_load_dwordx4 v[100:103], v1, s[44:47], s4 offen sc1
s_branch CK31C
CK32:
s_nop 3
s_add_u32 s4, s6, 0x1400
buffer_load_dwordx4 v[144:147], v1, s[44:47], s4 offen sc1
s_add_u32 s4, s5, 0x1800
buffer_load_dwordx4 v[148:151], v1, s[44:47], s4 offen sc1
s_waitcnt vmcnt(0)
s_sub_u32 s9, s9, 1
s_cbranch_scc1 CK32G
v_max_u32 v240, v147, v151
v_cmp_eq_u32 vcc, -1, v240
s_cbranch_vccnz CK32
s_branch CK32D
CK32G:
s_mov_b32 s9, 0
CK32D:
v_max3_u32 v240, v155, v159, v163
v_max3_u32 v240, v240, v167, v171
v_max3_u32 v240, v240, v175, v179
v_max3_u32 v240, v240, v103, v107
v_max_u32 v240, v240, v111
v_cmp_eq_u32 vcc, -1, v240
s_cbranch_vccz CK32C
s_add_u32 s4, s6, 0x1800
buffer_load_dwordx4 v[152:155], v1, s[44:47], s4 offen sc1
s_add_u32 s4, s5, 0x7000
buffer_load_dwordx4 v[156:159], v1, s[44:47], s4 offen sc1
s_add_u32 s4, s6, 0x7000
buffer_load_dwordx4 v[160:163], v1, s[44:47], s4 offen sc1
s_add_u32 s4, s5, 0x7400
buffer_load_dwordx4 v[164:167], v1, s[44:47], s4 offen sc1
s_add_u32 s4, s6, 0x7400
buffer_load_dwordx4 v[168:171], v1, s[44:47], s4 offen sc1
s_add_u32 s4, s5, 0x7800
buffer_load_dwordx4 v[172:175], v1, s[44:47], s4 offen sc1
s_add_u32 s4, s6, 0x7800
buffer_load_dwordx4 v[176:179], v1, s[44:47], s4 offen sc1
s_add_u32 s4, s5, 0x7c00
buffer_load_dwordx4 v[100:103], v1, s[44:47], s4 offen sc1
buffer_load_dword v240, off, s[44:47], 0
s_add_u32 s4, s6, 0x7c00
buffer_load_dwordx4 v[104:107], v1, s[44:47], s4 offen sc1
s_add_u32 s4, s5, 0x8000
buffer_load_dwordx4 v[108:111], v1, s[44:47], s4 offen sc1
s_branch CK32C
CK33:
s_nop 3
s_add_u32 s4, s6, 0x1800
buffer_load_dwordx4 v[152:155], v1, s[44:47], s4 offen sc1
s_add_u32 s4, s5, 0x7000
buffer_load_dwordx4 v[156:159], v1, s[44:47], s4 offen sc1
s_waitcnt vmcnt(0)
s_sub_u32 s9, s9, 1
s_cbranch_scc1 CK33G
v_max_u32 v240, v155, v159
v_cmp_eq_u32 vcc, -1, v240
s_cbranch_vccnz CK33
s_branch CK33D
CK33G:
s_mov_b32 s9, 0
CK33D:
v_max3_u32 v240, v163, v167, v171
v_max3_u32 v240, v240, v175, v179
v_max3_u32 v240, v240, v103, v107
v_max3_u32 v240, v240, v111, v115
v_max_u32 v240, v240, v119
v_cmp_eq_u32 vcc, -1, v240
s_cbranch_vccz CK33C
s_add_u32 s4, s6, 0x7000
buffer_load_dwordx4 v[160:163], v1, s[44:47], s4 offen sc1
s_add_u32 s4, s5, 0x7400
buffer_load_dwordx4 v[164:167], v1, s[44:47], s4 offen sc1
s_add_u32 s4, s6, 0x7400
buffer_load_dwordx4 v[168:171], v1, s[44:47], s4 offen sc1
s_add_u32 s4, s5, 0x7800
buffer_load_dwordx4 v[172:175], v1, s[44:47], s4 offen sc1
s_add_u32 s4, s6, 0x7800
buffer_load_dwordx4 v[176:179], v1, s[44:47], s4 offen sc1
s_add_u32 s4, s5, 0x7c00
buffer_load_dwordx4 v[100:103], v1, s[44:47], s4 offen sc1
buffer_load_dword v240, off, s[44:47], 0
s_add_u32 s4, s6, 0x7c00
buffer_load_dwordx4 v[104:107], v1, s[44:47], s4 offen sc1
s_add_u32 s4, s5, 0x8000
buffer_load_dwordx4 v[108:111], v1, s[44:47], s4 offen sc1
s_add_u32 s4, s6, 0x8000
buffer_load_dwordx4 v[112:115], v1, s[44:47], s4 offen sc1
s_add_u32 s4, s5, 0x8400
buffer_load_dwordx4 v[116:119], v1, s[44:47], s4 offen sc1
s_branch CK33C
CK34:
s_nop 3
s_add_u32 s4, s6, 0x7000
buffer_load_dwordx4 v[160:163], v1, s[44:47], s4 offen sc1
s_add_u32 s4, s5, 0x7400
buffer_load_dwordx4 v[164:167], v1, s[44:47], s4 offen sc1
s_waitcnt vmcnt(0)
s_sub_u32 s9, s9, 1
s_cbranch_scc1 CK34G
v_max_u32 v240, v163, v167
v_cmp_eq_u32 vcc, -1, v240
s_cbranch_vccnz CK34
s_branch CK34D
CK34G:
s_mov_b32 s9, 0
CK34D:
v_max3_u32 v240, v171, v175, v179
v_max3_u32 v240, v240, v103, v107
v_max3_u32 v240, v240, v111, v115
v_max3_u32 v240, v240, v119, v123
v_max_u32 v240, v240, v127
v_cmp_eq_u32 vcc, -1, v240
s_cbranch_vccz CK34C
s_add_u32 s4, s6, 0x7400
buffer_load_dwordx4 v[168:171], v1, s[44:47], s4 offen sc1
s_add_u32 s4, s5, 0x7800
buffer_load_dwordx4 v[172:175], v1, s[44:47], s4 offen sc1
s_add_u32 s4, s6, 0x7800
buffer_load_dwordx4 v[176:179], v1, s[44:47], s4 offen sc1
s_add_u32 s4, s5, 0x7c00
buffer_load_dwordx4 v[100:103], v1, s[44:47], s4 offen sc1
buffer_load_dword v240, off, s[44:47], 0
s_add_u32 s4, s6, 0x7c00
buffer_load_dwordx4 v[104:107], v1, s[44:47], s4 offen sc1
s_add_u32 s4, s5, 0x8000
buffer_load_dwordx4 v[108:111], v1, s[44:47], s4 offen sc1
s_add_u32 s4, s6, 0x8000
buffer_load_dwordx4 v[112:115], v1, s[44:47], s4 offen sc1
s_add_u32 s4, s5, 0x8400
buffer_load_dwordx4 v[116:119], v1, s[44:47], s4 offen sc1
s_add_u32 s4, s6, 0x8400
buffer_load_dwordx4 v[120:123], v1, s[44:47], s4 offen sc1
s_add_u32 s4, s5, 0x8800
buffer_load_dwordx4 v[124:127], v1, s[44:47], s4 offen sc1
s_branch CK34C
CK35:
s_nop 3
s_add_u32 s4, s6, 0x7400
buffer_load_dwordx4 v[168:171], v1, s[44:47], s4 offen sc1
s_add_u32 s4, s5, 0x7800
buffer_load_dwordx4 v[172:175], v1, s[44:47], s4 offen sc1
s_waitcnt vmcnt(0)
s_sub_u32 s9, s9, 1
s_cbranch_scc1 CK35G
v_max_u32 v240, v171, v175
v_cmp_eq_u32 vcc, -1, v240
s_cbranch_vccnz CK35
s_branch CK35D
CK35G:
s_mov_b32 s9, 0
CK35D:
v_max3_u32 v240, v179, v103, v107
v_max3_u32 v240, v240, v111, v115
v_max3_u32 v240, v240, v119, v123
v_max3_u32 v240, v240, v127, v131
v_max_u32 v240, v240, v135
v_cmp_eq_u32 vcc, -1, v240
s_cbranch_vccz CK35C
s_add_u32 s4, s6, 0x7800
buffer_load_dwordx4 v[176:179], v1, s[44:47], s4 offen sc1
s_add_u32 s4, s5, 0x7c00
buffer_load_dwordx4 v[100:103], v1, s[44:47], s4 offen sc1
buffer_load_dword v240, off, s[44:47], 0
s_add_u32 s4, s6, 0x7c00
buffer_load_dwordx4 v[104:107], v1, s[44:47], s4 offen sc1
s_add_u32 s4, s5, 0x8000
buffer_load_dwordx4 v[108:111], v1, s[44:47], s4 offen sc1
s_add_u32 s4, s6, 0x8000
buffer_load_dwordx4 v[112:115], v1, s[44:47], s4 offen sc1
s_add_u32 s4, s5, 0x8400
buffer_load_dwordx4 v[116:119], v1, s[44:47], s4 offen sc1
s_add_u32 s4, s6, 0x8400
buffer_load_dwordx4 v[120:123], v1, s[44:47], s4 offen sc1
s_add_u32 s4, s5, 0x8800
buffer_load_dwordx4 v[124:127], v1, s[44:47], s4 offen sc1
s_add_u32 s4, s6, 0x8800
buffer_load_dwordx4 v[128:131], v1, s[44:47], s4 offen sc1
s_add_u32 s4, s5, 0xe000
buffer_load_dwordx4 v[132:135], v1, s[44:47], s4 offen sc1
s_branch CK35C
CK36:
s_nop 3
s_add_u32 s4, s6, 0x7800
buffer_load_dwordx4 v[176:179], v1, s[44:47], s4 offen sc1
s_add_u32 s4, s5, 0x7c00
buffer_load_dwordx4 v[100:103], v1, s[44:47], s4 offen sc1
s_waitcnt vmcnt(0)
s_sub_u32 s9, s9, 1
s_cbranch_scc1 CK36G
v_max_u32 v240, v179, v103
v_cmp_eq_u32 vcc, -1, v240
s_cbranch_vccnz CK36
s_branch CK36D
CK36G:
s_mov_b32 s9, 0
CK36D:
v_max3_u32 v240, v107, v111, v115
v_max3_u32 v240, v240, v119, v123
v_max3_u32 v240, v240, v127, v131
v_max3_u32 v240, v240, v135, v139
v_max_u32 v240, v240, v143
v_cmp_eq_u32 vcc, -1, v240
s_cbranch_vccz CK36C
s_add_u32 s4, s6, 0x7c00
buffer_load_dwordx4 v[104:107], v1, s[44:47], s4 offen sc1
s_add_u32 s4, s5, 0x8000
buffer_load_dwordx4 v[108:111], v1, s[44:47], s4 offen sc1
s_add_u32 s4, s6, 0x8000
buffer_load_dwordx4 v[112:115], v1, s[44:47], s4 offen sc1
s_add_u32 s4, s5, 0x8400
buffer_load_dwordx4 v[116:119], v1, s[44:47], s4 offen sc1
s_add_u32 s4, s6, 0x8400
buffer_load_dwordx4 v[120:123], v1, s[44:47], s4 offen sc1
s_add_u32 s4, s5, 0x8800
buffer_load_dwordx4 v[124:127], v1, s[44:47], s4 offen sc1
s_add_u32 s4, s6, 0x8800
buffer_load_dwordx4 v[128:131], v1, s[44:47], s4 offen sc1
s_add_u32 s4, s5, 0xe000
buffer_load_dwordx4 v[132:135], v1, s[44:47], s4 offen sc1
s_add_u32 s4, s6, 0xe000
buffer_load_dwordx4 v[136:139], v1, s[44:47], s4 offen sc1
buffer_load_dword v240, off, s[44:47], 0
s_add_u32 s4, s5, 0xe400
buffer_load_dwordx4 v[140:143], v1, s[44:47], s4 offen sc1
s_branch CK36C
CK37:
s_nop 3
s_add_u32 s4, s6, 0x7c00
buffer_load_dwordx4 v[104:107], v1, s[44:47], s4 offen sc1
s_add_u32 s4, s5, 0x8000
buffer_load_dwordx4 v[108:111], v1, s[44:47], s4 offen sc1
s_waitcnt vmcnt(0)
s_sub_u32 s9, s9, 1
s_cbranch_scc1 CK37G
v_max_u32 v240, v107, v111
v_cmp_eq_u32 vcc, -1, v240
s_cbranch_vccnz CK37
s_branch CK37D
CK37G:
s_mov_b32 s9, 0
CK37D:
v_max3_u32 v240, v115, v119, v123
v_max3_u32 v240, v240, v127, v131
v_max3_u32 v240, v240, v135, v139
v_max3_u32 v240, v240, v143, v147
v_max_u32 v240, v240, v151
v_cmp_eq_u32 vcc, -1, v240
s_cbranch_vccz CK37C
s_add_u32 s4, s6, 0x8000
buffer_load_dwordx4 v[112:115], v1, s[44:47], s4 offen sc1
s_add_u32 s4, s5, 0x8400
buffer_load_dwordx4 v[116:119], v1, s[44:47], s4 offen sc1
s_add_u32 s4, s6, 0x8400
buffer_load_dwordx4 v[120:123], v1, s[44:47], s4 offen sc1
s_add_u32 s4, s5, 0x8800
buffer_load_dwordx4 v[124:127], v1, s[44:47], s4 offen sc1
s_add_u32 s4, s6, 0x8800
buffer_load_dwordx4 v[128:131], v1, s[44:47], s4 offen sc1
s_add_u32 s4, s5, 0xe000
buffer_load_dwordx4 v[132:135], v1, s[44:47], s4 offen sc1
s_add_u32 s4, s6, 0xe000
buffer_load_dwordx4 v[136:139], v1, s[44:47], s4 offen sc1
buffer_load_dword v240, off, s[44:47], 0
s_add_u32 s4, s5, 0xe400
buffer_load_dwordx4 v[140:143], v1, s[44:47], s4 offen sc1
s_add_u32 s4, s6, 0xe400
buffer_load_dwordx4 v[144:147], v1, s[44:47], s4 offen sc1
s_add_u32 s4, s5, 0xe800
buffer_load_dwordx4 v[148:151], v1, s[44:47], s4 offen sc1
s_branch CK37C
CK38:
s_nop 3
s_add_u32 s4, s6, 0x8000
buffer_load_dwordx4 v[112:115], v1, s[44:47], s4 offen sc1
s_add_u32 s4, s5, 0x8400
buffer_load_dwordx4 v[116:119], v1, s[44:47], s4 offen sc1
s_waitcnt vmcnt(0)
s_sub_u32 s9, s9, 1
s_cbranch_scc1 CK38G
v_max_u32 v240, v115, v119
v_cmp_eq_u32 vcc, -1, v240
s_cbranch_vccnz CK38
s_branch CK38D
CK38G:
)ASM"
R"ASM(
s_mov_b32 s9, 0
CK38D:
v_max3_u32 v240, v123, v127, v131
v_max3_u32 v240, v240, v135, v139
v_max3_u32 v240, v240, v143, v147
v_max3_u32 v240, v240, v151, v155
v_max_u32 v240, v240, v159
v_cmp_eq_u32 vcc, -1, v240
s_cbranch_vccz CK38C
s_add_u32 s4, s6, 0x8400
buffer_load_dwordx4 v[120:123], v1, s[44:47], s4 offen sc1
s_add_u32 s4, s5, 0x8800
buffer_load_dwordx4 v[124:127], v1, s[44:47], s4 offen sc1
s_add_u32 s4, s6, 0x8800
buffer_load_dwordx4 v[128:131], v1, s[44:47], s4 offen sc1
s_add_u32 s4, s5, 0xe000
buffer_load_dwordx4 v[132:135], v1, s[44:47], s4 offen sc1
s_add_u32 s4, s6, 0xe000
buffer_load_dwordx4 v[136:139], v1, s[44:47], s4 offen sc1
buffer_load_dword v240, off, s[44:47], 0
s_add_u32 s4, s5, 0xe400
buffer_load_dwordx4 v[140:143], v1, s[44:47], s4 offen sc1
s_add_u32 s4, s6, 0xe400
buffer_load_dwordx4 v[144:147], v1, s[44:47], s4 offen sc1
s_add_u32 s4, s5, 0xe800
buffer_load_dwordx4 v[148:151], v1, s[44:47], s4 offen sc1
s_add_u32 s4, s6, 0xe800
buffer_load_dwordx4 v[152:155], v1, s[44:47], s4 offen sc1
s_add_u32 s4, s5, 0xec00
buffer_load_dwordx4 v[156:159], v1, s[44:47], s4 offen sc1
s_branch CK38C
CK39:
s_nop 3
s_add_u32 s4, s6, 0x8400
buffer_load_dwordx4 v[120:123], v1, s[44:47], s4 offen sc1
s_add_u32 s4, s5, 0x8800
buffer_load_dwordx4 v[124:127], v1, s[44:47], s4 offen sc1
s_waitcnt vmcnt(0)
s_sub_u32 s9, s9, 1
s_cbranch_scc1 CK39G
v_max_u32 v240, v123, v127
v_cmp_eq_u32 vcc, -1, v240
s_cbranch_vccnz CK39
s_branch CK39D
CK39G:
s_mov_b32 s9, 0
CK39D:
v_max3_u32 v240, v131, v135, v139
v_max3_u32 v240, v240, v143, v147
v_max3_u32 v240, v240, v151, v155
v_max3_u32 v240, v240, v159, v163
v_max_u32 v240, v240, v167
v_cmp_eq_u32 vcc, -1, v240
s_cbranch_vccz CK39C
s_add_u32 s4, s6, 0x8800
buffer_load_dwordx4 v[128:131], v1, s[44:47], s4 offen sc1
s_add_u32 s4, s5, 0xe000
buffer_load_dwordx4 v[132:135], v1, s[44:47], s4 offen sc1
s_add_u32 s4, s6, 0xe000
buffer_load_dwordx4 v[136:139], v1, s[44:47], s4 offen sc1
buffer_load_dword v240, off, s[44:47], 0
s_add_u32 s4, s5, 0xe400
buffer_load_dwordx4 v[140:143], v1, s[44:47], s4 offen sc1
s_add_u32 s4, s6, 0xe400
buffer_load_dwordx4 v[144:147], v1, s[44:47], s4 offen sc1
s_add_u32 s4, s5, 0xe800
buffer_load_dwordx4 v[148:151], v1, s[44:47], s4 offen sc1
s_add_u32 s4, s6, 0xe800
buffer_load_dwordx4 v[152:155], v1, s[44:47], s4 offen sc1
s_add_u32 s4, s5, 0xec00
buffer_load_dwordx4 v[156:159], v1, s[44:47], s4 offen sc1
s_add_u32 s4, s6, 0xec00
buffer_load_dwordx4 v[160:163], v1, s[44:47], s4 offen sc1
s_add_u32 s4, s5, 0xf000
buffer_load_dwordx4 v[164:167], v1, s[44:47], s4 offen sc1
s_branch CK39C
CK40:
s_nop 3
s_add_u32 s4, s6, 0x8800
buffer_load_dwordx4 v[128:131], v1, s[44:47], s4 offen sc1
s_add_u32 s4, s5, 0xe000
buffer_load_dwordx4 v[132:135], v1, s[44:47], s4 offen sc1
s_waitcnt vmcnt(0)
s_sub_u32 s9, s9, 1
s_cbranch_scc1 CK40G
v_max_u32 v240, v131, v135
v_cmp_eq_u32 vcc, -1, v240
s_cbranch_vccnz CK40
s_branch CK40D
CK40G:
s_mov_b32 s9, 0
CK40D:
v_max3_u32 v240, v139, v143, v147
v_max3_u32 v240, v240, v151, v155
v_max3_u32 v240, v240, v159, v163
v_max3_u32 v240, v240, v167, v171
v_max_u32 v240, v240, v175
v_cmp_eq_u32 vcc, -1, v240
s_cbranch_vccz CK40C
s_add_u32 s4, s6, 0xe000
buffer_load_dwordx4 v[136:139], v1, s[44:47], s4 offen sc1
buffer_load_dword v240, off, s[44:47], 0
s_add_u32 s4, s5, 0xe400
buffer_load_dwordx4 v[140:143], v1, s[44:47], s4 offen sc1
s_add_u32 s4, s6, 0xe400
buffer_load_dwordx4 v[144:147], v1, s[44:47], s4 offen sc1
s_add_u32 s4, s5, 0xe800
buffer_load_dwordx4 v[148:151], v1, s[44:47], s4 offen sc1
s_add_u32 s4, s6, 0xe800
buffer_load_dwordx4 v[152:155], v1, s[44:47], s4 offen sc1
s_add_u32 s4, s5, 0xec00
buffer_load_dwordx4 v[156:159], v1, s[44:47], s4 offen sc1
s_add_u32 s4, s6, 0xec00
buffer_load_dwordx4 v[160:163], v1, s[44:47], s4 offen sc1
s_add_u32 s4, s5, 0xf000
buffer_load_dwordx4 v[164:167], v1, s[44:47], s4 offen sc1
s_add_u32 s4, s6, 0xf000
buffer_load_dwordx4 v[168:171], v1, s[44:47], s4 offen sc1
s_add_u32 s4, s5, 0xf400
buffer_load_dwordx4 v[172:175], v1, s[44:47], s4 offen sc1
s_branch CK40C
CK41:
s_nop 3
s_add_u32 s4, s6, 0xe000
buffer_load_dwordx4 v[136:139], v1, s[44:47], s4 offen sc1
s_add_u32 s4, s5, 0xe400
buffer_load_dwordx4 v[140:143], v1, s[44:47], s4 offen sc1
s_waitcnt vmcnt(0)
s_sub_u32 s9, s9, 1
s_cbranch_scc1 CK41G
v_max_u32 v240, v139, v143
v_cmp_eq_u32 vcc, -1, v240
s_cbranch_vccnz CK41
s_branch CK41D
CK41G:
s_mov_b32 s9, 0
CK41D:
v_max3_u32 v240, v147, v151, v155
v_max3_u32 v240, v240, v159, v163
v_max3_u32 v240, v240, v167, v171
v_max3_u32 v240, v240, v175, v179
v_max_u32 v240, v240, v103
v_cmp_eq_u32 vcc, -1, v240
s_cbranch_vccz CK41C
s_add_u32 s4, s6, 0xe400
buffer_load_dwordx4 v[144:147], v1, s[44:47], s4 offen sc1
s_add_u32 s4, s5, 0xe800
buffer_load_dwordx4 v[148:151], v1, s[44:47], s4 offen sc1
s_add_u32 s4, s6, 0xe800
buffer_load_dwordx4 v[152:155], v1, s[44:47], s4 offen sc1
s_add_u32 s4, s5, 0xec00
buffer_load_dwordx4 v[156:159], v1, s[44:47], s4 offen sc1
s_add_u32 s4, s6, 0xec00
buffer_load_dwordx4 v[160:163], v1, s[44:47], s4 offen sc1
s_add_u32 s4, s5, 0xf000
buffer_load_dwordx4 v[164:167], v1, s[44:47], s4 offen sc1
s_add_u32 s4, s6, 0xf000
buffer_load_dwordx4 v[168:171], v1, s[44:47], s4 offen sc1
s_add_u32 s4, s5, 0xf400
buffer_load_dwordx4 v[172:175], v1, s[44:47], s4 offen sc1
s_add_u32 s4, s6, 0xf400
buffer_load_dwordx4 v[176:179], v1, s[44:47], s4 offen sc1
s_add_u32 s4, s5, 0xf800
buffer_load_dwordx4 v[100:103], v1, s[44:47], s4 offen sc1
s_branch CK41C
CK42:
s_nop 3
s_add_u32 s4, s6, 0xe400
buffer_load_dwordx4 v[144:147], v1, s[44:47], s4 offen sc1
s_add_u32 s4, s5, 0xe800
buffer_load_dwordx4 v[148:151], v1, s[44:47], s4 offen sc1
s_waitcnt vmcnt(0)
s_sub_u32 s9, s9, 1
s_cbranch_scc1 CK42G
v_max_u32 v240, v147, v151
v_cmp_eq_u32 vcc, -1, v240
s_cbranch_vccnz CK42
s_branch CK42D
CK42G:
s_mov_b32 s9, 0
CK42D:
v_max3_u32 v240, v155, v159, v163
v_max3_u32 v240, v240, v167, v171
v_max3_u32 v240, v240, v175, v179
v_max3_u32 v240, v240, v103, v107
v_max_u32 v240, v240, v111
v_cmp_eq_u32 vcc, -1, v240
s_cbranch_vccz CK42C
s_add_u32 s4, s6, 0xe800
buffer_load_dwordx4 v[152:155], v1, s[44:47], s4 offen sc1
s_add_u32 s4, s5, 0xec00
buffer_load_dwordx4 v[156:159], v1, s[44:47], s4 offen sc1
s_add_u32 s4, s6, 0xec00
buffer_load_dwordx4 v[160:163], v1, s[44:47], s4 offen sc1
s_add_u32 s4, s5, 0xf000
buffer_load_dwordx4 v[164:167], v1, s[44:47], s4 offen sc1
s_add_u32 s4, s6, 0xf000
buffer_load_dwordx4 v[168:171], v1, s[44:47], s4 offen sc1
s_add_u32 s4, s5, 0xf400
buffer_load_dwordx4 v[172:175], v1, s[44:47], s4 offen sc1
s_add_u32 s4, s6, 0xf400
buffer_load_dwordx4 v[176:179], v1, s[44:47], s4 offen sc1
s_add_u32 s4, s5, 0xf800
buffer_load_dwordx4 v[100:103], v1, s[44:47], s4 offen sc1
s_add_u32 s4, s6, 0xf800
buffer_load_dwordx4 v[104:107], v1, s[44:47], s4 offen sc1
s_add_u32 s4, s6, 0x15000
buffer_load_dwordx4 v[108:111], v1, s[44:47], s4 offen sc1
s_branch CK42C
CK43:
s_nop 3
s_add_u32 s4, s6, 0xe800
buffer_load_dwordx4 v[152:155], v1, s[44:47], s4 offen sc1
s_add_u32 s4, s5, 0xec00
buffer_load_dwordx4 v[156:159], v1, s[44:47], s4 offen sc1
s_waitcnt vmcnt(0)
s_sub_u32 s9, s9, 1
s_cbranch_scc1 CK43G
v_max_u32 v240, v155, v159
v_cmp_eq_u32 vcc, -1, v240
s_cbranch_vccnz CK43
s_branch CK43D
CK43G:
s_mov_b32 s9, 0
CK43D:
v_max3_u32 v240, v163, v167, v171
v_max3_u32 v240, v240, v175, v179
v_max3_u32 v240, v240, v103, v107
v_max3_u32 v240, v240, v111, v115
v_max_u32 v240, v240, v119
v_cmp_eq_u32 vcc, -1, v240
s_cbranch_vccz CK43C
s_add_u32 s4, s6, 0xec00
buffer_load_dwordx4 v[160:163], v1, s[44:47], s4 offen sc1
s_add_u32 s4, s5, 0xf000
buffer_load_dwordx4 v[164:167], v1, s[44:47], s4 offen sc1
s_add_u32 s4, s6, 0xf000
buffer_load_dwordx4 v[168:171], v1, s[44:47], s4 offen sc1
s_add_u32 s4, s5, 0xf400
buffer_load_dwordx4 v[172:175], v1, s[44:47], s4 offen sc1
s_add_u32 s4, s6, 0xf400
buffer_load_dwordx4 v[176:179], v1, s[44:47], s4 offen sc1
s_add_u32 s4, s5, 0xf800
buffer_load_dwordx4 v[100:103], v1, s[44:47], s4 offen sc1
s_add_u32 s4, s6, 0xf800
buffer_load_dwordx4 v[104:107], v1, s[44:47], s4 offen sc1
s_add_u32 s4, s6, 0x15000
buffer_load_dwordx4 v[108:111], v1, s[44:47], s4 offen sc1
s_add_u32 s4, s6, 0x15400
buffer_load_dwordx4 v[112:115], v1, s[44:47], s4 offen sc1
s_add_u32 s4, s6, 0x15800
buffer_load_dwordx4 v[116:119], v1, s[44:47], s4 offen sc1
s_branch CK43C
CK44:
s_nop 3
s_add_u32 s4, s6, 0xec00
buffer_load_dwordx4 v[160:163], v1, s[44:47], s4 offen sc1
s_add_u32 s4, s5, 0xf000
buffer_load_dwordx4 v[164:167], v1, s[44:47], s4 offen sc1
s_waitcnt vmcnt(0)
s_sub_u32 s9, s9, 1
s_cbranch_scc1 CK44G
v_max_u32 v240, v163, v167
v_cmp_eq_u32 vcc, -1, v240
s_cbranch_vccnz CK44
s_branch CK44D
CK44G:
s_mov_b32 s9, 0
CK44D:
v_max3_u32 v240, v171, v175, v179
v_max3_u32 v240, v240, v103, v107
v_max3_u32 v240, v240, v111, v115
v_max3_u32 v240, v240, v119, v123
v_max_u32 v240, v240, v127
v_cmp_eq_u32 vcc, -1, v240
s_cbranch_vccz CK44C
s_add_u32 s4, s6, 0xf000
buffer_load_dwordx4 v[168:171], v1, s[44:47], s4 offen sc1
s_add_u32 s4, s5, 0xf400
buffer_load_dwordx4 v[172:175], v1, s[44:47], s4 offen sc1
s_add_u32 s4, s6, 0xf400
buffer_load_dwordx4 v[176:179], v1, s[44:47], s4 offen sc1
s_add_u32 s4, s5, 0xf800
buffer_load_dwordx4 v[100:103], v1, s[44:47], s4 offen sc1
s_add_u32 s4, s6, 0xf800
buffer_load_dwordx4 v[104:107], v1, s[44:47], s4 offen sc1
s_add_u32 s4, s6, 0x15000
buffer_load_dwordx4 v[108:111], v1, s[44:47], s4 offen sc1
s_add_u32 s4, s6, 0x15400
buffer_load_dwordx4 v[112:115], v1, s[44:47], s4 offen sc1
s_add_u32 s4, s6, 0x15800
buffer_load_dwordx4 v[116:119], v1, s[44:47], s4 offen sc1
s_add_u32 s4, s6, 0x15c00
buffer_load_dwordx4 v[120:123], v1, s[44:47], s4 offen sc1
s_add_u32 s4, s6, 0x16000
buffer_load_dwordx4 v[124:127], v1, s[44:47], s4 offen sc1
s_branch CK44C
CK45:
s_nop 3
s_add_u32 s4, s6, 0xf000
buffer_load_dwordx4 v[168:171], v1, s[44:47], s4 offen sc1
s_add_u32 s4, s5, 0xf400
buffer_load_dwordx4 v[172:175], v1, s[44:47], s4 offen sc1
s_waitcnt vmcnt(0)
s_sub_u32 s9, s9, 1
s_cbranch_scc1 CK45G
v_max_u32 v240, v171, v175
v_cmp_eq_u32 vcc, -1, v240
s_cbranch_vccnz CK45
s_branch CK45D
CK45G:
s_mov_b32 s9, 0
CK45D:
v_max3_u32 v240, v179, v103, v107
v_max3_u32 v240, v240, v111, v115
v_max3_u32 v240, v240, v119, v123
v_max3_u32 v240, v240, v127, v131
v_max_u32 v240, v240, v135
v_cmp_eq_u32 vcc, -1, v240
s_cbranch_vccz CK45C
s_add_u32 s4, s6, 0xf400
buffer_load_dwordx4 v[176:179], v1, s[44:47], s4 offen sc1
s_add_u32 s4, s5, 0xf800
buffer_load_dwordx4 v[100:103], v1, s[44:47], s4 offen sc1
s_add_u32 s4, s6, 0xf800
buffer_load_dwordx4 v[104:107], v1, s[44:47], s4 offen sc1
s_add_u32 s4, s6, 0x15000
buffer_load_dwordx4 v[108:111], v1, s[44:47], s4 offen sc1
s_add_u32 s4, s6, 0x15400
buffer_load_dwordx4 v[112:115], v1, s[44:47], s4 offen sc1
s_add_u32 s4, s6, 0x15800
buffer_load_dwordx4 v[116:119], v1, s[44:47], s4 offen sc1
s_add_u32 s4, s6, 0x15c00
buffer_load_dwordx4 v[120:123], v1, s[44:47], s4 offen sc1
s_add_u32 s4, s6, 0x16000
buffer_load_dwordx4 v[124:127], v1, s[44:47], s4 offen sc1
s_add_u32 s4, s6, 0x16400
buffer_load_dwordx4 v[128:131], v1, s[44:47], s4 offen sc1
s_add_u32 s4, s6, 0x16800
buffer_load_dwordx4 v[132:135], v1, s[44:47], s4 offen sc1
s_branch CK45C
CK46:
s_nop 3
s_add_u32 s4, s6, 0xf400
buffer_load_dwordx4 v[176:179], v1, s[44:47], s4 offen sc1
s_add_u32 s4, s5, 0xf800
buffer_load_dwordx4 v[100:103], v1, s[44:47], s4 offen sc1
s_waitcnt vmcnt(0)
s_sub_u32 s9, s9, 1
s_cbranch_scc1 CK46G
v_max_u32 v240, v179, v103
v_cmp_eq_u32 vcc, -1, v240
s_cbranch_vccnz CK46
s_branch CK46D
CK46G:
s_mov_b32 s9, 0
CK46D:
v_max3_u32 v240, v107, v111, v115
v_max3_u32 v240, v240, v119, v123
v_max3_u32 v240, v240, v127, v131
v_max3_u32 v240, v240, v135, v139
v_max_u32 v240, v240, v143
v_cmp_eq_u32 vcc, -1, v240
s_cbranch_vccz CK46C
s_add_u32 s4, s6, 0xf800
buffer_load_dwordx4 v[104:107], v1, s[44:47], s4 offen sc1
s_add_u32 s4, s6, 0x15000
buffer_load_dwordx4 v[108:111], v1, s[44:47], s4 offen sc1
s_add_u32 s4, s6, 0x15400
buffer_load_dwordx4 v[112:115], v1, s[44:47], s4 offen sc1
s_add_u32 s4, s6, 0x15800
buffer_load_dwordx4 v[116:119], v1, s[44:47], s4 offen sc1
s_add_u32 s4, s6, 0x15c00
buffer_load_dwordx4 v[120:123], v1, s[44:47], s4 offen sc1
s_add_u32 s4, s6, 0x16000
buffer_load_dwordx4 v[124:127], v1, s[44:47], s4 offen sc1
s_add_u32 s4, s6, 0x16400
buffer_load_dwordx4 v[128:131], v1, s[44:47], s4 offen sc1
s_add_u32 s4, s6, 0x16800
buffer_load_dwordx4 v[132:135], v1, s[44:47], s4 offen sc1
s_add_u32 s4, s5, 0x15000
buffer_load_dwordx4 v[136:139], v1, s[44:47], s4 offen sc1
s_add_u32 s4, s5, 0x15400
buffer_load_dwordx4 v[140:143], v1, s[44:47], s4 offen sc1
s_branch CK46C
CK47:
s_nop 3
s_add_u32 s4, s6, 0xf800
buffer_load_dwordx4 v[104:107], v1, s[44:47], s4 offen sc1
s_add_u32 s4, s6, 0x15000
buffer_load_dwordx4 v[108:111], v1, s[44:47], s4 offen sc1
s_waitcnt vmcnt(0)
s_sub_u32 s9, s9, 1
s_cbranch_scc1 CK47G
v_max_u32 v240, v107, v111
v_cmp_eq_u32 vcc, -1, v240
s_cbranch_vccnz CK47
s_branch CK47D
CK47G:
s_mov_b32 s9, 0
)ASM"
R"ASM(
CK47D:
v_max3_u32 v240, v115, v119, v123
v_max3_u32 v240, v240, v127, v131
v_max3_u32 v240, v240, v135, v139
v_max3_u32 v240, v240, v143, v147
v_max_u32 v240, v240, v151
v_cmp_eq_u32 vcc, -1, v240
s_cbranch_vccz CK47C
s_add_u32 s4, s6, 0x15400
buffer_load_dwordx4 v[112:115], v1, s[44:47], s4 offen sc1
s_add_u32 s4, s6, 0x15800
buffer_load_dwordx4 v[116:119], v1, s[44:47], s4 offen sc1
s_add_u32 s4, s6, 0x15c00
buffer_load_dwordx4 v[120:123], v1, s[44:47], s4 offen sc1
s_add_u32 s4, s6, 0x16000
buffer_load_dwordx4 v[124:127], v1, s[44:47], s4 offen sc1
s_add_u32 s4, s6, 0x16400
buffer_load_dwordx4 v[128:131], v1, s[44:47], s4 offen sc1
s_add_u32 s4, s6, 0x16800
buffer_load_dwordx4 v[132:135], v1, s[44:47], s4 offen sc1
s_add_u32 s4, s5, 0x15000
buffer_load_dwordx4 v[136:139], v1, s[44:47], s4 offen sc1
s_add_u32 s4, s5, 0x15400
buffer_load_dwordx4 v[140:143], v1, s[44:47], s4 offen sc1
s_add_u32 s4, s5, 0x15800
buffer_load_dwordx4 v[144:147], v1, s[44:47], s4 offen sc1
s_add_u32 s4, s5, 0x15c00
buffer_load_dwordx4 v[148:151], v1, s[44:47], s4 offen sc1
s_branch CK47C
CK48:
s_nop 3
s_add_u32 s4, s6, 0x15400
buffer_load_dwordx4 v[112:115], v1, s[44:47], s4 offen sc1
s_add_u32 s4, s6, 0x15800
buffer_load_dwordx4 v[116:119], v1, s[44:47], s4 offen sc1
s_waitcnt vmcnt(0)
s_sub_u32 s9, s9, 1
s_cbranch_scc1 CK48G
v_max_u32 v240, v115, v119
v_cmp_eq_u32 vcc, -1, v240
s_cbranch_vccnz CK48
s_branch CK48D
CK48G:
s_mov_b32 s9, 0
CK48D:
v_max3_u32 v240, v123, v127, v131
v_max3_u32 v240, v240, v135, v139
v_max3_u32 v240, v240, v143, v147
v_max3_u32 v240, v240, v151, v155
v_max_u32 v240, v240, v159
v_cmp_eq_u32 vcc, -1, v240
s_cbranch_vccz CK48C
s_add_u32 s4, s6, 0x15c00
buffer_load_dwordx4 v[120:123], v1, s[44:47], s4 offen sc1
s_add_u32 s4, s6, 0x16000
buffer_load_dwordx4 v[124:127], v1, s[44:47], s4 offen sc1
s_add_u32 s4, s6, 0x16400
buffer_load_dwordx4 v[128:131], v1, s[44:47], s4 offen sc1
s_add_u32 s4, s6, 0x16800
buffer_load_dwordx4 v[132:135], v1, s[44:47], s4 offen sc1
s_add_u32 s4, s5, 0x15000
buffer_load_dwordx4 v[136:139], v1, s[44:47], s4 offen sc1
s_add_u32 s4, s5, 0x15400
buffer_load_dwordx4 v[140:143], v1, s[44:47], s4 offen sc1
s_add_u32 s4, s5, 0x15800
buffer_load_dwordx4 v[144:147], v1, s[44:47], s4 offen sc1
s_add_u32 s4, s5, 0x15c00
buffer_load_dwordx4 v[148:151], v1, s[44:47], s4 offen sc1
s_add_u32 s4, s5, 0x16000
buffer_load_dwordx4 v[152:155], v1, s[44:47], s4 offen sc1
s_add_u32 s4, s5, 0x16400
buffer_load_dwordx4 v[156:159], v1, s[44:47], s4 offen sc1
s_branch CK48C
CK49:
s_nop 3
s_add_u32 s4, s6, 0x15c00
buffer_load_dwordx4 v[120:123], v1, s[44:47], s4 offen sc1
s_add_u32 s4, s6, 0x16000
buffer_load_dwordx4 v[124:127], v1, s[44:47], s4 offen sc1
s_waitcnt vmcnt(0)
s_sub_u32 s9, s9, 1
s_cbranch_scc1 CK49G
v_max_u32 v240, v123, v127
v_cmp_eq_u32 vcc, -1, v240
s_cbranch_vccnz CK49
s_branch CK49D
CK49G:
s_mov_b32 s9, 0
CK49D:
v_max3_u32 v240, v131, v135, v139
v_max3_u32 v240, v240, v143, v147
v_max3_u32 v240, v240, v151, v155
v_max3_u32 v240, v240, v159, v163
v_cmp_eq_u32 vcc, -1, v240
s_cbranch_vccz CK49C
s_add_u32 s4, s6, 0x16400
buffer_load_dwordx4 v[128:131], v1, s[44:47], s4 offen sc1
s_add_u32 s4, s6, 0x16800
buffer_load_dwordx4 v[132:135], v1, s[44:47], s4 offen sc1
s_add_u32 s4, s5, 0x15000
buffer_load_dwordx4 v[136:139], v1, s[44:47], s4 offen sc1
s_add_u32 s4, s5, 0x15400
buffer_load_dwordx4 v[140:143], v1, s[44:47], s4 offen sc1
s_add_u32 s4, s5, 0x15800
buffer_load_dwordx4 v[144:147], v1, s[44:47], s4 offen sc1
s_add_u32 s4, s5, 0x15c00
buffer_load_dwordx4 v[148:151], v1, s[44:47], s4 offen sc1
s_add_u32 s4, s5, 0x16000
buffer_load_dwordx4 v[152:155], v1, s[44:47], s4 offen sc1
s_add_u32 s4, s5, 0x16400
buffer_load_dwordx4 v[156:159], v1, s[44:47], s4 offen sc1
s_add_u32 s4, s5, 0x16800
buffer_load_dwordx4 v[160:163], v1, s[44:47], s4 offen sc1
s_branch CK49C
CK50:
s_nop 3
s_add_u32 s4, s6, 0x16400
buffer_load_dwordx4 v[128:131], v1, s[44:47], s4 offen sc1
s_add_u32 s4, s6, 0x16800
buffer_load_dwordx4 v[132:135], v1, s[44:47], s4 offen sc1
s_waitcnt vmcnt(0)
s_sub_u32 s9, s9, 1
s_cbranch_scc1 CK50G
v_max_u32 v240, v131, v135
v_cmp_eq_u32 vcc, -1, v240
s_cbranch_vccnz CK50
s_branch CK50D
CK50G:
s_mov_b32 s9, 0
CK50D:
v_max3_u32 v240, v139, v143, v147
v_max3_u32 v240, v240, v151, v155
v_max3_u32 v240, v240, v159, v163
v_cmp_eq_u32 vcc, -1, v240
s_cbranch_vccz CK50C
s_add_u32 s4, s5, 0x15000
buffer_load_dwordx4 v[136:139], v1, s[44:47], s4 offen sc1
s_add_u32 s4, s5, 0x15400
buffer_load_dwordx4 v[140:143], v1, s[44:47], s4 offen sc1
s_add_u32 s4, s5, 0x15800
buffer_load_dwordx4 v[144:147], v1, s[44:47], s4 offen sc1
s_add_u32 s4, s5, 0x15c00
buffer_load_dwordx4 v[148:151], v1, s[44:47], s4 offen sc1
s_add_u32 s4, s5, 0x16000
buffer_load_dwordx4 v[152:155], v1, s[44:47], s4 offen sc1
s_add_u32 s4, s5, 0x16400
buffer_load_dwordx4 v[156:159], v1, s[44:47], s4 offen sc1
s_add_u32 s4, s5, 0x16800
buffer_load_dwordx4 v[160:163], v1, s[44:47], s4 offen sc1
s_branch CK50C
CK51:
s_nop 3
s_add_u32 s4, s5, 0x15000
buffer_load_dwordx4 v[136:139], v1, s[44:47], s4 offen sc1
s_add_u32 s4, s5, 0x15400
buffer_load_dwordx4 v[140:143], v1, s[44:47], s4 offen sc1
s_waitcnt vmcnt(0)
s_sub_u32 s9, s9, 1
s_cbranch_scc1 CK51G
v_max_u32 v240, v139, v143
v_cmp_eq_u32 vcc, -1, v240
s_cbranch_vccnz CK51
s_branch CK51D
CK51G:
s_mov_b32 s9, 0
CK51D:
v_max3_u32 v240, v147, v151, v155
v_max3_u32 v240, v240, v159, v163
v_cmp_eq_u32 vcc, -1, v240
s_cbranch_vccz CK51C
s_add_u32 s4, s5, 0x15800
buffer_load_dwordx4 v[144:147], v1, s[44:47], s4 offen sc1
s_add_u32 s4, s5, 0x15c00
buffer_load_dwordx4 v[148:151], v1, s[44:47], s4 offen sc1
s_add_u32 s4, s5, 0x16000
buffer_load_dwordx4 v[152:155], v1, s[44:47], s4 offen sc1
s_add_u32 s4, s5, 0x16400
buffer_load_dwordx4 v[156:159], v1, s[44:47], s4 offen sc1
s_add_u32 s4, s5, 0x16800
buffer_load_dwordx4 v[160:163], v1, s[44:47], s4 offen sc1
s_branch CK51C
CK52:
s_nop 3
s_add_u32 s4, s5, 0x15800
buffer_load_dwordx4 v[144:147], v1, s[44:47], s4 offen sc1
s_add_u32 s4, s5, 0x15c00
buffer_load_dwordx4 v[148:151], v1, s[44:47], s4 offen sc1
s_waitcnt vmcnt(0)
s_sub_u32 s9, s9, 1
s_cbranch_scc1 CK52G
v_max_u32 v240, v147, v151
v_cmp_eq_u32 vcc, -1, v240
s_cbranch_vccnz CK52
s_branch CK52D
CK52G:
s_mov_b32 s9, 0
CK52D:
v_max3_u32 v240, v155, v159, v163
v_cmp_eq_u32 vcc, -1, v240
s_cbranch_vccz CK52C
s_add_u32 s4, s5, 0x16000
buffer_load_dwordx4 v[152:155], v1, s[44:47], s4 offen sc1
s_add_u32 s4, s5, 0x16400
buffer_load_dwordx4 v[156:159], v1, s[44:47], s4 offen sc1
s_add_u32 s4, s5, 0x16800
buffer_load_dwordx4 v[160:163], v1, s[44:47], s4 offen sc1
s_branch CK52C
CK53:
s_nop 3
s_add_u32 s4, s5, 0x16000
buffer_load_dwordx4 v[152:155], v1, s[44:47], s4 offen sc1
s_add_u32 s4, s5, 0x16400
buffer_load_dwordx4 v[156:159], v1, s[44:47], s4 offen sc1
s_waitcnt vmcnt(0)
s_sub_u32 s9, s9, 1
s_cbranch_scc1 CK53G
v_max_u32 v240, v155, v159
v_cmp_eq_u32 vcc, -1, v240
s_cbranch_vccnz CK53
s_branch CK53D
CK53G:
s_mov_b32 s9, 0
CK53D:
v_mov_b32 v240, v163
v_cmp_eq_u32 vcc, -1, v240
s_cbranch_vccz CK53C
s_add_u32 s4, s5, 0x16800
buffer_load_dwordx4 v[160:163], v1, s[44:47], s4 offen sc1
s_branch CK53C
CK54:
s_nop 3
s_add_u32 s4, s5, 0x16800
buffer_load_dwordx4 v[160:163], v1, s[44:47], s4 offen sc1
s_waitcnt vmcnt(0)
s_sub_u32 s9, s9, 1
s_cbranch_scc1 CK54G
v_mov_b32 v240, v163
v_cmp_eq_u32 vcc, -1, v240
s_cbranch_vccnz CK54
s_branch CK54D
CK54G:
s_mov_b32 s9, 0
CK54D:
s_branch CK54C
CODE1:
PC6:
s_nop 7
s_nop 7
s_waitcnt vmcnt(0)
s_waitcnt lgkmcnt(0)
PC6S:
s_nop 3
s_add_u32 s75, s6, 0
buffer_load_dwordx4 v[4:7], v1, s[44:47], s75 offen sc1
buffer_load_dwordx4 v[8:11], v1, s[44:47], s75 offen offset:1024 sc1
buffer_load_dwordx4 v[28:31], v1, s[44:47], s75 offen offset:2048 sc1
buffer_load_dwordx4 v[32:35], v1, s[44:47], s75 offen offset:3072 sc1
s_add_u32 s75, s6, 0x1000
buffer_load_dwordx4 v[36:39], v1, s[44:47], s75 offen sc1
buffer_load_dwordx4 v[40:43], v1, s[44:47], s75 offen offset:1024 sc1
buffer_load_dwordx4 v[44:47], v1, s[44:47], s75 offen offset:2048 sc1
buffer_load_dwordx4 v[48:51], v1, s[44:47], s75 offen offset:3072 sc1
s_add_u32 s75, s6, 0x2000
buffer_load_dwordx4 v[52:55], v1, s[44:47], s75 offen sc1
buffer_load_dwordx4 v[56:59], v1, s[44:47], s75 offen offset:1024 sc1
buffer_load_dwordx4 v[60:63], v1, s[44:47], s75 offen offset:2048 sc1
buffer_load_dwordx4 v[64:67], v1, s[44:47], s75 offen offset:3072 sc1
s_add_u32 s75, s6, 0x3000
buffer_load_dwordx4 v[68:71], v1, s[44:47], s75 offen sc1
buffer_load_dwordx4 v[72:75], v1, s[44:47], s75 offen offset:1024 sc1
buffer_load_dwordx4 v[76:79], v1, s[44:47], s75 offen offset:2048 sc1
buffer_load_dwordx4 v[80:83], v1, s[44:47], s75 offen offset:3072 sc1
s_waitcnt vmcnt(0)
s_sub_u32 s9, s9, 1
s_cbranch_scc1 PC6SG
v_max3_u32 v240, v4, v5, v6
v_max3_u32 v240, v240, v7, v8
v_max3_u32 v240, v240, v9, v10
v_max3_u32 v240, v240, v11, v28
v_max3_u32 v240, v240, v29, v30
v_max3_u32 v240, v240, v31, v32
v_max3_u32 v240, v240, v33, v34
v_max3_u32 v240, v240, v35, v36
v_max3_u32 v240, v240, v37, v38
v_max3_u32 v240, v240, v39, v40
v_max3_u32 v240, v240, v41, v42
v_max3_u32 v240, v240, v43, v44
v_max3_u32 v240, v240, v45, v46
v_max3_u32 v240, v240, v47, v48
v_max3_u32 v240, v240, v49, v50
v_max3_u32 v240, v240, v51, v52
v_max3_u32 v240, v240, v53, v54
v_max3_u32 v240, v240, v55, v56
v_max3_u32 v240, v240, v57, v58
v_max3_u32 v240, v240, v59, v60
v_max3_u32 v240, v240, v61, v62
v_max3_u32 v240, v240, v63, v64
v_max3_u32 v240, v240, v65, v66
v_max3_u32 v240, v240, v67, v68
v_max3_u32 v240, v240, v69, v70
v_max3_u32 v240, v240, v71, v72
v_max3_u32 v240, v240, v73, v74
v_max3_u32 v240, v240, v75, v76
v_max3_u32 v240, v240, v77, v78
v_max3_u32 v240, v240, v79, v80
v_max3_u32 v240, v240, v81, v82
v_max_u32 v240, v240, v83
v_cmp_eq_u32 vcc, -1, v240
s_cbranch_vccnz PC6S
s_branch PC6SD
PC6SG:
s_mov_b32 s9, 0
PC6SD:
v_mfma_f32_16x16x32_bf16 v[100:103], v[12:15], v[4:7], 0
v_mfma_f32_16x16x32_bf16 v[104:107], v[16:19], v[4:7], 0
v_mfma_f32_16x16x32_bf16 v[100:103], v[12:15], v[8:11], v[100:103]
v_mfma_f32_16x16x32_bf16 v[104:107], v[16:19], v[8:11], v[104:107]
v_mfma_f32_16x16x32_bf16 v[100:103], v[12:15], v[28:31], v[100:103]
v_mfma_f32_16x16x32_bf16 v[104:107], v[16:19], v[28:31], v[104:107]
v_mfma_f32_16x16x32_bf16 v[100:103], v[12:15], v[32:35], v[100:103]
v_mfma_f32_16x16x32_bf16 v[104:107], v[16:19], v[32:35], v[104:107]
v_mfma_f32_16x16x32_bf16 v[100:103], v[12:15], v[36:39], v[100:103]
v_mfma_f32_16x16x32_bf16 v[104:107], v[16:19], v[36:39], v[104:107]
v_mfma_f32_16x16x32_bf16 v[100:103], v[12:15], v[40:43], v[100:103]
v_mfma_f32_16x16x32_bf16 v[104:107], v[16:19], v[40:43], v[104:107]
v_mfma_f32_16x16x32_bf16 v[100:103], v[12:15], v[44:47], v[100:103]
v_mfma_f32_16x16x32_bf16 v[104:107], v[16:19], v[44:47], v[104:107]
v_mfma_f32_16x16x32_bf16 v[100:103], v[12:15], v[48:51], v[100:103]
v_mfma_f32_16x16x32_bf16 v[104:107], v[16:19], v[48:51], v[104:107]
v_mfma_f32_16x16x32_bf16 v[100:103], v[12:15], v[52:55], v[100:103]
v_mfma_f32_16x16x32_bf16 v[104:107], v[16:19], v[52:55], v[104:107]
v_mfma_f32_16x16x32_bf16 v[100:103], v[12:15], v[56:59], v[100:103]
v_mfma_f32_16x16x32_bf16 v[104:107], v[16:19], v[56:59], v[104:107]
v_mfma_f32_16x16x32_bf16 v[100:103], v[12:15], v[60:63], v[100:103]
v_mfma_f32_16x16x32_bf16 v[104:107], v[16:19], v[60:63], v[104:107]
v_mfma_f32_16x16x32_bf16 v[100:103], v[12:15], v[64:67], v[100:103]
v_mfma_f32_16x16x32_bf16 v[104:107], v[16:19], v[64:67], v[104:107]
v_mfma_f32_16x16x32_bf16 v[100:103], v[12:15], v[68:71], v[100:103]
v_mfma_f32_16x16x32_bf16 v[104:107], v[16:19], v[68:71], v[104:107]
v_mfma_f32_16x16x32_bf16 v[100:103], v[12:15], v[72:75], v[100:103]
v_mfma_f32_16x16x32_bf16 v[104:107], v[16:19], v[72:75], v[104:107]
v_mfma_f32_16x16x32_bf16 v[100:103], v[12:15], v[76:79], v[100:103]
v_mfma_f32_16x16x32_bf16 v[104:107], v[16:19], v[76:79], v[104:107]
v_mfma_f32_16x16x32_bf16 v[100:103], v[12:15], v[80:83], v[100:103]
v_mfma_f32_16x16x32_bf16 v[104:107], v[16:19], v[80:83], v[104:107]
s_waitcnt lgkmcnt(0)
s_nop 7
s_nop 7
s_branch PC6C
PC15:
s_nop 7
s_nop 7
s_waitcnt vmcnt(0)
s_waitcnt lgkmcnt(0)
PC15S:
s_nop 3
s_add_u32 s4, s7, 0
buffer_load_dwordx4 v[20:23], v1, s[44:47], s4 offen sc1
buffer_load_dwordx4 v[24:27], v1, s[44:47], s4 offen offset:1024 sc1
s_add_u32 s4, s7, 0x800
buffer_load_dwordx4 v[28:31], v1, s[44:47], s4 offen sc1
buffer_load_dwordx4 v[32:35], v1, s[44:47], s4 offen offset:1024 sc1
s_add_u32 s4, s7, 0x1000
buffer_load_dwordx4 v[36:39], v1, s[44:47], s4 offen sc1
buffer_load_dwordx4 v[40:43], v1, s[44:47], s4 offen offset:1024 sc1
s_add_u32 s4, s7, 0x1800
buffer_load_dwordx4 v[44:47], v1, s[44:47], s4 offen sc1
buffer_load_dwordx4 v[48:51], v1, s[44:47], s4 offen offset:1024 sc1
s_add_u32 s4, s7, 0x2000
buffer_load_dwordx4 v[52:55], v1, s[44:47], s4 offen sc1
buffer_load_dwordx4 v[56:59], v1, s[44:47], s4 offen offset:1024 sc1
s_add_u32 s4, s7, 0x2800
buffer_load_dwordx4 v[60:63], v1, s[44:47], s4 offen sc1
buffer_load_dwordx4 v[64:67], v1, s[44:47], s4 offen offset:1024 sc1
s_add_u32 s4, s7, 0x3000
buffer_load_dwordx4 v[68:71], v1, s[44:47], s4 offen sc1
buffer_load_dwordx4 v[72:75], v1, s[44:47], s4 offen offset:1024 sc1
s_add_u32 s4, s7, 0x3800
buffer_load_dwordx4 v[76:79], v1, s[44:47], s4 offen sc1
buffer_load_dwordx4 v[80:83], v1, s[44:47], s4 offen offset:1024 sc1
s_waitcnt vmcnt(0)
s_sub_u32 s9, s9, 1
s_cbranch_scc1 PC15SG
v_max3_u32 v240, v20, v21, v22
v_max3_u32 v240, v240, v23, v24
v_max3_u32 v240, v240, v25, v26
v_max3_u32 v240, v240, v27, v28
v_max3_u32 v240, v240, v29, v30
v_max3_u32 v240, v240, v31, v32
v_max3_u32 v240, v240, v33, v34
v_max3_u32 v240, v240, v35, v36
v_max3_u32 v240, v240, v37, v38
v_max3_u32 v240, v240, v39, v40
v_max3_u32 v240, v240, v41, v42
v_max3_u32 v240, v240, v43, v44
v_max3_u32 v240, v240, v45, v46
v_max3_u32 v240, v240, v47, v48
v_max3_u32 v240, v240, v49, v50
v_max3_u32 v240, v240, v51, v52
v_max3_u32 v240, v240, v53, v54
)ASM"
R"ASM(
v_max3_u32 v240, v240, v55, v56
v_max3_u32 v240, v240, v57, v58
v_max3_u32 v240, v240, v59, v60
v_max3_u32 v240, v240, v61, v62
v_max3_u32 v240, v240, v63, v64
v_max3_u32 v240, v240, v65, v66
v_max3_u32 v240, v240, v67, v68
v_max3_u32 v240, v240, v69, v70
v_max3_u32 v240, v240, v71, v72
v_max3_u32 v240, v240, v73, v74
v_max3_u32 v240, v240, v75, v76
v_max3_u32 v240, v240, v77, v78
v_max3_u32 v240, v240, v79, v80
v_max3_u32 v240, v240, v81, v82
v_max_u32 v240, v240, v83
v_cmp_eq_u32 vcc, -1, v240
s_cbranch_vccnz PC15S
s_branch PC15SD
PC15SG:
s_mov_b32 s9, 0
PC15SD:
v_lshlrev_b32 v4, 16, v108
v_and_b32 v5, 0xffff0000, v108
v_lshlrev_b32 v6, 16, v109
v_and_b32 v7, 0xffff0000, v109
v_lshlrev_b32 v8, 16, v110
v_and_b32 v9, 0xffff0000, v110
v_lshlrev_b32 v10, 16, v111
v_and_b32 v11, 0xffff0000, v111
v_lshlrev_b32 v12, 16, v112
v_and_b32 v13, 0xffff0000, v112
v_lshlrev_b32 v14, 16, v113
v_and_b32 v15, 0xffff0000, v113
v_lshlrev_b32 v16, 16, v114
v_and_b32 v17, 0xffff0000, v114
v_lshlrev_b32 v18, 16, v115
v_and_b32 v19, 0xffff0000, v115
v_mfma_f32_16x16x32_bf16 v[4:7], v[84:87], v[20:23], v[4:7]
v_mfma_f32_16x16x32_bf16 v[8:11], v[88:91], v[20:23], v[8:11]
v_mfma_f32_16x16x32_bf16 v[12:15], v[92:95], v[20:23], v[12:15]
v_mfma_f32_16x16x32_bf16 v[16:19], v[96:99], v[20:23], v[16:19]
v_mfma_f32_16x16x32_bf16 v[4:7], v[100:103], v[24:27], v[4:7]
v_mfma_f32_16x16x32_bf16 v[8:11], v[104:107], v[24:27], v[8:11]
v_mfma_f32_16x16x32_bf16 v[12:15], v[116:119], v[24:27], v[12:15]
v_mfma_f32_16x16x32_bf16 v[16:19], v[120:123], v[24:27], v[16:19]
v_mfma_f32_16x16x32_bf16 v[4:7], v[124:127], v[28:31], v[4:7]
v_mfma_f32_16x16x32_bf16 v[8:11], v[128:131], v[28:31], v[8:11]
v_mfma_f32_16x16x32_bf16 v[12:15], v[132:135], v[28:31], v[12:15]
v_mfma_f32_16x16x32_bf16 v[16:19], v[136:139], v[28:31], v[16:19]
v_mfma_f32_16x16x32_bf16 v[4:7], v[140:143], v[32:35], v[4:7]
v_mfma_f32_16x16x32_bf16 v[8:11], v[144:147], v[32:35], v[8:11]
v_mfma_f32_16x16x32_bf16 v[12:15], v[148:151], v[32:35], v[12:15]
v_mfma_f32_16x16x32_bf16 v[16:19], v[152:155], v[32:35], v[16:19]
v_mfma_f32_16x16x32_bf16 v[4:7], v[156:159], v[36:39], v[4:7]
v_mfma_f32_16x16x32_bf16 v[8:11], v[160:163], v[36:39], v[8:11]
v_mfma_f32_16x16x32_bf16 v[12:15], v[164:167], v[36:39], v[12:15]
v_mfma_f32_16x16x32_bf16 v[16:19], v[168:171], v[36:39], v[16:19]
v_mfma_f32_16x16x32_bf16 v[4:7], v[172:175], v[40:43], v[4:7]
v_mfma_f32_16x16x32_bf16 v[8:11], v[176:179], v[40:43], v[8:11]
v_mfma_f32_16x16x32_bf16 v[12:15], v[180:183], v[40:43], v[12:15]
v_mfma_f32_16x16x32_bf16 v[16:19], v[192:195], v[40:43], v[16:19]
v_mfma_f32_16x16x32_bf16 v[4:7], v[196:199], v[44:47], v[4:7]
v_mfma_f32_16x16x32_bf16 v[8:11], v[200:203], v[44:47], v[8:11]
v_mfma_f32_16x16x32_bf16 v[12:15], v[204:207], v[44:47], v[12:15]
v_mfma_f32_16x16x32_bf16 v[16:19], v[208:211], v[44:47], v[16:19]
v_mfma_f32_16x16x32_bf16 v[4:7], a[112:115], v[48:51], v[4:7]
v_mfma_f32_16x16x32_bf16 v[8:11], a[116:119], v[48:51], v[8:11]
v_mfma_f32_16x16x32_bf16 v[12:15], a[120:123], v[48:51], v[12:15]
v_mfma_f32_16x16x32_bf16 v[16:19], a[124:127], v[48:51], v[16:19]
v_mfma_f32_16x16x32_bf16 v[4:7], a[128:131], v[52:55], v[4:7]
v_mfma_f32_16x16x32_bf16 v[8:11], a[132:135], v[52:55], v[8:11]
v_mfma_f32_16x16x32_bf16 v[12:15], a[136:139], v[52:55], v[12:15]
v_mfma_f32_16x16x32_bf16 v[16:19], a[140:143], v[52:55], v[16:19]
v_mfma_f32_16x16x32_bf16 v[4:7], a[144:147], v[56:59], v[4:7]
v_mfma_f32_16x16x32_bf16 v[8:11], a[148:151], v[56:59], v[8:11]
v_mfma_f32_16x16x32_bf16 v[12:15], a[152:155], v[56:59], v[12:15]
v_mfma_f32_16x16x32_bf16 v[16:19], a[156:159], v[56:59], v[16:19]
v_mfma_f32_16x16x32_bf16 v[4:7], a[160:163], v[60:63], v[4:7]
v_mfma_f32_16x16x32_bf16 v[8:11], a[164:167], v[60:63], v[8:11]
v_mfma_f32_16x16x32_bf16 v[12:15], a[168:171], v[60:63], v[12:15]
v_mfma_f32_16x16x32_bf16 v[16:19], a[172:175], v[60:63], v[16:19]
v_mfma_f32_16x16x32_bf16 v[4:7], a[176:179], v[64:67], v[4:7]
v_mfma_f32_16x16x32_bf16 v[8:11], a[180:183], v[64:67], v[8:11]
v_mfma_f32_16x16x32_bf16 v[12:15], a[184:187], v[64:67], v[12:15]
v_mfma_f32_16x16x32_bf16 v[16:19], a[188:191], v[64:67], v[16:19]
v_mfma_f32_16x16x32_bf16 v[4:7], a[192:195], v[68:71], v[4:7]
v_mfma_f32_16x16x32_bf16 v[8:11], a[196:199], v[68:71], v[8:11]
v_mfma_f32_16x16x32_bf16 v[12:15], a[200:203], v[68:71], v[12:15]
v_mfma_f32_16x16x32_bf16 v[16:19], a[204:207], v[68:71], v[16:19]
v_mfma_f32_16x16x32_bf16 v[4:7], a[208:211], v[72:75], v[4:7]
v_mfma_f32_16x16x32_bf16 v[8:11], a[212:215], v[72:75], v[8:11]
v_mfma_f32_16x16x32_bf16 v[12:15], a[216:219], v[72:75], v[12:15]
v_mfma_f32_16x16x32_bf16 v[16:19], a[220:223], v[72:75], v[16:19]
v_mfma_f32_16x16x32_bf16 v[4:7], a[224:227], v[76:79], v[4:7]
v_mfma_f32_16x16x32_bf16 v[8:11], a[228:231], v[76:79], v[8:11]
v_mfma_f32_16x16x32_bf16 v[12:15], a[232:235], v[76:79], v[12:15]
v_mfma_f32_16x16x32_bf16 v[16:19], a[236:239], v[76:79], v[16:19]
v_mfma_f32_16x16x32_bf16 v[4:7], a[240:243], v[80:83], v[4:7]
v_mfma_f32_16x16x32_bf16 v[8:11], a[244:247], v[80:83], v[8:11]
v_mfma_f32_16x16x32_bf16 v[12:15], a[248:251], v[80:83], v[12:15]
v_mfma_f32_16x16x32_bf16 v[16:19], a[252:255], v[80:83], v[16:19]
s_waitcnt lgkmcnt(0)
s_nop 7
s_nop 7
s_branch PC15C
PC26:
s_nop 7
s_nop 7
s_waitcnt vmcnt(0)
s_waitcnt lgkmcnt(0)
PC26S:
s_nop 3
s_add_u32 s4, s57, 0
buffer_load_dwordx4 v[84:87], v1, s[44:47], s4 offen sc1
buffer_load_dwordx4 v[88:91], v1, s[44:47], s4 offen offset:1024 sc1
buffer_load_dwordx4 v[92:95], v1, s[44:47], s4 offen offset:2048 sc1
buffer_load_dwordx4 v[96:99], v1, s[44:47], s4 offen offset:3072 sc1
s_add_u32 s4, s57, 0x1000
buffer_load_dwordx4 v[100:103], v1, s[44:47], s4 offen sc1
buffer_load_dwordx4 v[104:107], v1, s[44:47], s4 offen offset:1024 sc1
buffer_load_dwordx4 v[108:111], v1, s[44:47], s4 offen offset:2048 sc1
buffer_load_dwordx4 v[112:115], v1, s[44:47], s4 offen offset:3072 sc1
s_add_u32 s4, s57, 0x2000
buffer_load_dwordx4 v[116:119], v1, s[44:47], s4 offen sc1
buffer_load_dwordx4 v[120:123], v1, s[44:47], s4 offen offset:1024 sc1
buffer_load_dwordx4 v[124:127], v1, s[44:47], s4 offen offset:2048 sc1
buffer_load_dwordx4 v[128:131], v1, s[44:47], s4 offen offset:3072 sc1
s_add_u32 s4, s57, 0x3000
buffer_load_dwordx4 v[132:135], v1, s[44:47], s4 offen sc1
buffer_load_dwordx4 v[136:139], v1, s[44:47], s4 offen offset:1024 sc1
buffer_load_dwordx4 v[140:143], v1, s[44:47], s4 offen offset:2048 sc1
buffer_load_dwordx4 v[144:147], v1, s[44:47], s4 offen offset:3072 sc1
s_add_u32 s4, s57, 0x4000
buffer_load_dwordx4 v[148:151], v1, s[44:47], s4 offen sc1
buffer_load_dwordx4 v[152:155], v1, s[44:47], s4 offen offset:1024 sc1
buffer_load_dwordx4 v[156:159], v1, s[44:47], s4 offen offset:2048 sc1
buffer_load_dwordx4 v[160:163], v1, s[44:47], s4 offen offset:3072 sc1
s_add_u32 s4, s57, 0x5000
buffer_load_dwordx4 v[164:167], v1, s[44:47], s4 offen sc1
buffer_load_dwordx4 v[168:171], v1, s[44:47], s4 offen offset:1024 sc1
buffer_load_dwordx4 v[172:175], v1, s[44:47], s4 offen offset:2048 sc1
buffer_load_dwordx4 v[176:179], v1, s[44:47], s4 offen offset:3072 sc1
s_add_u32 s4, s57, 0x6000
buffer_load_dwordx4 v[180:183], v1, s[44:47], s4 offen sc1
buffer_load_dwordx4 v[192:195], v1, s[44:47], s4 offen offset:1024 sc1
buffer_load_dwordx4 v[196:199], v1, s[44:47], s4 offen offset:2048 sc1
buffer_load_dwordx4 v[200:203], v1, s[44:47], s4 offen offset:3072 sc1
s_waitcnt vmcnt(0)
s_sub_u32 s9, s9, 1
s_cbranch_scc1 PC26SG
v_max3_u32 v240, v84, v85, v86
v_max3_u32 v240, v240, v87, v88
v_max3_u32 v240, v240, v89, v90
v_max3_u32 v240, v240, v91, v92
v_max3_u32 v240, v240, v93, v94
v_max3_u32 v240, v240, v95, v96
v_max3_u32 v240, v240, v97, v98
v_max3_u32 v240, v240, v99, v100
v_max3_u32 v240, v240, v101, v102
v_max3_u32 v240, v240, v103, v104
v_max3_u32 v240, v240, v105, v106
v_max3_u32 v240, v240, v107, v108
v_max3_u32 v240, v240, v109, v110
v_max3_u32 v240, v240, v111, v112
v_max3_u32 v240, v240, v113, v114
v_max3_u32 v240, v240, v115, v116
v_max3_u32 v240, v240, v117, v118
v_max3_u32 v240, v240, v119, v120
v_max3_u32 v240, v240, v121, v122
v_max3_u32 v240, v240, v123, v124
v_max3_u32 v240, v240, v125, v126
v_max3_u32 v240, v240, v127, v128
v_max3_u32 v240, v240, v129, v130
v_max3_u32 v240, v240, v131, v132
v_max3_u32 v240, v240, v133, v134
v_max3_u32 v240, v240, v135, v136
v_max3_u32 v240, v240, v137, v138
v_max3_u32 v240, v240, v139, v140
v_max3_u32 v240, v240, v141, v142
v_max3_u32 v240, v240, v143, v144
v_max3_u32 v240, v240, v145, v146
v_max3_u32 v240, v240, v147, v148
v_max3_u32 v240, v240, v149, v150
v_max3_u32 v240, v240, v151, v152
v_max3_u32 v240, v240, v153, v154
v_max3_u32 v240, v240, v155, v156
v_max3_u32 v240, v240, v157, v158
v_max3_u32 v240, v240, v159, v160
v_max3_u32 v240, v240, v161, v162
v_max3_u32 v240, v240, v163, v164
v_max3_u32 v240, v240, v165, v166
v_max3_u32 v240, v240, v167, v168
v_max3_u32 v240, v240, v169, v170
v_max3_u32 v240, v240, v171, v172
v_max3_u32 v240, v240, v173, v174
v_max3_u32 v240, v240, v175, v176
v_max3_u32 v240, v240, v177, v178
v_max3_u32 v240, v240, v179, v180
v_max3_u32 v240, v240, v181, v182
v_max3_u32 v240, v240, v183, v192
v_max3_u32 v240, v240, v193, v194
v_max3_u32 v240, v240, v195, v196
v_max3_u32 v240, v240, v197, v198
v_max3_u32 v240, v240, v199, v200
v_max3_u32 v240, v240, v201, v202
v_max_u32 v240, v240, v203
v_cmp_eq_u32 vcc, -1, v240
s_cbranch_vccnz PC26S
s_branch PC26SD
PC26SG:
s_mov_b32 s9, 0
PC26SD:
s_sub_u32 s71, s82, s88
s_add_u32 s4, s71, 0
buffer_load_dwordx4 a[224:227], v1, s[44:47], s4 offen sc1
s_waitcnt vmcnt(0)
v_mfma_f32_32x32x16_bf16 v[4:19], a[224:227], v[84:87], 0
s_add_u32 s4, s71, 0x400
buffer_load_dwordx4 a[228:231], v1, s[44:47], s4 offen sc1
s_waitcnt vmcnt(0)
v_mfma_f32_32x32x16_bf16 v[20:35], a[228:231], v[84:87], 0
s_add_u32 s4, s71, 0x800
buffer_load_dwordx4 a[232:235], v1, s[44:47], s4 offen sc1
s_waitcnt vmcnt(0)
v_mfma_f32_32x32x16_bf16 v[4:19], a[232:235], v[88:91], v[4:19]
s_add_u32 s4, s71, 0xc00
buffer_load_dwordx4 a[236:239], v1, s[44:47], s4 offen sc1
s_waitcnt vmcnt(0)
v_mfma_f32_32x32x16_bf16 v[20:35], a[236:239], v[88:91], v[20:35]
s_add_u32 s4, s71, 0x1000
buffer_load_dwordx4 a[240:243], v1, s[44:47], s4 offen sc1
s_waitcnt vmcnt(0)
v_mfma_f32_32x32x16_bf16 v[4:19], a[240:243], v[92:95], v[4:19]
s_add_u32 s4, s71, 0x1400
buffer_load_dwordx4 a[244:247], v1, s[44:47], s4 offen sc1
s_waitcnt vmcnt(0)
v_mfma_f32_32x32x16_bf16 v[20:35], a[244:247], v[92:95], v[20:35]
s_add_u32 s4, s71, 0x1800
buffer_load_dwordx4 a[248:251], v1, s[44:47], s4 offen sc1
s_waitcnt vmcnt(0)
v_mfma_f32_32x32x16_bf16 v[4:19], a[248:251], v[96:99], v[4:19]
s_add_u32 s4, s71, 0x1c00
buffer_load_dwordx4 a[252:255], v1, s[44:47], s4 offen sc1
s_waitcnt vmcnt(0)
v_mfma_f32_32x32x16_bf16 v[20:35], a[252:255], v[96:99], v[20:35]
s_add_u32 s4, s71, 0x2000
buffer_load_dwordx4 a[112:115], v1, s[44:47], s4 offen sc1
s_waitcnt vmcnt(0)
v_mfma_f32_32x32x16_bf16 v[4:19], a[112:115], v[100:103], v[4:19]
s_add_u32 s4, s71, 0x2400
buffer_load_dwordx4 a[116:119], v1, s[44:47], s4 offen sc1
s_waitcnt vmcnt(0)
v_mfma_f32_32x32x16_bf16 v[20:35], a[116:119], v[100:103], v[20:35]
s_add_u32 s4, s71, 0x2800
buffer_load_dwordx4 a[120:123], v1, s[44:47], s4 offen sc1
s_waitcnt vmcnt(0)
v_mfma_f32_32x32x16_bf16 v[4:19], a[120:123], v[104:107], v[4:19]
s_add_u32 s4, s71, 0x2c00
buffer_load_dwordx4 a[124:127], v1, s[44:47], s4 offen sc1
s_waitcnt vmcnt(0)
v_mfma_f32_32x32x16_bf16 v[20:35], a[124:127], v[104:107], v[20:35]
s_add_u32 s4, s71, 0x3000
buffer_load_dwordx4 a[224:227], v1, s[44:47], s4 offen sc1
s_waitcnt vmcnt(0)
v_mfma_f32_32x32x16_bf16 v[4:19], a[224:227], v[108:111], v[4:19]
s_add_u32 s4, s71, 0x3400
buffer_load_dwordx4 a[228:231], v1, s[44:47], s4 offen sc1
s_waitcnt vmcnt(0)
v_mfma_f32_32x32x16_bf16 v[20:35], a[228:231], v[108:111], v[20:35]
s_add_u32 s4, s71, 0x3800
buffer_load_dwordx4 a[232:235], v1, s[44:47], s4 offen sc1
s_waitcnt vmcnt(0)
v_mfma_f32_32x32x16_bf16 v[4:19], a[232:235], v[112:115], v[4:19]
s_add_u32 s4, s71, 0x3c00
buffer_load_dwordx4 a[236:239], v1, s[44:47], s4 offen sc1
s_waitcnt vmcnt(0)
v_mfma_f32_32x32x16_bf16 v[20:35], a[236:239], v[112:115], v[20:35]
s_add_u32 s4, s71, 0x4000
buffer_load_dwordx4 a[240:243], v1, s[44:47], s4 offen sc1
s_waitcnt vmcnt(0)
v_mfma_f32_32x32x16_bf16 v[4:19], a[240:243], v[116:119], v[4:19]
s_add_u32 s4, s71, 0x4400
buffer_load_dwordx4 a[244:247], v1, s[44:47], s4 offen sc1
s_waitcnt vmcnt(0)
v_mfma_f32_32x32x16_bf16 v[20:35], a[244:247], v[116:119], v[20:35]
s_add_u32 s4, s71, 0x4800
buffer_load_dwordx4 a[248:251], v1, s[44:47], s4 offen sc1
s_waitcnt vmcnt(0)
v_mfma_f32_32x32x16_bf16 v[4:19], a[248:251], v[120:123], v[4:19]
s_add_u32 s4, s71, 0x4c00
buffer_load_dwordx4 a[252:255], v1, s[44:47], s4 offen sc1
s_waitcnt vmcnt(0)
v_mfma_f32_32x32x16_bf16 v[20:35], a[252:255], v[120:123], v[20:35]
s_add_u32 s4, s71, 0x5000
buffer_load_dwordx4 a[112:115], v1, s[44:47], s4 offen sc1
s_waitcnt vmcnt(0)
v_mfma_f32_32x32x16_bf16 v[4:19], a[112:115], v[124:127], v[4:19]
s_add_u32 s4, s71, 0x5400
buffer_load_dwordx4 a[116:119], v1, s[44:47], s4 offen sc1
s_waitcnt vmcnt(0)
v_mfma_f32_32x32x16_bf16 v[20:35], a[116:119], v[124:127], v[20:35]
s_add_u32 s4, s71, 0x5800
buffer_load_dwordx4 a[120:123], v1, s[44:47], s4 offen sc1
s_waitcnt vmcnt(0)
v_mfma_f32_32x32x16_bf16 v[4:19], a[120:123], v[128:131], v[4:19]
s_add_u32 s4, s71, 0x5c00
buffer_load_dwordx4 a[124:127], v1, s[44:47], s4 offen sc1
s_waitcnt vmcnt(0)
v_mfma_f32_32x32x16_bf16 v[20:35], a[124:127], v[128:131], v[20:35]
s_add_u32 s4, s71, 0x6000
buffer_load_dwordx4 a[224:227], v1, s[44:47], s4 offen sc1
s_waitcnt vmcnt(0)
v_mfma_f32_32x32x16_bf16 v[4:19], a[224:227], v[132:135], v[4:19]
s_add_u32 s4, s71, 0x6400
buffer_load_dwordx4 a[228:231], v1, s[44:47], s4 offen sc1
s_waitcnt vmcnt(0)
v_mfma_f32_32x32x16_bf16 v[20:35], a[228:231], v[132:135], v[20:35]
s_add_u32 s4, s71, 0x6800
buffer_load_dwordx4 a[232:235], v1, s[44:47], s4 offen sc1
s_waitcnt vmcnt(0)
v_mfma_f32_32x32x16_bf16 v[4:19], a[232:235], v[136:139], v[4:19]
s_add_u32 s4, s71, 0x6c00
buffer_load_dwordx4 a[236:239], v1, s[44:47], s4 offen sc1
s_waitcnt vmcnt(0)
v_mfma_f32_32x32x16_bf16 v[20:35], a[236:239], v[136:139], v[20:35]
s_add_u32 s4, s71, 0x7000
buffer_load_dwordx4 a[240:243], v1, s[44:47], s4 offen sc1
s_waitcnt vmcnt(0)
v_mfma_f32_32x32x16_bf16 v[4:19], a[240:243], v[140:143], v[4:19]
s_add_u32 s4, s71, 0x7400
buffer_load_dwordx4 a[244:247], v1, s[44:47], s4 offen sc1
s_waitcnt vmcnt(0)
v_mfma_f32_32x32x16_bf16 v[20:35], a[244:247], v[140:143], v[20:35]
s_add_u32 s4, s71, 0x7800
buffer_load_dwordx4 a[248:251], v1, s[44:47], s4 offen sc1
s_waitcnt vmcnt(0)
v_mfma_f32_32x32x16_bf16 v[4:19], a[248:251], v[144:147], v[4:19]
s_add_u32 s4, s71, 0x7c00
buffer_load_dwordx4 a[252:255], v1, s[44:47], s4 offen sc1
s_waitcnt vmcnt(0)
v_mfma_f32_32x32x16_bf16 v[20:35], a[252:255], v[144:147], v[20:35]
s_add_u32 s4, s71, 0x8000
buffer_load_dwordx4 a[112:115], v1, s[44:47], s4 offen sc1
s_waitcnt vmcnt(0)
v_mfma_f32_32x32x16_bf16 v[4:19], a[112:115], v[148:151], v[4:19]
s_add_u32 s4, s71, 0x8400
buffer_load_dwordx4 a[116:119], v1, s[44:47], s4 offen sc1
s_waitcnt vmcnt(0)
v_mfma_f32_32x32x16_bf16 v[20:35], a[116:119], v[148:151], v[20:35]
s_add_u32 s4, s71, 0x8800
buffer_load_dwordx4 a[120:123], v1, s[44:47], s4 offen sc1
s_waitcnt vmcnt(0)
v_mfma_f32_32x32x16_bf16 v[4:19], a[120:123], v[152:155], v[4:19]
s_add_u32 s4, s71, 0x8c00
buffer_load_dwordx4 a[124:127], v1, s[44:47], s4 offen sc1
s_waitcnt vmcnt(0)
v_mfma_f32_32x32x16_bf16 v[20:35], a[124:127], v[152:155], v[20:35]
s_add_u32 s4, s71, 0x9000
buffer_load_dwordx4 a[224:227], v1, s[44:47], s4 offen sc1
s_waitcnt vmcnt(0)
v_mfma_f32_32x32x16_bf16 v[4:19], a[224:227], v[156:159], v[4:19]
s_add_u32 s4, s71, 0x9400
buffer_load_dwordx4 a[228:231], v1, s[44:47], s4 offen sc1
s_waitcnt vmcnt(0)
v_mfma_f32_32x32x16_bf16 v[20:35], a[228:231], v[156:159], v[20:35]
s_add_u32 s4, s71, 0x9800
buffer_load_dwordx4 a[232:235], v1, s[44:47], s4 offen sc1
s_waitcnt vmcnt(0)
v_mfma_f32_32x32x16_bf16 v[4:19], a[232:235], v[160:163], v[4:19]
s_add_u32 s4, s71, 0x9c00
buffer_load_dwordx4 a[236:239], v1, s[44:47], s4 offen sc1
s_waitcnt vmcnt(0)
v_mfma_f32_32x32x16_bf16 v[20:35], a[236:239], v[160:163], v[20:35]
s_add_u32 s4, s71, 0xa000
buffer_load_dwordx4 a[240:243], v1, s[44:47], s4 offen sc1
s_waitcnt vmcnt(0)
v_mfma_f32_32x32x16_bf16 v[4:19], a[240:243], v[164:167], v[4:19]
s_add_u32 s4, s71, 0xa400
buffer_load_dwordx4 a[244:247], v1, s[44:47], s4 offen sc1
s_waitcnt vmcnt(0)
v_mfma_f32_32x32x16_bf16 v[20:35], a[244:247], v[164:167], v[20:35]
s_add_u32 s4, s71, 0xa800
buffer_load_dwordx4 a[248:251], v1, s[44:47], s4 offen sc1
s_waitcnt vmcnt(0)
v_mfma_f32_32x32x16_bf16 v[4:19], a[248:251], v[168:171], v[4:19]
s_add_u32 s4, s71, 0xac00
buffer_load_dwordx4 a[252:255], v1, s[44:47], s4 offen sc1
s_waitcnt vmcnt(0)
v_mfma_f32_32x32x16_bf16 v[20:35], a[252:255], v[168:171], v[20:35]
s_add_u32 s4, s71, 0xb000
buffer_load_dwordx4 a[112:115], v1, s[44:47], s4 offen sc1
s_waitcnt vmcnt(0)
v_mfma_f32_32x32x16_bf16 v[4:19], a[112:115], v[172:175], v[4:19]
s_add_u32 s4, s71, 0xb400
buffer_load_dwordx4 a[116:119], v1, s[44:47], s4 offen sc1
s_waitcnt vmcnt(0)
v_mfma_f32_32x32x16_bf16 v[20:35], a[116:119], v[172:175], v[20:35]
s_add_u32 s4, s71, 0xb800
buffer_load_dwordx4 a[120:123], v1, s[44:47], s4 offen sc1
s_waitcnt vmcnt(0)
)ASM"
R"ASM(
v_mfma_f32_32x32x16_bf16 v[4:19], a[120:123], v[176:179], v[4:19]
s_add_u32 s4, s71, 0xbc00
buffer_load_dwordx4 a[124:127], v1, s[44:47], s4 offen sc1
s_waitcnt vmcnt(0)
v_mfma_f32_32x32x16_bf16 v[20:35], a[124:127], v[176:179], v[20:35]
s_add_u32 s4, s71, 0xc000
buffer_load_dwordx4 a[224:227], v1, s[44:47], s4 offen sc1
s_waitcnt vmcnt(0)
v_mfma_f32_32x32x16_bf16 v[4:19], a[224:227], v[180:183], v[4:19]
s_add_u32 s4, s71, 0xc400
buffer_load_dwordx4 a[228:231], v1, s[44:47], s4 offen sc1
s_waitcnt vmcnt(0)
v_mfma_f32_32x32x16_bf16 v[20:35], a[228:231], v[180:183], v[20:35]
s_add_u32 s4, s71, 0xc800
buffer_load_dwordx4 a[232:235], v1, s[44:47], s4 offen sc1
s_waitcnt vmcnt(0)
v_mfma_f32_32x32x16_bf16 v[4:19], a[232:235], v[192:195], v[4:19]
s_add_u32 s4, s71, 0xcc00
buffer_load_dwordx4 a[236:239], v1, s[44:47], s4 offen sc1
s_waitcnt vmcnt(0)
v_mfma_f32_32x32x16_bf16 v[20:35], a[236:239], v[192:195], v[20:35]
s_add_u32 s4, s71, 0xd000
buffer_load_dwordx4 a[240:243], v1, s[44:47], s4 offen sc1
s_waitcnt vmcnt(0)
v_mfma_f32_32x32x16_bf16 v[4:19], a[240:243], v[196:199], v[4:19]
s_add_u32 s4, s71, 0xd400
buffer_load_dwordx4 a[244:247], v1, s[44:47], s4 offen sc1
s_waitcnt vmcnt(0)
v_mfma_f32_32x32x16_bf16 v[20:35], a[244:247], v[196:199], v[20:35]
s_add_u32 s4, s71, 0xd800
buffer_load_dwordx4 a[248:251], v1, s[44:47], s4 offen sc1
s_waitcnt vmcnt(0)
v_mfma_f32_32x32x16_bf16 v[4:19], a[248:251], v[200:203], v[4:19]
s_add_u32 s4, s71, 0xdc00
buffer_load_dwordx4 a[252:255], v1, s[44:47], s4 offen sc1
s_waitcnt vmcnt(0)
v_mfma_f32_32x32x16_bf16 v[20:35], a[252:255], v[200:203], v[20:35]
v_mfma_f32_32x32x16_bf16 v[4:19], v[68:71], v[204:207], v[4:19]
v_mfma_f32_32x32x16_bf16 v[20:35], v[72:75], v[204:207], v[20:35]
ds_read_b128 a[112:115], v230 offset:24576
s_waitcnt lgkmcnt(0)
v_mfma_f32_32x32x16_bf16 v[36:51], a[112:115], v[84:87], 0
ds_read_b128 a[116:119], v230 offset:26624
s_waitcnt lgkmcnt(0)
v_mfma_f32_32x32x16_bf16 v[36:51], a[116:119], v[88:91], v[36:51]
s_nop 3
ds_read_b128 a[120:123], v230 offset:28672
ds_read_b128 a[124:127], v230 offset:30720
ds_read_b128 a[224:227], v230 offset:32768
ds_read_b128 a[228:231], v230 offset:34816
ds_read_b128 a[232:235], v230 offset:36864
ds_read_b128 a[236:239], v230 offset:38912
ds_read_b128 a[240:243], v230 offset:40960
s_waitcnt lgkmcnt(0)
s_nop 7
s_nop 7
s_branch PC26C
REDO:
s_nop 7
s_nop 7
s_waitcnt vmcnt(0)
s_waitcnt lgkmcnt(0)
REDOS0:
s_nop 3
s_add_u32 s4, s5, 0
buffer_load_dwordx4 v[100:103], v1, s[44:47], s4 offen sc1
s_add_u32 s4, s6, 0
buffer_load_dwordx4 v[104:107], v1, s[44:47], s4 offen sc1
s_add_u32 s4, s5, 0x400
buffer_load_dwordx4 v[108:111], v1, s[44:47], s4 offen sc1
s_add_u32 s4, s6, 0x400
buffer_load_dwordx4 v[112:115], v1, s[44:47], s4 offen sc1
s_add_u32 s4, s5, 0x800
buffer_load_dwordx4 v[116:119], v1, s[44:47], s4 offen sc1
s_add_u32 s4, s6, 0x800
buffer_load_dwordx4 v[120:123], v1, s[44:47], s4 offen sc1
s_add_u32 s4, s5, 0xc00
buffer_load_dwordx4 v[124:127], v1, s[44:47], s4 offen sc1
s_add_u32 s4, s6, 0xc00
buffer_load_dwordx4 v[128:131], v1, s[44:47], s4 offen sc1
s_add_u32 s4, s5, 0x1000
buffer_load_dwordx4 v[132:135], v1, s[44:47], s4 offen sc1
s_add_u32 s4, s6, 0x1000
buffer_load_dwordx4 v[136:139], v1, s[44:47], s4 offen sc1
s_add_u32 s4, s5, 0x1400
buffer_load_dwordx4 v[140:143], v1, s[44:47], s4 offen sc1
s_add_u32 s4, s6, 0x1400
buffer_load_dwordx4 v[144:147], v1, s[44:47], s4 offen sc1
s_add_u32 s4, s5, 0x1800
buffer_load_dwordx4 v[148:151], v1, s[44:47], s4 offen sc1
s_add_u32 s4, s6, 0x1800
buffer_load_dwordx4 v[152:155], v1, s[44:47], s4 offen sc1
s_waitcnt vmcnt(0)
s_sub_u32 s9, s9, 1
s_cbranch_scc1 REDOS0G
v_max3_u32 v240, v100, v101, v102
v_max3_u32 v240, v240, v103, v104
v_max3_u32 v240, v240, v105, v106
v_max3_u32 v240, v240, v107, v108
v_max3_u32 v240, v240, v109, v110
v_max3_u32 v240, v240, v111, v112
v_max3_u32 v240, v240, v113, v114
v_max3_u32 v240, v240, v115, v116
v_max3_u32 v240, v240, v117, v118
v_max3_u32 v240, v240, v119, v120
v_max3_u32 v240, v240, v121, v122
v_max3_u32 v240, v240, v123, v124
v_max3_u32 v240, v240, v125, v126
v_max3_u32 v240, v240, v127, v128
v_max3_u32 v240, v240, v129, v130
v_max3_u32 v240, v240, v131, v132
v_max3_u32 v240, v240, v133, v134
v_max3_u32 v240, v240, v135, v136
v_max3_u32 v240, v240, v137, v138
v_max3_u32 v240, v240, v139, v140
v_max3_u32 v240, v240, v141, v142
v_max3_u32 v240, v240, v143, v144
v_max3_u32 v240, v240, v145, v146
v_max3_u32 v240, v240, v147, v148
v_max3_u32 v240, v240, v149, v150
v_max3_u32 v240, v240, v151, v152
v_max3_u32 v240, v240, v153, v154
v_max_u32 v240, v240, v155
v_cmp_eq_u32 vcc, -1, v240
s_cbranch_vccnz REDOS0
s_branch REDOS0D
REDOS0G:
s_mov_b32 s9, 0
REDOS0D:
v_mfma_f32_32x32x16_bf16 v[20:35], a[4:7], v[100:103], 0
v_mfma_f32_32x32x16_bf16 v[4:19], a[0:3], v[100:103], 0
v_mfma_f32_32x32x16_bf16 v[84:99], a[4:7], v[104:107], 0
v_mfma_f32_32x32x16_bf16 v[68:83], a[0:3], v[104:107], 0
v_mfma_f32_32x32x16_bf16 v[20:35], a[12:15], v[108:111], v[20:35]
v_mfma_f32_32x32x16_bf16 v[4:19], a[8:11], v[108:111], v[4:19]
v_mfma_f32_32x32x16_bf16 v[84:99], a[12:15], v[112:115], v[84:99]
v_mfma_f32_32x32x16_bf16 v[68:83], a[8:11], v[112:115], v[68:83]
v_mfma_f32_32x32x16_bf16 v[20:35], a[20:23], v[116:119], v[20:35]
v_mfma_f32_32x32x16_bf16 v[4:19], a[16:19], v[116:119], v[4:19]
v_mfma_f32_32x32x16_bf16 v[84:99], a[20:23], v[120:123], v[84:99]
v_mfma_f32_32x32x16_bf16 v[68:83], a[16:19], v[120:123], v[68:83]
v_mfma_f32_32x32x16_bf16 v[20:35], a[28:31], v[124:127], v[20:35]
v_mfma_f32_32x32x16_bf16 v[4:19], a[24:27], v[124:127], v[4:19]
v_mfma_f32_32x32x16_bf16 v[84:99], a[28:31], v[128:131], v[84:99]
v_mfma_f32_32x32x16_bf16 v[68:83], a[24:27], v[128:131], v[68:83]
v_mfma_f32_32x32x16_bf16 v[20:35], a[36:39], v[132:135], v[20:35]
v_mfma_f32_32x32x16_bf16 v[4:19], a[32:35], v[132:135], v[4:19]
v_mfma_f32_32x32x16_bf16 v[84:99], a[36:39], v[136:139], v[84:99]
v_mfma_f32_32x32x16_bf16 v[68:83], a[32:35], v[136:139], v[68:83]
v_mfma_f32_32x32x16_bf16 v[20:35], a[44:47], v[140:143], v[20:35]
v_mfma_f32_32x32x16_bf16 v[4:19], a[40:43], v[140:143], v[4:19]
v_mfma_f32_32x32x16_bf16 v[84:99], a[44:47], v[144:147], v[84:99]
v_mfma_f32_32x32x16_bf16 v[68:83], a[40:43], v[144:147], v[68:83]
v_mfma_f32_32x32x16_bf16 v[20:35], a[52:55], v[148:151], v[20:35]
v_mfma_f32_32x32x16_bf16 v[4:19], a[48:51], v[148:151], v[4:19]
v_mfma_f32_32x32x16_bf16 v[84:99], a[52:55], v[152:155], v[84:99]
v_mfma_f32_32x32x16_bf16 v[68:83], a[48:51], v[152:155], v[68:83]
s_nop 7
s_nop 7
REDOS1:
s_nop 3
s_add_u32 s4, s5, 0x7000
buffer_load_dwordx4 v[156:159], v1, s[44:47], s4 offen sc1
s_add_u32 s4, s6, 0x7000
buffer_load_dwordx4 v[160:163], v1, s[44:47], s4 offen sc1
s_add_u32 s4, s5, 0x7400
buffer_load_dwordx4 v[164:167], v1, s[44:47], s4 offen sc1
s_add_u32 s4, s6, 0x7400
buffer_load_dwordx4 v[168:171], v1, s[44:47], s4 offen sc1
s_add_u32 s4, s5, 0x7800
buffer_load_dwordx4 v[172:175], v1, s[44:47], s4 offen sc1
s_add_u32 s4, s6, 0x7800
buffer_load_dwordx4 v[176:179], v1, s[44:47], s4 offen sc1
s_add_u32 s4, s5, 0x7c00
buffer_load_dwordx4 v[100:103], v1, s[44:47], s4 offen sc1
s_add_u32 s4, s6, 0x7c00
buffer_load_dwordx4 v[104:107], v1, s[44:47], s4 offen sc1
s_add_u32 s4, s5, 0x8000
buffer_load_dwordx4 v[108:111], v1, s[44:47], s4 offen sc1
s_add_u32 s4, s6, 0x8000
buffer_load_dwordx4 v[112:115], v1, s[44:47], s4 offen sc1
s_add_u32 s4, s5, 0x8400
buffer_load_dwordx4 v[116:119], v1, s[44:47], s4 offen sc1
s_add_u32 s4, s6, 0x8400
buffer_load_dwordx4 v[120:123], v1, s[44:47], s4 offen sc1
s_add_u32 s4, s5, 0x8800
buffer_load_dwordx4 v[124:127], v1, s[44:47], s4 offen sc1
s_add_u32 s4, s6, 0x8800
buffer_load_dwordx4 v[128:131], v1, s[44:47], s4 offen sc1
s_waitcnt vmcnt(0)
s_sub_u32 s9, s9, 1
s_cbranch_scc1 REDOS1G
v_max3_u32 v240, v156, v157, v158
v_max3_u32 v240, v240, v159, v160
v_max3_u32 v240, v240, v161, v162
v_max3_u32 v240, v240, v163, v164
v_max3_u32 v240, v240, v165, v166
v_max3_u32 v240, v240, v167, v168
v_max3_u32 v240, v240, v169, v170
v_max3_u32 v240, v240, v171, v172
v_max3_u32 v240, v240, v173, v174
v_max3_u32 v240, v240, v175, v176
v_max3_u32 v240, v240, v177, v178
v_max3_u32 v240, v240, v179, v100
v_max3_u32 v240, v240, v101, v102
v_max3_u32 v240, v240, v103, v104
v_max3_u32 v240, v240, v105, v106
v_max3_u32 v240, v240, v107, v108
v_max3_u32 v240, v240, v109, v110
v_max3_u32 v240, v240, v111, v112
v_max3_u32 v240, v240, v113, v114
v_max3_u32 v240, v240, v115, v116
v_max3_u32 v240, v240, v117, v118
v_max3_u32 v240, v240, v119, v120
v_max3_u32 v240, v240, v121, v122
v_max3_u32 v240, v240, v123, v124
v_max3_u32 v240, v240, v125, v126
v_max3_u32 v240, v240, v127, v128
v_max3_u32 v240, v240, v129, v130
v_max_u32 v240, v240, v131
v_cmp_eq_u32 vcc, -1, v240
s_cbranch_vccnz REDOS1
s_branch REDOS1D
REDOS1G:
s_mov_b32 s9, 0
REDOS1D:
v_mfma_f32_32x32x16_bf16 v[20:35], a[60:63], v[156:159], v[20:35]
v_mfma_f32_32x32x16_bf16 v[4:19], a[56:59], v[156:159], v[4:19]
v_mfma_f32_32x32x16_bf16 v[84:99], a[60:63], v[160:163], v[84:99]
v_mfma_f32_32x32x16_bf16 v[68:83], a[56:59], v[160:163], v[68:83]
v_mfma_f32_32x32x16_bf16 v[20:35], a[68:71], v[164:167], v[20:35]
v_mfma_f32_32x32x16_bf16 v[4:19], a[64:67], v[164:167], v[4:19]
v_mfma_f32_32x32x16_bf16 v[84:99], a[68:71], v[168:171], v[84:99]
v_mfma_f32_32x32x16_bf16 v[68:83], a[64:67], v[168:171], v[68:83]
v_mfma_f32_32x32x16_bf16 v[20:35], a[76:79], v[172:175], v[20:35]
v_mfma_f32_32x32x16_bf16 v[4:19], a[72:75], v[172:175], v[4:19]
v_mfma_f32_32x32x16_bf16 v[84:99], a[76:79], v[176:179], v[84:99]
v_mfma_f32_32x32x16_bf16 v[68:83], a[72:75], v[176:179], v[68:83]
v_mfma_f32_32x32x16_bf16 v[20:35], a[84:87], v[100:103], v[20:35]
v_mfma_f32_32x32x16_bf16 v[4:19], a[80:83], v[100:103], v[4:19]
v_mfma_f32_32x32x16_bf16 v[84:99], a[84:87], v[104:107], v[84:99]
v_mfma_f32_32x32x16_bf16 v[68:83], a[80:83], v[104:107], v[68:83]
v_mfma_f32_32x32x16_bf16 v[20:35], a[92:95], v[108:111], v[20:35]
v_mfma_f32_32x32x16_bf16 v[4:19], a[88:91], v[108:111], v[4:19]
v_mfma_f32_32x32x16_bf16 v[84:99], a[92:95], v[112:115], v[84:99]
v_mfma_f32_32x32x16_bf16 v[68:83], a[88:91], v[112:115], v[68:83]
v_mfma_f32_32x32x16_bf16 v[20:35], a[100:103], v[116:119], v[20:35]
v_mfma_f32_32x32x16_bf16 v[4:19], a[96:99], v[116:119], v[4:19]
v_mfma_f32_32x32x16_bf16 v[84:99], a[100:103], v[120:123], v[84:99]
v_mfma_f32_32x32x16_bf16 v[68:83], a[96:99], v[120:123], v[68:83]
v_mfma_f32_32x32x16_bf16 v[20:35], a[108:111], v[124:127], v[20:35]
v_mfma_f32_32x32x16_bf16 v[4:19], a[104:107], v[124:127], v[4:19]
v_mfma_f32_32x32x16_bf16 v[84:99], a[108:111], v[128:131], v[84:99]
v_mfma_f32_32x32x16_bf16 v[68:83], a[104:107], v[128:131], v[68:83]
s_nop 7
s_nop 7
REDOS2:
s_nop 3
s_add_u32 s4, s5, 0xe000
buffer_load_dwordx4 v[132:135], v1, s[44:47], s4 offen sc1
s_add_u32 s4, s6, 0xe000
buffer_load_dwordx4 v[136:139], v1, s[44:47], s4 offen sc1
s_add_u32 s4, s5, 0xe400
buffer_load_dwordx4 v[140:143], v1, s[44:47], s4 offen sc1
s_add_u32 s4, s6, 0xe400
buffer_load_dwordx4 v[144:147], v1, s[44:47], s4 offen sc1
s_add_u32 s4, s5, 0xe800
buffer_load_dwordx4 v[148:151], v1, s[44:47], s4 offen sc1
s_add_u32 s4, s6, 0xe800
buffer_load_dwordx4 v[152:155], v1, s[44:47], s4 offen sc1
s_add_u32 s4, s5, 0xec00
buffer_load_dwordx4 v[156:159], v1, s[44:47], s4 offen sc1
s_add_u32 s4, s6, 0xec00
buffer_load_dwordx4 v[160:163], v1, s[44:47], s4 offen sc1
s_add_u32 s4, s5, 0xf000
buffer_load_dwordx4 v[164:167], v1, s[44:47], s4 offen sc1
s_add_u32 s4, s6, 0xf000
buffer_load_dwordx4 v[168:171], v1, s[44:47], s4 offen sc1
s_add_u32 s4, s5, 0xf400
buffer_load_dwordx4 v[172:175], v1, s[44:47], s4 offen sc1
s_add_u32 s4, s6, 0xf400
buffer_load_dwordx4 v[176:179], v1, s[44:47], s4 offen sc1
s_add_u32 s4, s5, 0xf800
buffer_load_dwordx4 v[100:103], v1, s[44:47], s4 offen sc1
s_add_u32 s4, s6, 0xf800
buffer_load_dwordx4 v[104:107], v1, s[44:47], s4 offen sc1
s_waitcnt vmcnt(0)
s_sub_u32 s9, s9, 1
s_cbranch_scc1 REDOS2G
v_max3_u32 v240, v132, v133, v134
v_max3_u32 v240, v240, v135, v136
v_max3_u32 v240, v240, v137, v138
v_max3_u32 v240, v240, v139, v140
v_max3_u32 v240, v240, v141, v142
v_max3_u32 v240, v240, v143, v144
v_max3_u32 v240, v240, v145, v146
v_max3_u32 v240, v240, v147, v148
v_max3_u32 v240, v240, v149, v150
v_max3_u32 v240, v240, v151, v152
v_max3_u32 v240, v240, v153, v154
v_max3_u32 v240, v240, v155, v156
v_max3_u32 v240, v240, v157, v158
v_max3_u32 v240, v240, v159, v160
v_max3_u32 v240, v240, v161, v162
v_max3_u32 v240, v240, v163, v164
v_max3_u32 v240, v240, v165, v166
v_max3_u32 v240, v240, v167, v168
v_max3_u32 v240, v240, v169, v170
v_max3_u32 v240, v240, v171, v172
v_max3_u32 v240, v240, v173, v174
v_max3_u32 v240, v240, v175, v176
v_max3_u32 v240, v240, v177, v178
v_max3_u32 v240, v240, v179, v100
v_max3_u32 v240, v240, v101, v102
v_max3_u32 v240, v240, v103, v104
v_max3_u32 v240, v240, v105, v106
v_max_u32 v240, v240, v107
v_cmp_eq_u32 vcc, -1, v240
s_cbranch_vccnz REDOS2
s_branch REDOS2D
REDOS2G:
s_mov_b32 s9, 0
REDOS2D:
v_mfma_f32_32x32x16_bf16 v[20:35], a[116:119], v[132:135], v[20:35]
v_mfma_f32_32x32x16_bf16 v[4:19], a[112:115], v[132:135], v[4:19]
v_mfma_f32_32x32x16_bf16 v[84:99], a[116:119], v[136:139], v[84:99]
v_mfma_f32_32x32x16_bf16 v[68:83], a[112:115], v[136:139], v[68:83]
v_mfma_f32_32x32x16_bf16 v[20:35], a[124:127], v[140:143], v[20:35]
v_mfma_f32_32x32x16_bf16 v[4:19], a[120:123], v[140:143], v[4:19]
v_mfma_f32_32x32x16_bf16 v[84:99], a[124:127], v[144:147], v[84:99]
v_mfma_f32_32x32x16_bf16 v[68:83], a[120:123], v[144:147], v[68:83]
v_mfma_f32_32x32x16_bf16 v[20:35], a[132:135], v[148:151], v[20:35]
v_mfma_f32_32x32x16_bf16 v[4:19], a[128:131], v[148:151], v[4:19]
v_mfma_f32_32x32x16_bf16 v[84:99], a[132:135], v[152:155], v[84:99]
v_mfma_f32_32x32x16_bf16 v[68:83], a[128:131], v[152:155], v[68:83]
v_mfma_f32_32x32x16_bf16 v[20:35], a[140:143], v[156:159], v[20:35]
v_mfma_f32_32x32x16_bf16 v[4:19], a[136:139], v[156:159], v[4:19]
v_mfma_f32_32x32x16_bf16 v[84:99], a[140:143], v[160:163], v[84:99]
v_mfma_f32_32x32x16_bf16 v[68:83], a[136:139], v[160:163], v[68:83]
v_mfma_f32_32x32x16_bf16 v[20:35], a[148:151], v[164:167], v[20:35]
v_mfma_f32_32x32x16_bf16 v[4:19], a[144:147], v[164:167], v[4:19]
v_mfma_f32_32x32x16_bf16 v[84:99], a[148:151], v[168:171], v[84:99]
v_mfma_f32_32x32x16_bf16 v[68:83], a[144:147], v[168:171], v[68:83]
v_mfma_f32_32x32x16_bf16 v[20:35], a[156:159], v[172:175], v[20:35]
v_mfma_f32_32x32x16_bf16 v[4:19], a[152:155], v[172:175], v[4:19]
v_mfma_f32_32x32x16_bf16 v[84:99], a[156:159], v[176:179], v[84:99]
v_mfma_f32_32x32x16_bf16 v[68:83], a[152:155], v[176:179], v[68:83]
v_mfma_f32_32x32x16_bf16 v[20:35], a[164:167], v[100:103], v[20:35]
v_mfma_f32_32x32x16_bf16 v[4:19], a[160:163], v[100:103], v[4:19]
v_mfma_f32_32x32x16_bf16 v[84:99], a[164:167], v[104:107], v[84:99]
v_mfma_f32_32x32x16_bf16 v[68:83], a[160:163], v[104:107], v[68:83]
s_nop 7
s_nop 7
REDOS3:
s_nop 3
s_add_u32 s4, s6, 0x15000
buffer_load_dwordx4 v[108:111], v1, s[44:47], s4 offen sc1
s_add_u32 s4, s6, 0x15400
buffer_load_dwordx4 v[112:115], v1, s[44:47], s4 offen sc1
s_add_u32 s4, s6, 0x15800
buffer_load_dwordx4 v[116:119], v1, s[44:47], s4 offen sc1
s_add_u32 s4, s6, 0x15c00
buffer_load_dwordx4 v[120:123], v1, s[44:47], s4 offen sc1
s_add_u32 s4, s6, 0x16000
buffer_load_dwordx4 v[124:127], v1, s[44:47], s4 offen sc1
s_add_u32 s4, s6, 0x16400
buffer_load_dwordx4 v[128:131], v1, s[44:47], s4 offen sc1
s_add_u32 s4, s6, 0x16800
buffer_load_dwordx4 v[132:135], v1, s[44:47], s4 offen sc1
s_add_u32 s4, s5, 0x15000
buffer_load_dwordx4 v[136:139], v1, s[44:47], s4 offen sc1
s_add_u32 s4, s5, 0x15400
buffer_load_dwordx4 v[140:143], v1, s[44:47], s4 offen sc1
s_add_u32 s4, s5, 0x15800
buffer_load_dwordx4 v[144:147], v1, s[44:47], s4 offen sc1
s_add_u32 s4, s5, 0x15c00
buffer_load_dwordx4 v[148:151], v1, s[44:47], s4 offen sc1
s_add_u32 s4, s5, 0x16000
buffer_load_dwordx4 v[152:155], v1, s[44:47], s4 offen sc1
s_add_u32 s4, s5, 0x16400
buffer_load_dwordx4 v[156:159], v1, s[44:47], s4 offen sc1
s_add_u32 s4, s5, 0x16800
buffer_load_dwordx4 v[160:163], v1, s[44:47], s4 offen sc1
s_waitcnt vmcnt(0)
s_sub_u32 s9, s9, 1
s_cbranch_scc1 REDOS3G
v_max3_u32 v240, v108, v109, v110
v_max3_u32 v240, v240, v111, v112
v_max3_u32 v240, v240, v113, v114
v_max3_u32 v240, v240, v115, v116
v_max3_u32 v240, v240, v117, v118
v_max3_u32 v240, v240, v119, v120
v_max3_u32 v240, v240, v121, v122
v_max3_u32 v240, v240, v123, v124
v_max3_u32 v240, v240, v125, v126
v_max3_u32 v240, v240, v127, v128
v_max3_u32 v240, v240, v129, v130
v_max3_u32 v240, v240, v131, v132
v_max3_u32 v240, v240, v133, v134
v_max3_u32 v240, v240, v135, v136
)ASM"
R"ASM(
v_max3_u32 v240, v240, v137, v138
v_max3_u32 v240, v240, v139, v140
v_max3_u32 v240, v240, v141, v142
v_max3_u32 v240, v240, v143, v144
v_max3_u32 v240, v240, v145, v146
v_max3_u32 v240, v240, v147, v148
v_max3_u32 v240, v240, v149, v150
v_max3_u32 v240, v240, v151, v152
v_max3_u32 v240, v240, v153, v154
v_max3_u32 v240, v240, v155, v156
v_max3_u32 v240, v240, v157, v158
v_max3_u32 v240, v240, v159, v160
v_max3_u32 v240, v240, v161, v162
v_max_u32 v240, v240, v163
v_cmp_eq_u32 vcc, -1, v240
s_cbranch_vccnz REDOS3
s_branch REDOS3D
REDOS3G:
s_mov_b32 s9, 0
REDOS3D:
v_mfma_f32_32x32x16_bf16 v[84:99], a[172:175], v[108:111], v[84:99]
v_mfma_f32_32x32x16_bf16 v[68:83], a[168:171], v[108:111], v[68:83]
v_mfma_f32_32x32x16_bf16 v[84:99], a[180:183], v[112:115], v[84:99]
v_mfma_f32_32x32x16_bf16 v[68:83], a[176:179], v[112:115], v[68:83]
v_mfma_f32_32x32x16_bf16 v[84:99], a[188:191], v[116:119], v[84:99]
v_mfma_f32_32x32x16_bf16 v[68:83], a[184:187], v[116:119], v[68:83]
v_mfma_f32_32x32x16_bf16 v[84:99], a[196:199], v[120:123], v[84:99]
v_mfma_f32_32x32x16_bf16 v[68:83], a[192:195], v[120:123], v[68:83]
v_mfma_f32_32x32x16_bf16 v[84:99], a[204:207], v[124:127], v[84:99]
v_mfma_f32_32x32x16_bf16 v[68:83], a[200:203], v[124:127], v[68:83]
v_mfma_f32_32x32x16_bf16 v[84:99], a[212:215], v[128:131], v[84:99]
v_mfma_f32_32x32x16_bf16 v[68:83], a[208:211], v[128:131], v[68:83]
v_mfma_f32_32x32x16_bf16 v[84:99], a[220:223], v[132:135], v[84:99]
v_mfma_f32_32x32x16_bf16 v[68:83], a[216:219], v[132:135], v[68:83]
v_mfma_f32_32x32x16_bf16 v[20:35], a[172:175], v[136:139], v[20:35]
v_mfma_f32_32x32x16_bf16 v[4:19], a[168:171], v[136:139], v[4:19]
v_mfma_f32_32x32x16_bf16 v[20:35], a[180:183], v[140:143], v[20:35]
v_mfma_f32_32x32x16_bf16 v[4:19], a[176:179], v[140:143], v[4:19]
v_mfma_f32_32x32x16_bf16 v[20:35], a[188:191], v[144:147], v[20:35]
v_mfma_f32_32x32x16_bf16 v[4:19], a[184:187], v[144:147], v[4:19]
v_mfma_f32_32x32x16_bf16 v[20:35], a[196:199], v[148:151], v[20:35]
v_mfma_f32_32x32x16_bf16 v[4:19], a[192:195], v[148:151], v[4:19]
v_mfma_f32_32x32x16_bf16 v[20:35], a[204:207], v[152:155], v[20:35]
v_mfma_f32_32x32x16_bf16 v[4:19], a[200:203], v[152:155], v[4:19]
v_mfma_f32_32x32x16_bf16 v[20:35], a[212:215], v[156:159], v[20:35]
v_mfma_f32_32x32x16_bf16 v[4:19], a[208:211], v[156:159], v[4:19]
v_mfma_f32_32x32x16_bf16 v[20:35], a[220:223], v[160:163], v[20:35]
v_mfma_f32_32x32x16_bf16 v[4:19], a[216:219], v[160:163], v[4:19]
s_nop 7
s_nop 7
ds_write_b128 v239, v[20:23]
ds_write_b128 v239, v[24:27] offset:1024
ds_write_b128 v239, v[28:31] offset:2048
ds_write_b128 v239, v[32:35] offset:3072
ds_write_b128 v238, v[68:71] offset:4096
ds_write_b128 v238, v[72:75] offset:5120
ds_write_b128 v238, v[76:79] offset:6144
ds_write_b128 v238, v[80:83] offset:7168
ds_write_b128 v236, v[84:87] offset:8192
ds_write_b128 v236, v[88:91] offset:9216
ds_write_b128 v236, v[92:95] offset:10240
ds_write_b128 v236, v[96:99] offset:11264
s_branch REDOB
)ASM"
  :: "s"(__builtin_amdgcn_kernarg_segment_ptr()), "s"(__builtin_amdgcn_workgroup_id_x()), "v"(__builtin_amdgcn_workitem_id_x())
  : "v1", "v2", "v3", "v4", "v5", "v6", "v7", "v8", "v9", "v10", "v11", "v12", "v13", "v14", "v15", "v16", "v17", "v18", "v19", "v20", "v21", "v22", "v23", "v24", "v25", "v26", "v27", "v28", "v29", "v30", "v31", "v32", "v33", "v34", "v35", "v36", "v37", "v38", "v39", "v40", "v41", "v42", "v43", "v44", "v45", "v46", "v47", "v48", "v49", "v50", "v51", "v52", "v53", "v54", "v55", "v56", "v57", "v58", "v59", "v60", "v61", "v62", "v63", "v64", "v65", "v66", "v67", "v68", "v69", "v70", "v71", "v72", "v73", "v74", "v75", "v76", "v77", "v78", "v79", "v80", "v81", "v82", "v83", "v84", "v85", "v86", "v87", "v88", "v89", "v90", "v91", "v92", "v93", "v94", "v95", "v96", "v97", "v98", "v99", "v100", "v101", "v102", "v103", "v104", "v105", "v106", "v107", "v108", "v109", "v110", "v111", "v112", "v113", "v114", "v115", "v116", "v117", "v118", "v119", "v120", "v121", "v122", "v123", "v124", "v125", "v126", "v127", "v128", "v129", "v130", "v131", "v132", "v133", "v134", "v135", "v136", "v137", "v138", "v139", "v140", "v141", "v142", "v143", "v144", "v145", "v146", "v147", "v148", "v149", "v150", "v151", "v152", "v153", "v154", "v155", "v156", "v157", "v158", "v159", "v160", "v161", "v162", "v163", "v164", "v165", "v166", "v167", "v168", "v169", "v170", "v171", "v172", "v173", "v174", "v175", "v176", "v177", "v178", "v179", "v180", "v181", "v182", "v183", "v184", "v185", "v186", "v187", "v188", "v189", "v190", "v191", "v192", "v193", "v194", "v195", "v196", "v197", "v198", "v199", "v200", "v201", "v202", "v203", "v204", "v205", "v206", "v207", "v208", "v209", "v210", "v211", "v212", "v213", "v214", "v215", "v216", "v217", "v218", "v219", "v220", "v221", "v222", "v223", "v224", "v225", "v226", "v227", "v228", "v229", "v230", "v231", "v232", "v233", "v234", "v235", "v236", "v237", "v238", "v239", "v240", "v241", "v242", "v243", "v244", "v245", "v246", "v247", "v248", "v249", "v250", "v251", "v252", "v253", "v254", "v255", "a0", "a1", "a2", "a3", "a4", "a5", "a6", "a7", "a8", "a9", "a10", "a11", "a12", "a13", "a14", "a15", "a16", "a17", "a18", "a19", "a20", "a21", "a22", "a23", "a24", "a25", "a26", "a27", "a28", "a29", "a30", "a31", "a32", "a33", "a34", "a35", "a36", "a37", "a38", "a39", "a40", "a41", "a42", "a43", "a44", "a45", "a46", "a47", "a48", "a49", "a50", "a51", "a52", "a53", "a54", "a55", "a56", "a57", "a58", "a59", "a60", "a61", "a62", "a63", "a64", "a65", "a66", "a67", "a68", "a69", "a70", "a71", "a72", "a73", "a74", "a75", "a76", "a77", "a78", "a79", "a80", "a81", "a82", "a83", "a84", "a85", "a86", "a87", "a88", "a89", "a90", "a91", "a92", "a93", "a94", "a95", "a96", "a97", "a98", "a99", "a100", "a101", "a102", "a103", "a104", "a105", "a106", "a107", "a108", "a109", "a110", "a111", "a112", "a113", "a114", "a115", "a116", "a117", "a118", "a119", "a120", "a121", "a122", "a123", "a124", "a125", "a126", "a127", "a128", "a129", "a130", "a131", "a132", "a133", "a134", "a135", "a136", "a137", "a138", "a139", "a140", "a141", "a142", "a143", "a144", "a145", "a146", "a147", "a148", "a149", "a150", "a151", "a152", "a153", "a154", "a155", "a156", "a157", "a158", "a159", "a160", "a161", "a162", "a163", "a164", "a165", "a166", "a167", "a168", "a169", "a170", "a171", "a172", "a173", "a174", "a175", "a176", "a177", "a178", "a179", "a180", "a181", "a182", "a183", "a184", "a185", "a186", "a187", "a188", "a189", "a190", "a191", "a192", "a193", "a194", "a195", "a196", "a197", "a198", "a199", "a200", "a201", "a202", "a203", "a204", "a205", "a206", "a207", "a208", "a209", "a210", "a211", "a212", "a213", "a214", "a215", "a216", "a217", "a218", "a219", "a220", "a221", "a222", "a223", "a224", "a225", "a226", "a227", "a228", "a229", "a230", "a231", "a232", "a233", "a234", "a235", "a236", "a237", "a238", "a239", "a240", "a241", "a242", "a243", "a244", "a245", "a246", "a247", "a248", "a249", "a250", "a251", "a252", "a253", "a254", "a255", "s4", "s5", "s6", "s7", "s8", "s9", "s10", "s11", "s12", "s13", "s14", "s15", "s16", "s17", "s18", "s19", "s20", "s21", "s22", "s23", "s24", "s25", "s26", "s27", "s28", "s29", "s30", "s31", "s33", "s34", "s35", "s36", "s37", "s38", "s39", "s40", "s41", "s42", "s43", "s44", "s45", "s46", "s47", "s48", "s49", "s50", "s51", "s52", "s53", "s54", "s55", "s56", "s57", "s58", "s59", "s60", "s61", "s62", "s63", "s64", "s65", "s66", "s67", "s68", "s69", "s70", "s71", "s72", "s73", "s74", "s75", "s76", "s77", "s78", "s79", "s80", "s81", "s82", "s83", "s84", "s85", "s86", "s87", "s88", "s89", "s90", "s91", "s92", "s93", "s94", "s95", "s96", "s97", "s98", "s99", "vcc", "scc", "memory");
}

extern "C" void kernel_launch(void* const* d_in, const int* in_sizes, int n_in, void* d_out, int out_size, void* d_ws, size_t ws_size, hipStream_t stream) {
  (void)hipFuncSetAttribute((const void*)fused_block, hipFuncAttributeMaxDynamicSharedMemorySize, 163840);
  fused_block<<<256, 256, 163840, stream>>>(d_in[0], d_in[1], d_in[2], d_in[3], d_in[5], d_in[6], d_out, d_ws);
}
```
